```python
import torch

def _softmax(x, axis=-1):
    return torch.softmax(x, dim=axis)
def _gelu(x):
    return torch.nn.functional.gelu(x, approximate="tanh")
def _silu(x):
    return torch.nn.functional.silu(x)
def _sigmoid(x):
    return torch.sigmoid(x)
def _layernorm(x, axis=-1, eps=1e-5):
    m = x.mean(dim=axis, keepdim=True)
    v = x.var(dim=axis, keepdim=True, unbiased=False)
    return (x - m) / torch.sqrt(v + eps)
def _scatter_add(tgt, idx, src, axis=0):
    return tgt.clone().index_add_(axis, idx, src)
def _segment_sum(src, seg, n):
    out = torch.zeros((n,) + src.shape[1:], dtype=src.dtype)
    return out.index_add_(0, seg, src)
def _pad(x, pw):
    flat = tuple(v for pair in reversed(pw) for v in pair)
    return torch.nn.functional.pad(x, flat)

kernel_name = 'gated_mix__sliding_block__swiglu_ffn__prompt'

def setup_inputs():

    _w = (lambda a: (lambda t: t.to(torch.bfloat16) if t.is_floating_point() else t)(torch.as_tensor(a)))
    def _A_setup():
        B, S, D = 1, 2048, 640
        return {
            "x": _w((lambda *s: torch.randn(*s, dtype=torch.bfloat16) * 0.05)(B, S, D)),
            "wqkv": _w((lambda *s: torch.randn(*s, dtype=torch.bfloat16) * 1.6)(D, 3 * D)),
            "wo": _w((lambda *s: torch.randn(*s, dtype=torch.bfloat16) * 0.05)(D, D)),
        }
    _A_in = _A_setup()

    def _B_setup():
        B, S, D, F = 1, 2048, 640, 1728
        return {
            "x": _w((lambda *s: torch.randn(*s, dtype=torch.bfloat16) * 0.05)(B, S, D)),
            "w_gate": _w((lambda *s: torch.randn(*s, dtype=torch.bfloat16) * 0.4)(D, F)),
            "w_up": _w((lambda *s: torch.randn(*s, dtype=torch.bfloat16) * 0.4)(D, F)),
            "w_down": _w((lambda *s: torch.randn(*s, dtype=torch.bfloat16) * 0.4)(F, D)),
        }
    _B_in = _B_setup()

    return {
        "A_x": _A_in["x"],
        "A_wqkv": _A_in["wqkv"],
        "A_wo": _A_in["wo"],
        "B_x": _B_in["x"],
        "B_w_gate": _B_in["w_gate"],
        "B_w_up": _B_in["w_up"],
        "B_w_down": _B_in["w_down"],
        "gate_w": _w((lambda *s: torch.randn(*s, dtype=torch.bfloat16) * 0.05)(640) * 16.0),
    }

def reference(A_x, A_wqkv, A_wo, B_x, B_w_gate, B_w_up, B_w_down, gate_w):
    def _A_ref(x, wqkv, wo):
        B, S, D = x.shape
        H = 32; Dh = D // H; W = 256
        qkv = (lambda x, *d: x.permute(*d))((x @ wqkv).reshape(B, S, 3, H, Dh), 2, 0, 3, 1, 4)
        q, k, v = qkv[0], qkv[1], qkv[2]
        scores = torch.einsum("bhqd,bhkd->bhqk", q, k) * (Dh ** -0.5)
        idx = torch.arange(S)
        mask = (idx[:, None] - idx[None, :] >= 0) & (idx[:, None] - idx[None, :] < W)
        scores = torch.where(mask[None, None], scores, -1e9)
        p = _softmax(scores, axis=-1)
        o = torch.einsum("bhqk,bhkd->bhqd", p, v)
        return (lambda x, *d: x.permute(*d))(o, 0, 2, 1, 3).reshape(B, S, D) @ wo

    def _B_ref(x, w_gate, w_up, w_down):
        g = _silu(x @ w_gate)
        u = x @ w_up
        return (g * u) @ w_down

    x = A_x
    g = _sigmoid((x * gate_w).sum(-1, keepdims=True))
    a = _A_ref(x, A_wqkv, A_wo)
    b = _B_ref(x, B_w_gate, B_w_up, B_w_down)
    return g * a + (1 - g) * b
```

```cpp
#include <hip/hip_runtime.h>
#include <hip/hip_bf16.h>
#include <stdint.h>

typedef unsigned short u16;
typedef short bf16x8 __attribute__((ext_vector_type(8)));
typedef short s16x4 __attribute__((ext_vector_type(4)));
typedef float f32x4 __attribute__((ext_vector_type(4)));
typedef unsigned u32x4 __attribute__((ext_vector_type(4)));
typedef unsigned u32x2 __attribute__((ext_vector_type(2)));
typedef __bf16 bf2_t __attribute__((ext_vector_type(2)));

#define LDS_AS __attribute__((address_space(3)))

constexpr int S_ = 2048, D_ = 640, F_ = 1728, H_ = 32, DH_ = 20, NQKV = 1920;
constexpr int KS_O = D_ / 32;
constexpr int KS_H = F_ / 32;

constexpr size_t WS_Q = 0;
constexpr size_t WS_K = WS_Q + (size_t)H_ * S_ * 32 * 2;
constexpr size_t WS_V = WS_K + (size_t)H_ * S_ * 32 * 2;
constexpr size_t WS_H = WS_V + (size_t)H_ * S_ * 32 * 2;
constexpr size_t WS_O = WS_H + (size_t)S_ * F_ * 2;
constexpr size_t WS_WO = WS_O + (size_t)S_ * D_ * 2;
constexpr size_t WS_WD = WS_WO + (size_t)D_ * D_ * 2;
constexpr size_t WS_G = WS_WD + (size_t)F_ * D_ * 2;

__device__ __forceinline__ unsigned pack_bf16(float a, float b) {
    bf2_t v = {(__bf16)a, (__bf16)b};
    return __builtin_bit_cast(unsigned, v);
}
__device__ __forceinline__ float rbf(float x) { return (float)(__bf16)x; }
__device__ __forceinline__ float bf2f(u16 v) { return __uint_as_float(((unsigned)v) << 16); }

__device__ __forceinline__ s16x4 tr_read(const char* lds_ptr) {
    return __builtin_amdgcn_ds_read_tr16_b64_v4i16((LDS_AS s16x4*)(lds_ptr));
}
__device__ __forceinline__ bf16x8 cat4(s16x4 a, s16x4 b) {
    bf16x8 r;
    r[0] = a[0]; r[1] = a[1]; r[2] = a[2]; r[3] = a[3];
    r[4] = b[0]; r[5] = b[1]; r[6] = b[2]; r[7] = b[3];
    return r;
}
__device__ __forceinline__ void glds16(const void* g, void* l) {
    __builtin_amdgcn_global_load_lds((const unsigned*)g, (LDS_AS unsigned*)l, 16, 0, 0);
}

constexpr int K1_ABYTES = 256 * 64 * 2;
constexpr int K1_BBYTES = 64 * 192 * 2;
constexpr int K1_STAGE = K1_ABYTES + K1_BBYTES;

__global__ __launch_bounds__(512, 2) void k1_gemm(const u16* __restrict__ X, const u16* __restrict__ Wqkv,
                                                  const u16* __restrict__ Wg, const u16* __restrict__ Wu,
                                                  const u16* __restrict__ Wo, const u16* __restrict__ Wd,
                                                  const u16* __restrict__ gate_w, char* __restrict__ ws) {
    __shared__ __attribute__((aligned(16))) char smem[2 * K1_STAGE];
    const int bid = blockIdx.x;
    const int xcd = bid & 7, idx = bid >> 3;
    const int t = threadIdx.x;
    const int lane = t & 63, wid = t >> 6;

    if (idx >= 28) {
        const int ab = xcd * 4 + (idx - 28);
        const int gw = ab * 8 + wid;
        float* gout = (float*)(ws + WS_G);
        {
            u32x4 w0 = *(const u32x4*)(gate_w + lane * 8);
            u32x4 w1 = {0, 0, 0, 0};
            if (lane < 16) w1 = *(const u32x4*)(gate_w + 512 + lane * 8);
            for (int r = 0; r < 8; ++r) {
                const int row = gw * 8 + r;
                u32x4 x0 = *(const u32x4*)(X + (size_t)row * D_ + lane * 8);
                u32x4 x1 = {0, 0, 0, 0};
                if (lane < 16) x1 = *(const u32x4*)(X + (size_t)row * D_ + 512 + lane * 8);
                float s = 0.f;
#pragma unroll
                for (int e = 0; e < 4; ++e) {
                    s += rbf(__uint_as_float(x0[e] << 16) * __uint_as_float(w0[e] << 16));
                    s += rbf(__uint_as_float(x0[e] & 0xffff0000u) * __uint_as_float(w0[e] & 0xffff0000u));
                    s += rbf(__uint_as_float(x1[e] << 16) * __uint_as_float(w1[e] << 16));
                    s += rbf(__uint_as_float(x1[e] & 0xffff0000u) * __uint_as_float(w1[e] & 0xffff0000u));
                }
#pragma unroll
                for (int o = 32; o >= 1; o >>= 1) s += __shfl_xor(s, o);
                if (lane == 0) {
                    float z = rbf(s);
                    float gg = rbf(1.f / (1.f + __expf(-z)));
                    gout[row] = gg;
                }
            }
        }
        {
            u16* woF = (u16*)(ws + WS_WO);
            u16* wdF = (u16*)(ws + WS_WD);
            const int nblk = 40 * (KS_O + KS_H);
            for (int b = gw; b < nblk; b += 256) {
                const int nt = b % 40, ksg = b / 40;
                const u16* src; u16* dst;
                if (ksg < KS_O) { src = Wo + (size_t)(ksg * 32) * D_; dst = woF + ((size_t)(nt * KS_O + ksg) * 64 + lane) * 8; }
                else { const int ks = ksg - KS_O; src = Wd + (size_t)(ks * 32) * D_; dst = wdF + ((size_t)(nt * KS_H + ks) * 64 + lane) * 8; }
                src += (size_t)((lane >> 4) * 8) * D_ + nt * 16 + (lane & 15);
                unsigned v[4];
#pragma unroll
                for (int j = 0; j < 4; ++j) {
                    unsigned lo = src[(size_t)(2 * j) * D_];
                    unsigned hi = src[(size_t)(2 * j + 1) * D_];
                    v[j] = lo | (hi << 16);
                }
                u32x4 o = {v[0], v[1], v[2], v[3]};
                *(u32x4*)dst = o;
            }
        }
        return;
    }

    const int mt = (xcd & 1) * 4 + (idx & 3);
    const int nt = (xcd >> 1) * 7 + (idx >> 2);
    const int m0 = mt * 256;
    const bool is_qkv = nt < 10;
    const int f0 = (nt - 10) * 96;

    const u16* asrc[4];
#pragma unroll
    for (int i = 0; i < 4; ++i) {
        const int row = i * 64 + (t >> 3);
        const int c = (t & 7) ^ (row & 7);
        asrc[i] = X + (size_t)(m0 + row) * D_ + c * 8;
    }
    const u16* bsrc[3];
    const int ldb = is_qkv ? NQKV : F_;
#pragma unroll
    for (int i = 0; i < 3; ++i) {
        const int p = i * 512 + t;
        const int st = p >> 4;
        const int kb = st / 12, nb = st % 12;
        const int rr = (p >> 1) & 7, half = p & 1;
        const int krow = kb * 8 + (rr ^ ((kb & 1) << 2));
        if (is_qkv) {
            bsrc[i] = Wqkv + (size_t)krow * NQKV + nt * 192 + nb * 16 + half * 8;
        } else {
            const int wc = nb / 6, j = nb % 6;
            const u16* mat = (j < 3) ? Wg : Wu;
            bsrc[i] = mat + (size_t)krow * F_ + f0 + 48 * wc + 16 * (j % 3) + half * 8;
        }
    }

    const int wr = wid >> 1, wc = wid & 1;
    const int fr = lane & 15, g = lane >> 4;
    f32x4 acc[4][6];
#pragma unroll
    for (int i = 0; i < 4; ++i)
#pragma unroll
        for (int j = 0; j < 6; ++j) acc[i][j] = (f32x4){0.f, 0.f, 0.f, 0.f};

    int aoff[4][2];
#pragma unroll
    for (int i = 0; i < 4; ++i)
#pragma unroll
        for (int ks = 0; ks < 2; ++ks) {
            const int row = wr * 64 + i * 16 + fr;
            aoff[i][ks] = row * 128 + (((ks * 4 + g) ^ (row & 7)) << 4);
        }
    const int q4 = (lane & 15) >> 2, p4 = lane & 3;
    const int bbase0 = K1_ABYTES + (g * 12 + wc * 6) * 256 + ((q4 ^ ((g & 1) << 2)) * 32) + p4 * 8;
    const int bbase1 = K1_ABYTES + (g * 12 + wc * 6) * 256 + (((4 + q4) ^ ((g & 1) << 2)) * 32) + p4 * 8;

    auto stage = [&](int kt, int buf) {
        char* base = smem + buf * K1_STAGE;
#pragma unroll
        for (int i = 0; i < 4; ++i) glds16(asrc[i] + kt * 64, base + (i * 512 + t) * 16);
#pragma unroll
        for (int i = 0; i < 3; ++i) glds16(bsrc[i] + (size_t)kt * 64 * ldb, base + K1_ABYTES + (i * 512 + t) * 16);
    };

    stage(0, 0);
    for (int kt = 0; kt < 10; ++kt) {
        __syncthreads();
        if (kt + 1 < 10) stage(kt + 1, (kt + 1) & 1);
        const char* base = smem + (kt & 1) * K1_STAGE;
#pragma unroll
        for (int ks = 0; ks < 2; ++ks) {
            bf16x8 af[4], bfr[6];
#pragma unroll
            for (int i = 0; i < 4; ++i) af[i] = *(const bf16x8*)(base + aoff[i][ks]);
#pragma unroll
            for (int j = 0; j < 6; ++j) {
                s16x4 lo = tr_read(base + bbase0 + ks * (4 * 12 * 256) + j * 256);
                s16x4 hi = tr_read(base + bbase1 + ks * (4 * 12 * 256) + j * 256);
                bfr[j] = cat4(lo, hi);
            }
#pragma unroll
            for (int i = 0; i < 4; ++i)
#pragma unroll
                for (int j = 0; j < 6; ++j)
                    acc[i][j] = __builtin_amdgcn_mfma_f32_16x16x32_bf16(bfr[j], af[i], acc[i][j], 0, 0, 0);
        }
    }

    if (is_qkv) {
#pragma unroll
        for (int j = 0; j < 6; ++j) {
            const int n = nt * 192 + wc * 96 + j * 16 + g * 4;
            const int which = n / D_;
            const int within = n - which * D_;
            const int h = within / DH_;
            const int d0 = within - h * DH_;
            u16* dst0 = (u16*)(ws + WS_Q + (size_t)which * ((size_t)H_ * S_ * 32 * 2)) + (size_t)h * S_ * 32 + d0;
            const unsigned padw = (which == 2) ? 0x00003f80u : 0u;
#pragma unroll
            for (int i = 0; i < 4; ++i) {
                const int m = m0 + wr * 64 + i * 16 + fr;
                u16* dst = dst0 + (size_t)m * 32;
                const unsigned lo = pack_bf16(acc[i][j][0], acc[i][j][1]);
                const unsigned hi = pack_bf16(acc[i][j][2], acc[i][j][3]);
                if (d0 == 16) {
                    u32x4 a = {lo, hi, padw, 0u};
                    u32x4 z = {0u, 0u, 0u, 0u};
                    *(u32x4*)dst = a;
                    *(u32x4*)(dst + 8) = z;
                } else {
                    u32x2 a = {lo, hi};
                    *(u32x2*)dst = a;
                }
            }
        }
    } else {
        u16* hF = (u16*)(ws + WS_H);
#pragma unroll
        for (int j = 0; j < 3; ++j) {
            const int f = f0 + wc * 48 + j * 16 + g * 4;
            const int ks = f >> 5;
            const int lq = (f & 31) >> 3;
            const int hf = f & 4;
#pragma unroll
            for (int i = 0; i < 4; ++i) {
                const int m = m0 + wr * 64 + i * 16 + fr;
                float hv[4];
#pragma unroll
                for (int r = 0; r < 4; ++r) {
                    const float gt = rbf(acc[i][j][r]);
                    const float up = rbf(acc[i][j + 3][r]);
                    const float sl = rbf(gt / (1.f + __expf(-gt)));
                    hv[r] = sl * up;
                }
                u32x2 a = {pack_bf16(hv[0], hv[1]), pack_bf16(hv[2], hv[3])};
                u16* dst = hF + ((size_t)((m >> 4) * KS_H + ks) * 64 + (m & 15) + 16 * lq) * 8 + hf;
                *(u32x2*)dst = a;
            }
        }
    }
}

__global__ __launch_bounds__(256, 2) void k2_attn(char* __restrict__ ws) {
    __shared__ __attribute__((aligned(16))) char smem[2 * 384 * 64];
    const int bid = blockIdx.x;
    const int h = (bid & 7) + 8 * ((bid >> 3) & 3);
    const int qb = bid >> 5;
    const int q0 = qb * 128;
    const int t = threadIdx.x, lane = t & 63, w = t >> 6;
    const u16* Qb = (const u16*)(ws + WS_Q) + (size_t)h * S_ * 32;
    const u16* Kb = (const u16*)(ws + WS_K) + (size_t)h * S_ * 32;
    const u16* Vb = (const u16*)(ws + WS_V) + (size_t)h * S_ * 32;
    char* sK = smem;
    char* sV = smem + 384 * 64;

#pragma unroll
    for (int i = 0; i < 6; ++i) {
        const int p = i * 256 + t;
        const int row = p >> 2, cp = p & 3;
        int key = q0 - 256 + row;
        key = key < 0 ? 0 : key;
        const int ck = cp ^ ((-(row >> 2)) & 3);
        const int cv = cp ^ (((row >> 2) & 1) << 1);
        glds16(Kb + (size_t)key * 32 + ck * 8, sK + p * 16);
        glds16(Vb + (size_t)key * 32 + cv * 8, sV + p * 16);
    }
    const int fr = lane & 15, g = lane >> 4;
    const int qw = q0 + 32 * w;
    bf16x8 qf[2];
#pragma unroll
    for (int qt = 0; qt < 2; ++qt) qf[qt] = *(const bf16x8*)(Qb + (size_t)(qw + qt * 16 + fr) * 32 + g * 8);
    __syncthreads();

    const int bstart = (qw >= 256) ? 0 : (8 - (qw >> 5));
    const float NEG = -__builtin_inff();
    f32x4 sc[9][2][2];
    float mx[2] = {NEG, NEG};
#pragma unroll
    for (int b = 0; b < 9; ++b) {
        if (b >= bstart) {
#pragma unroll
            for (int kt = 0; kt < 2; ++kt) {
                const int row = 32 * (w + b) + 16 * kt + fr;
                const bf16x8 kf = *(const bf16x8*)(sK + row * 64 + ((g ^ ((-(row >> 2)) & 3)) << 4));
#pragma unroll
                for (int qt = 0; qt < 2; ++qt) {
                    f32x4 z = {0.f, 0.f, 0.f, 0.f};
                    f32x4 s = __builtin_amdgcn_mfma_f32_16x16x32_bf16(kf, qf[qt], z, 0, 0, 0);
                    if (b == 0) {
#pragma unroll
                        for (int r = 0; r < 4; ++r)
                            if (16 * qt + fr >= 16 * kt + 4 * g + r) s[r] = NEG;
                    }
                    if (b == 8) {
#pragma unroll
                        for (int r = 0; r < 4; ++r)
                            if (16 * kt + 4 * g + r > 16 * qt + fr) s[r] = NEG;
                    }
                    sc[b][kt][qt] = s;
                    mx[qt] = fmaxf(mx[qt], fmaxf(fmaxf(s[0], s[1]), fmaxf(s[2], s[3])));
                }
            }
        }
    }
    constexpr float CSC = 0.22360679774997896f * 1.4426950408889634f;
    float mc[2];
#pragma unroll
    for (int qt = 0; qt < 2; ++qt) {
        float m = mx[qt];
        m = fmaxf(m, __shfl_xor(m, 16));
        m = fmaxf(m, __shfl_xor(m, 32));
        mc[qt] = -m * CSC;
    }
    f32x4 oacc[2][2];
#pragma unroll
    for (int qt = 0; qt < 2; ++qt)
#pragma unroll
        for (int dt = 0; dt < 2; ++dt) oacc[qt][dt] = (f32x4){0.f, 0.f, 0.f, 0.f};
    const int q4 = (lane & 15) >> 2, p4 = lane & 3;
#pragma unroll
    for (int b = 0; b < 9; ++b) {
        if (b >= bstart) {
            bf16x8 vf[2];
#pragma unroll
            for (int dt = 0; dt < 2; ++dt) {
                const int r0 = 32 * (w + b) + 4 * g + q4;
                const int r1 = r0 + 16;
                const int sw = (g & 1) << 5;
                s16x4 lo = tr_read(sV + r0 * 64 + ((dt * 32) ^ sw) + p4 * 8);
                s16x4 hi = tr_read(sV + r1 * 64 + ((dt * 32) ^ sw) + p4 * 8);
                vf[dt] = cat4(lo, hi);
            }
#pragma unroll
            for (int qt = 0; qt < 2; ++qt) {
                float pv[8];
#pragma unroll
                for (int kt = 0; kt < 2; ++kt)
#pragma unroll
                    for (int r = 0; r < 4; ++r)
                        pv[kt * 4 + r] = __builtin_amdgcn_exp2f(__builtin_fmaf(sc[b][kt][qt][r], CSC, mc[qt]));
                u32x4 pk = {pack_bf16(pv[0], pv[1]), pack_bf16(pv[2], pv[3]), pack_bf16(pv[4], pv[5]), pack_bf16(pv[6], pv[7])};
                const bf16x8 pf = __builtin_bit_cast(bf16x8, pk);
#pragma unroll
                for (int dt = 0; dt < 2; ++dt)
                    oacc[qt][dt] = __builtin_amdgcn_mfma_f32_16x16x32_bf16(vf[dt], pf, oacc[qt][dt], 0, 0, 0);
            }
        }
    }
    u16* oF = (u16*)(ws + WS_O);
#pragma unroll
    for (int qt = 0; qt < 2; ++qt) {
        const float sum = __shfl(oacc[qt][1][0], fr + 16);
        const float inv = 1.f / sum;
        const int m = qw + qt * 16 + fr;
        {
            const int k0 = h * DH_ + 4 * g;
            u32x2 a = {pack_bf16(oacc[qt][0][0] * inv, oacc[qt][0][1] * inv), pack_bf16(oacc[qt][0][2] * inv, oacc[qt][0][3] * inv)};
            u16* dst = oF + ((size_t)((m >> 4) * KS_O + (k0 >> 5)) * 64 + (m & 15) + 16 * ((k0 & 31) >> 3)) * 8 + (k0 & 4);
            *(u32x2*)dst = a;
        }
        if (g == 0) {
            const int k0 = h * DH_ + 16;
            u32x2 a = {pack_bf16(oacc[qt][1][0] * inv, oacc[qt][1][1] * inv), pack_bf16(oacc[qt][1][2] * inv, oacc[qt][1][3] * inv)};
            u16* dst = oF + ((size_t)((m >> 4) * KS_O + (k0 >> 5)) * 64 + (m & 15) + 16 * ((k0 & 31) >> 3)) * 8 + (k0 & 4);
            *(u32x2*)dst = a;
        }
    }
}

constexpr int RED_LD = 84;
__global__ __launch_bounds__(256, 1) void k3_gemm(const char* __restrict__ ws, u16* __restrict__ out) {
    __shared__ __attribute__((aligned(16))) float red[4 * 64 * RED_LD];
    const int bid = blockIdx.x;
    const int xcd = bid & 7, idx = bid >> 3;
    const int mt = (xcd >> 1) * 8 + (idx & 7);
    const int nt = (xcd & 1) * 4 + (idx >> 3);
    const int t = threadIdx.x, lane = t & 63, w = t >> 6;

    const u16* A; const u16* B; int nsteps, kstot, ks0;
    if (w == 0) { A = (const u16*)(ws + WS_O); B = (const u16*)(ws + WS_WO); nsteps = KS_O; kstot = KS_O; ks0 = 0; }
    else { A = (const u16*)(ws + WS_H); B = (const u16*)(ws + WS_WD); nsteps = 18; kstot = KS_H; ks0 = 18 * (w - 1); }
    const u16* ap = A + ((size_t)(mt * 4) * kstot + ks0) * 512 + lane * 8;
    const u16* bp = B + ((size_t)(nt * 5) * kstot + ks0) * 512 + lane * 8;
    const size_t fstride = (size_t)kstot * 512;

    f32x4 acc[4][5];
#pragma unroll
    for (int i = 0; i < 4; ++i)
#pragma unroll
        for (int j = 0; j < 5; ++j) acc[i][j] = (f32x4){0.f, 0.f, 0.f, 0.f};

    bf16x8 a0[4], b0[5], a1[4], b1[5];
#pragma unroll
    for (int i = 0; i < 4; ++i) a0[i] = *(const bf16x8*)(ap + i * fstride);
#pragma unroll
    for (int j = 0; j < 5; ++j) b0[j] = *(const bf16x8*)(bp + j * fstride);
    for (int s = 0; s < nsteps; s += 2) {
#pragma unroll
        for (int i = 0; i < 4; ++i) a1[i] = *(const bf16x8*)(ap + i * fstride + (s + 1) * 512);
#pragma unroll
        for (int j = 0; j < 5; ++j) b1[j] = *(const bf16x8*)(bp + j * fstride + (s + 1) * 512);
#pragma unroll
        for (int i = 0; i < 4; ++i)
#pragma unroll
            for (int j = 0; j < 5; ++j)
                acc[i][j] = __builtin_amdgcn_mfma_f32_16x16x32_bf16(b0[j], a0[i], acc[i][j], 0, 0, 0);
        if (s + 2 < nsteps) {
#pragma unroll
            for (int i = 0; i < 4; ++i) a0[i] = *(const bf16x8*)(ap + i * fstride + (s + 2) * 512);
#pragma unroll
            for (int j = 0; j < 5; ++j) b0[j] = *(const bf16x8*)(bp + j * fstride + (s + 2) * 512);
        }
#pragma unroll
        for (int i = 0; i < 4; ++i)
#pragma unroll
            for (int j = 0; j < 5; ++j)
                acc[i][j] = __builtin_amdgcn_mfma_f32_16x16x32_bf16(b1[j], a1[i], acc[i][j], 0, 0, 0);
    }
    {
        const int fr = lane & 15, g = lane >> 4;
        float* rw = red + w * 64 * RED_LD;
#pragma unroll
        for (int i = 0; i < 4; ++i)
#pragma unroll
            for (int j = 0; j < 5; ++j)
                *(f32x4*)(rw + (i * 16 + fr) * RED_LD + j * 16 + g * 4) = acc[i][j];
    }
    __syncthreads();
    {
        const int m = t >> 2, c0 = (t & 3) * 20;
        const float gg = ((const float*)(ws + WS_G))[mt * 64 + m];
        const float omg = rbf(1.f - gg);
        u16* dst = out + (size_t)(mt * 64 + m) * D_ + nt * 80 + c0;
#pragma unroll
        for (int c = 0; c < 20; c += 4) {
            f32x4 va = *(const f32x4*)(red + (0 * 64 + m) * RED_LD + c0 + c);
            f32x4 v1 = *(const f32x4*)(red + (1 * 64 + m) * RED_LD + c0 + c);
            f32x4 v2 = *(const f32x4*)(red + (2 * 64 + m) * RED_LD + c0 + c);
            f32x4 v3 = *(const f32x4*)(red + (3 * 64 + m) * RED_LD + c0 + c);
            float o[4];
#pragma unroll
            for (int e = 0; e < 4; ++e) {
                const float a = rbf(va[e]);
                const float b = rbf(v1[e] + v2[e] + v3[e]);
                o[e] = rbf(gg * a) + rbf(omg * b);
            }
            u32x2 pk = {pack_bf16(o[0], o[1]), pack_bf16(o[2], o[3])};
            *(u32x2*)(dst + c) = pk;
        }
    }
}

extern "C" void kernel_launch(void* const* d_in, const int* in_sizes, int n_in,
                              void* d_out, int out_size, void* d_ws, size_t ws_size,
                              hipStream_t stream) {
    const u16* X = (const u16*)d_in[0];
    const u16* Wqkv = (const u16*)d_in[1];
    const u16* Wo = (const u16*)d_in[2];
    const u16* Wg = (const u16*)d_in[4];
    const u16* Wu = (const u16*)d_in[5];
    const u16* Wd = (const u16*)d_in[6];
    const u16* gw = (const u16*)d_in[7];
    char* ws = (char*)d_ws;
    k1_gemm<<<256, 512, 0, stream>>>(X, Wqkv, Wg, Wu, Wo, Wd, gw, ws);
    k2_attn<<<512, 256, 0, stream>>>(ws);
    k3_gemm<<<256, 256, 0, stream>>>(ws, (u16*)d_out);
}
```

```cpp
#include <hip/hip_runtime.h>
#include <hip/hip_bf16.h>
#include <stdint.h>

typedef unsigned short u16;
typedef short bf16x8 __attribute__((ext_vector_type(8)));
typedef short s16x4 __attribute__((ext_vector_type(4)));
typedef float f32x4 __attribute__((ext_vector_type(4)));
typedef unsigned u32x4 __attribute__((ext_vector_type(4)));
typedef unsigned u32x2 __attribute__((ext_vector_type(2)));
typedef __bf16 bf2_t __attribute__((ext_vector_type(2)));

#define LDS_AS __attribute__((address_space(3)))

constexpr int S_ = 2048, D_ = 640, F_ = 1728, H_ = 32, DH_ = 20, NQKV = 1920;
constexpr int KS_O = D_ / 32;
constexpr int KS_H = F_ / 32;

constexpr size_t WS_Q = 0;
constexpr size_t WS_K = WS_Q + (size_t)H_ * S_ * 32 * 2;
constexpr size_t WS_V = WS_K + (size_t)H_ * S_ * 32 * 2;
constexpr size_t WS_H = WS_V + (size_t)H_ * S_ * 32 * 2;
constexpr size_t WS_O = WS_H + (size_t)S_ * F_ * 2;
constexpr size_t WS_WO = WS_O + (size_t)S_ * D_ * 2;
constexpr size_t WS_WD = WS_WO + (size_t)D_ * D_ * 2;
constexpr size_t WS_G = WS_WD + (size_t)F_ * D_ * 2;

__device__ __forceinline__ unsigned pack_bf16(float a, float b) {
    bf2_t v = {(__bf16)a, (__bf16)b};
    return __builtin_bit_cast(unsigned, v);
}
__device__ __forceinline__ float rbf(float x) { return (float)(__bf16)x; }
__device__ __forceinline__ float bf2f(u16 v) { return __uint_as_float(((unsigned)v) << 16); }

__device__ __forceinline__ s16x4 tr_read(const char* lds_ptr) {
    return __builtin_amdgcn_ds_read_tr16_b64_v4i16((LDS_AS s16x4*)(lds_ptr));
}
__device__ __forceinline__ bf16x8 cat4(s16x4 a, s16x4 b) {
    bf16x8 r;
    r[0] = a[0]; r[1] = a[1]; r[2] = a[2]; r[3] = a[3];
    r[4] = b[0]; r[5] = b[1]; r[6] = b[2]; r[7] = b[3];
    return r;
}
__device__ __forceinline__ void glds16(const void* g, void* l) {
    __builtin_amdgcn_global_load_lds((const unsigned*)g, (LDS_AS unsigned*)l, 16, 0, 0);
}
__device__ __forceinline__ void glds16_asm(const void* gsrc, unsigned lds_dst) {
    unsigned keep;
    asm volatile("s_mov_b32 %0, m0\n\ts_mov_b32 m0, %2\n\ts_nop 0\n\tglobal_load_lds_dwordx4 %1, off\n\ts_mov_b32 m0, %0"
                 : "=&s"(keep) : "v"(gsrc), "s"(lds_dst) : "memory");
}
__device__ __forceinline__ void glds4_asm(const void* gsrc, unsigned lds_dst) {
    unsigned keep;
    asm volatile("s_mov_b32 %0, m0\n\ts_mov_b32 m0, %2\n\ts_nop 0\n\tglobal_load_lds_dword %1, off\n\ts_mov_b32 m0, %0"
                 : "=&s"(keep) : "v"(gsrc), "s"(lds_dst) : "memory");
}
__device__ __forceinline__ unsigned lds_addr(const void* p) { return (unsigned)(size_t)(LDS_AS const char*)p; }
#define WAIT_VM(n) asm volatile("s_waitcnt vmcnt(" #n ")" ::: "memory")
#define CBAR() asm volatile("" ::: "memory")
#define STAMP(kid, i) do {} while (0)

constexpr int K1_ABYTES = 256 * 64 * 2;
constexpr int K1_BBYTES = 64 * 192 * 2;
constexpr int K1_STAGE = K1_ABYTES + K1_BBYTES;
constexpr int K1_OLD = 328;

template <int NJ>
__device__ __forceinline__ void k1_tile(const u16* __restrict__ X, const u16* __restrict__ W0, const u16* __restrict__ W1,
                                        char* __restrict__ ws, char* smem, int mt, int nt, int xs, int xcnt) {
    constexpr int BN = 32 * NJ;
    constexpr int ROWB = BN * 2;
    constexpr int UPR = BN / 8;
    constexpr int NBU = 64 * UPR;
    constexpr int LDB = (NJ == 5) ? NQKV : F_;
    const int t = threadIdx.x;
    const int lane = t & 63, wid = t >> 6;
    const int m0 = mt * 256;
    const int c0 = (NJ == 5) ? nt * 160 : nt * 96;

    const u16* asrc[4];
#pragma unroll
    for (int i = 0; i < 4; ++i) {
        const int row = i * 64 + (t >> 3);
        const int c = (t & 7) ^ (row & 7);
        asrc[i] = X + (size_t)(m0 + row) * D_ + c * 8;
    }
    const u16* bsrc[3];
#pragma unroll
    for (int i = 0; i < 3; ++i) {
        int p = i * 512 + t;
        if (p >= NBU) p = NBU - 1;
        const int k = p / UPR, u = p % UPR;
        const int sw = (NJ == 5) ? ((k >> 3) & 1) : (((k >> 1) & 1) | (((k >> 3) & 1) << 1));
        const int c32 = (u >> 1) ^ sw, half = u & 1;
        if (NJ == 5) bsrc[i] = W0 + (size_t)k * LDB + c0 + c32 * 16 + half * 8;
        else bsrc[i] = ((c32 < 6) ? W0 : W1) + (size_t)k * LDB + c0 + (c32 < 6 ? c32 : c32 - 6) * 16 + half * 8;
    }

    const int wr = wid >> 1, wc = wid & 1;
    const int fr = lane & 15, g = lane >> 4;
    f32x4 acc[4][NJ];
#pragma unroll
    for (int i = 0; i < 4; ++i)
#pragma unroll
        for (int j = 0; j < NJ; ++j) acc[i][j] = (f32x4){0.f, 0.f, 0.f, 0.f};

    int aoff[4][2];
#pragma unroll
    for (int i = 0; i < 4; ++i)
#pragma unroll
        for (int ks = 0; ks < 2; ++ks) {
            const int row = wr * 64 + i * 16 + fr;
            aoff[i][ks] = row * 128 + (((ks * 4 + g) ^ (row & 7)) << 4);
        }
    const int q4 = (lane & 15) >> 2, p4 = lane & 3;
    const int swl = (NJ == 5) ? (g & 1) : (((q4 >> 1) & 1) | ((g & 1) << 1));
    int boff[NJ];
#pragma unroll
    for (int j = 0; j < NJ; ++j) {
        const int c = (NJ == 5) ? (5 * wc + j) : (j < 3 ? 3 * wc + j : 6 + 3 * wc + (j - 3));
        boff[j] = K1_ABYTES + (8 * g + q4) * ROWB + ((c ^ swl) << 5) + p4 * 8;
    }

    const int uw = __builtin_amdgcn_readfirstlane(wid);
    const unsigned sbase = lds_addr(smem) + uw * 1024;
    const bool b3 = (NJ == 6) || (uw < 4);
    auto stage_piece = [&](int kt, int buf, int pc) {
        const unsigned base = sbase + buf * K1_STAGE;
        if (pc < 4) glds16_asm(asrc[pc] + kt * 64, base + pc * 8192);
        else if (pc < 6 || b3) glds16_asm(bsrc[pc - 4] + (size_t)kt * 64 * LDB, base + K1_ABYTES + (pc - 4) * 8192);
    };

    const u16* pfp[2]; size_t pfstep[2];
#pragma unroll
    for (int i = 0; i < 2; ++i) {
        constexpr int NWS = 16 * UPR;
        const int xr = (256 + xcnt - 1) / xcnt;
        int e = wid * 128 + i * 64 + lane;
        if (e < NWS) {
            const int kr = (mt & 3) * 16 + e / UPR, u = e % UPR;
            if (NJ == 5) pfp[i] = W0 + (size_t)kr * LDB + c0 + u * 8;
            else pfp[i] = ((u < 12) ? W0 : W1) + (size_t)kr * LDB + c0 + (u % 12) * 8;
            pfstep[i] = (size_t)64 * LDB;
        } else {
            e -= NWS;
            int r = xs * xr + (e >> 3);
            if ((e >> 3) >= xr || r > 255) { r = xs * xr; }
            pfp[i] = X + (size_t)(m0 + r) * D_ + (e & 7) * 8;
            pfstep[i] = 64;
        }
    }
    const unsigned junk = lds_addr(smem) + 2 * K1_STAGE + uw * 1024;
    STAMP(0, 0);
#pragma unroll
    for (int pc = 0; pc < 7; ++pc) stage_piece(0, 0, pc);
    glds16_asm(pfp[0] + pfstep[0], junk);
    glds16_asm(pfp[1] + pfstep[1], junk);
    glds16_asm(pfp[0] + 2 * pfstep[0], junk);
    glds16_asm(pfp[1] + 2 * pfstep[1], junk);
    for (int kt = 0; kt < 10; ++kt) {
        STAMP(0, 1 + kt * 3);
        if (kt == 0) { WAIT_VM(4); } else { WAIT_VM(2); }
        STAMP(0, 2 + kt * 3);
        __builtin_amdgcn_s_barrier();
        CBAR();
        STAMP(0, 3 + kt * 3);
        const bool more = kt + 1 < 10;
        const int nb = (kt + 1) & 1;
        const char* base = smem + (kt & 1) * K1_STAGE;
#pragma unroll
        for (int ks = 0; ks < 2; ++ks) {
            bf16x8 af[4], bfr[NJ];
#pragma unroll
            for (int j = 0; j < NJ; ++j) {
                s16x4 lo = tr_read(base + boff[j] + ks * (32 * ROWB));
                s16x4 hi = tr_read(base + boff[j] + ks * (32 * ROWB) + 4 * ROWB);
                bfr[j] = cat4(lo, hi);
            }
#pragma unroll
            for (int i = 0; i < 4; ++i) af[i] = *(const bf16x8*)(base + aoff[i][ks]);
#pragma unroll
            for (int i = 0; i < 4; ++i) {
#pragma unroll
                for (int j = 0; j < NJ; ++j)
                    acc[i][j] = __builtin_amdgcn_mfma_f32_16x16x32_bf16(bfr[j], af[i], acc[i][j], 0, 0, 0);
                const int pc = ks * 4 + i;
                if (pc < 4 && more) { stage_piece(kt + 1, nb, 2 * pc); if (pc < 3) stage_piece(kt + 1, nb, 2 * pc + 1); }
                if (pc == 3) {
                    const int pk = kt + 3 < 10 ? kt + 3 : 9;
                    glds16_asm(pfp[0] + (size_t)pk * pfstep[0], junk);
                    glds16_asm(pfp[1] + (size_t)pk * pfstep[1], junk);
                }
            }
        }
    }

    STAMP(0, 40);
    WAIT_VM(0);
    if (NJ == 5) {
        __syncthreads();
#pragma unroll
        for (int i = 0; i < 4; ++i)
#pragma unroll
            for (int j = 0; j < NJ; ++j) {
                u32x2 a = {pack_bf16(acc[i][j][0], acc[i][j][1]), pack_bf16(acc[i][j][2], acc[i][j][3])};
                *(u32x2*)(smem + (wr * 64 + i * 16 + fr) * K1_OLD + (wc * 80 + j * 16 + g * 4) * 2) = a;
            }
        __syncthreads();
        const int which = nt >> 2;
        const int h0 = (nt & 3) * 8;
        u16* dbase = (u16*)(ws + WS_Q + (size_t)which * ((size_t)H_ * S_ * 32 * 2));
        const unsigned padw = (which == 2) ? 0x00003f80u : 0u;
        const int ck = t & 3;
#pragma unroll
        for (int it = 0; it < 16; ++it) {
            const int e = it * 128 + (t >> 2);
            const int hh = e >> 8, m = e & 255;
            const char* src = smem + m * K1_OLD + hh * 40 + ck * 16;
            u32x2 lo = {0u, 0u}, hi = {0u, 0u};
            if (ck < 3) lo = *(const u32x2*)src;
            if (ck < 2) hi = *(const u32x2*)(src + 8);
            if (ck == 2) hi[0] = padw;
            u32x4 v = {lo[0], lo[1], hi[0], hi[1]};
            *(u32x4*)(dbase + ((size_t)(h0 + hh) * S_ + m0 + m) * 32 + ck * 8) = v;
        }
    } else {
        u16* hF = (u16*)(ws + WS_H);
#pragma unroll
        for (int j = 0; j < 3; ++j) {
            const int f = c0 + wc * 48 + j * 16 + g * 4;
            const int ks = f >> 5;
            const int lq = (f & 31) >> 3;
            const int hf = f & 4;
#pragma unroll
            for (int i = 0; i < 4; ++i) {
                const int m = m0 + wr * 64 + i * 16 + fr;
                float hv[4];
#pragma unroll
                for (int r = 0; r < 4; ++r) {
                    const float gt = rbf(acc[i][j][r]);
                    const float up = rbf(acc[i][(j + 3) % NJ][r]);
                    const float sl = rbf(gt * __builtin_amdgcn_rcpf(1.f + __builtin_amdgcn_exp2f(gt * -1.4426950408889634f)));
                    hv[r] = sl * up;
                }
                u32x2 a = {pack_bf16(hv[0], hv[1]), pack_bf16(hv[2], hv[3])};
                u16* dst = hF + ((size_t)((m >> 4) * KS_H + ks) * 64 + (m & 15) + 16 * lq) * 8 + hf;
                *(u32x2*)dst = a;
            }
        }
    }
    STAMP(0, 41);
    STAMP(0, 42);
}

__global__ __launch_bounds__(512, 2) void k1_gemm(const u16* __restrict__ X, const u16* __restrict__ Wqkv,
                                                  const u16* __restrict__ Wg, const u16* __restrict__ Wu,
                                                  const u16* __restrict__ Wo, const u16* __restrict__ Wd,
                                                  const u16* __restrict__ gate_w, char* __restrict__ ws) {
    __shared__ __attribute__((aligned(16))) char smem[2 * K1_STAGE + 8192];
    const int bid = blockIdx.x;
    const int xcd = bid & 7, idx = bid >> 3;
    const int t = threadIdx.x;
    const int lane = t & 63, wid = t >> 6;
    const int ng = xcd >> 1, mg = xcd & 1;
    const int cnt = (ng < 2) ? 8 : 7;
    const int nstart = (ng < 2) ? ng * 8 : 16 + (ng - 2) * 7;

    if (idx >= 4 * cnt) {
        STAMP(0, 0);
        const int ab = (xcd - 4) * 4 + (idx - 28);
        const int uw = __builtin_amdgcn_readfirstlane(wid);
        const int gw = ab * 8 + uw;
        float* gout = (float*)(ws + WS_G);
        u16* woF = (u16*)(ws + WS_WO);
        u16* wdF = (u16*)(ws + WS_WD);
        const int q4 = (lane & 15) >> 2, p4 = lane & 3, g = lane >> 4;
        char* wl = smem + uw * 12288;
        const unsigned wla = lds_addr(wl);
        u32x4 w0 = *(const u32x4*)(gate_w + lane * 8);
        u32x4 w1 = {0, 0, 0, 0};
        if (lane < 16) w1 = *(const u32x4*)(gate_w + 512 + lane * 8);
        for (int round = 0; round < 2; ++round) {
#pragma unroll
            for (int uu = 0; uu < 3; ++uu) {
                int unit = gw + 128 * (round * 3 + uu);
                if (unit >= 740) unit = 739;
                const int ksg = unit / 10, cc = unit % 10;
                const u16* src = (ksg < KS_O) ? (Wo + (size_t)ksg * 32 * D_) : (Wd + (size_t)(ksg - KS_O) * 32 * D_);
#pragma unroll
                for (int i = 0; i < 4; ++i)
                    glds16_asm(src + (size_t)(i * 8 + (lane >> 3)) * D_ + cc * 64 + (lane & 7) * 8, wla + uu * 4096 + i * 1024);
            }
            {
                u32x4 x0[8], x1[8];
#pragma unroll
                for (int r = 0; r < 8; ++r) {
                    const int row = gw * 16 + round * 8 + r;
                    x0[r] = *(const u32x4*)(X + (size_t)row * D_ + lane * 8);
                    x1[r] = (u32x4){0, 0, 0, 0};
                    if (lane < 16) x1[r] = *(const u32x4*)(X + (size_t)row * D_ + 512 + lane * 8);
                }
#pragma unroll
                for (int r = 0; r < 8; ++r) {
                    float sacc = 0.f;
#pragma unroll
                    for (int e = 0; e < 4; ++e) {
                        sacc += rbf(__uint_as_float(x0[r][e] << 16) * __uint_as_float(w0[e] << 16));
                        sacc += rbf(__uint_as_float(x0[r][e] & 0xffff0000u) * __uint_as_float(w0[e] & 0xffff0000u));
                        sacc += rbf(__uint_as_float(x1[r][e] << 16) * __uint_as_float(w1[e] << 16));
                        sacc += rbf(__uint_as_float(x1[r][e] & 0xffff0000u) * __uint_as_float(w1[e] & 0xffff0000u));
                    }
#pragma unroll
                    for (int o = 32; o >= 1; o >>= 1) sacc += __shfl_xor(sacc, o);
                    if (lane == 0) {
                        float z = rbf(sacc);
                        gout[gw * 16 + round * 8 + r] = rbf(1.f / (1.f + __expf(-z)));
                    }
                }
            }
            WAIT_VM(0);
#pragma unroll
            for (int uu = 0; uu < 3; ++uu) {
                const int unit = gw + 128 * (round * 3 + uu);
                if (unit < 740) {
                    const int ksg = unit / 10, cc = unit % 10;
#pragma unroll
                    for (int j = 0; j < 4; ++j) {
                        const char* a = wl + uu * 4096 + (8 * g + q4) * 128 + j * 32 + p4 * 8;
                        s16x4 lo = tr_read(a);
                        s16x4 hi = tr_read(a + 4 * 128);
                        bf16x8 v = cat4(lo, hi);
                        const int nt = cc * 4 + j;
                        u16* dst = (ksg < KS_O) ? (woF + ((size_t)(nt * KS_O + ksg) * 64 + lane) * 8)
                                                : (wdF + ((size_t)(nt * KS_H + (ksg - KS_O)) * 64 + lane) * 8);
                        *(bf16x8*)dst = v;
                    }
                }
            }
            asm volatile("s_waitcnt lgkmcnt(0)" ::: "memory");
            STAMP(0, 40 + round);
        }
        return;
    }
    const int mt = mg * 4 + (idx & 3);
    const int nt = nstart + (idx >> 2);
    if (nt < 12) k1_tile<5>(X, Wqkv, Wqkv, ws, smem, mt, nt, idx >> 2, cnt);
    else k1_tile<6>(X, Wg, Wu, ws, smem, mt, nt - 12, idx >> 2, cnt);
}

__global__ __launch_bounds__(256, 2) void k2_attn(char* __restrict__ ws) {
    __shared__ __attribute__((aligned(16))) char smem[2 * 384 * 64];
    const int bid = blockIdx.x;
    const int h = (bid & 7) + 8 * ((bid >> 3) & 3);
    const int qb = bid >> 5;
    const int q0 = qb * 128;
    const int t = threadIdx.x, lane = t & 63, w = t >> 6;
    const u16* Qb = (const u16*)(ws + WS_Q) + (size_t)h * S_ * 32;
    const u16* Kb = (const u16*)(ws + WS_K) + (size_t)h * S_ * 32;
    const u16* Vb = (const u16*)(ws + WS_V) + (size_t)h * S_ * 32;
    char* sK = smem;
    char* sV = smem + 384 * 64;

#pragma unroll
    for (int i = 0; i < 6; ++i) {
        const int p = i * 256 + t;
        const int row = p >> 2, cp = p & 3;
        int key = q0 - 256 + row;
        key = key < 0 ? 0 : key;
        const int ck = cp ^ ((-(row >> 2)) & 3);
        const int cv = cp ^ (((row >> 2) & 1) << 1);
        glds16(Kb + (size_t)key * 32 + ck * 8, sK + p * 16);
        glds16(Vb + (size_t)key * 32 + cv * 8, sV + p * 16);
    }
    const int fr = lane & 15, g = lane >> 4;
    const int qw = q0 + 32 * w;
    bf16x8 qf[2];
#pragma unroll
    for (int qt = 0; qt < 2; ++qt) qf[qt] = *(const bf16x8*)(Qb + (size_t)(qw + qt * 16 + fr) * 32 + g * 8);
    __syncthreads();

    const int bstart = (qw >= 256) ? 0 : (8 - (qw >> 5));
    const float NEG = -__builtin_inff();
    f32x4 sc[9][2][2];
    float mx[2] = {NEG, NEG};
#pragma unroll
    for (int b = 0; b < 9; ++b) {
        if (b >= bstart) {
#pragma unroll
            for (int kt = 0; kt < 2; ++kt) {
                const int row = 32 * (w + b) + 16 * kt + fr;
                const bf16x8 kf = *(const bf16x8*)(sK + row * 64 + ((g ^ ((-(row >> 2)) & 3)) << 4));
#pragma unroll
                for (int qt = 0; qt < 2; ++qt) {
                    f32x4 z = {0.f, 0.f, 0.f, 0.f};
                    f32x4 s = __builtin_amdgcn_mfma_f32_16x16x32_bf16(kf, qf[qt], z, 0, 0, 0);
                    if (b == 0) {
#pragma unroll
                        for (int r = 0; r < 4; ++r)
                            if (16 * qt + fr >= 16 * kt + 4 * g + r) s[r] = NEG;
                    }
                    if (b == 8) {
#pragma unroll
                        for (int r = 0; r < 4; ++r)
                            if (16 * kt + 4 * g + r > 16 * qt + fr) s[r] = NEG;
                    }
                    sc[b][kt][qt] = s;
                    mx[qt] = fmaxf(mx[qt], fmaxf(fmaxf(s[0], s[1]), fmaxf(s[2], s[3])));
                }
            }
        }
    }
    constexpr float CSC = 0.22360679774997896f * 1.4426950408889634f;
    float mc[2];
#pragma unroll
    for (int qt = 0; qt < 2; ++qt) {
        float m = mx[qt];
        m = fmaxf(m, __shfl_xor(m, 16));
        m = fmaxf(m, __shfl_xor(m, 32));
        mc[qt] = -m * CSC;
    }
    f32x4 oacc[2][2];
#pragma unroll
    for (int qt = 0; qt < 2; ++qt)
#pragma unroll
        for (int dt = 0; dt < 2; ++dt) oacc[qt][dt] = (f32x4){0.f, 0.f, 0.f, 0.f};
    const int q4 = (lane & 15) >> 2, p4 = lane & 3;
#pragma unroll
    for (int b = 0; b < 9; ++b) {
        if (b >= bstart) {
            bf16x8 vf[2];
#pragma unroll
            for (int dt = 0; dt < 2; ++dt) {
                const int r0 = 32 * (w + b) + 4 * g + q4;
                const int r1 = r0 + 16;
                const int sw = (g & 1) << 5;
                s16x4 lo = tr_read(sV + r0 * 64 + ((dt * 32) ^ sw) + p4 * 8);
                s16x4 hi = tr_read(sV + r1 * 64 + ((dt * 32) ^ sw) + p4 * 8);
                vf[dt] = cat4(lo, hi);
            }
#pragma unroll
            for (int qt = 0; qt < 2; ++qt) {
                float pv[8];
#pragma unroll
                for (int kt = 0; kt < 2; ++kt)
#pragma unroll
                    for (int r = 0; r < 4; ++r)
                        pv[kt * 4 + r] = __builtin_amdgcn_exp2f(__builtin_fmaf(sc[b][kt][qt][r], CSC, mc[qt]));
                u32x4 pk = {pack_bf16(pv[0], pv[1]), pack_bf16(pv[2], pv[3]), pack_bf16(pv[4], pv[5]), pack_bf16(pv[6], pv[7])};
                const bf16x8 pf = __builtin_bit_cast(bf16x8, pk);
#pragma unroll
                for (int dt = 0; dt < 2; ++dt)
                    oacc[qt][dt] = __builtin_amdgcn_mfma_f32_16x16x32_bf16(vf[dt], pf, oacc[qt][dt], 0, 0, 0);
            }
        }
    }
    u16* oF = (u16*)(ws + WS_O);
#pragma unroll
    for (int qt = 0; qt < 2; ++qt) {
        const float sum = __shfl(oacc[qt][1][0], fr + 16);
        const float inv = 1.f / sum;
        const int m = qw + qt * 16 + fr;
        {
            const int k0 = h * DH_ + 4 * g;
            u32x2 a = {pack_bf16(oacc[qt][0][0] * inv, oacc[qt][0][1] * inv), pack_bf16(oacc[qt][0][2] * inv, oacc[qt][0][3] * inv)};
            u16* dst = oF + ((size_t)((m >> 4) * KS_O + (k0 >> 5)) * 64 + (m & 15) + 16 * ((k0 & 31) >> 3)) * 8 + (k0 & 4);
            *(u32x2*)dst = a;
        }
        if (g == 0) {
            const int k0 = h * DH_ + 16;
            u32x2 a = {pack_bf16(oacc[qt][1][0] * inv, oacc[qt][1][1] * inv), pack_bf16(oacc[qt][1][2] * inv, oacc[qt][1][3] * inv)};
            u16* dst = oF + ((size_t)((m >> 4) * KS_O + (k0 >> 5)) * 64 + (m & 15) + 16 * ((k0 & 31) >> 3)) * 8 + (k0 & 4);
            *(u32x2*)dst = a;
        }
    }
}

constexpr int RED_LD = 84;
__global__ __launch_bounds__(256, 1) void k3_gemm(const char* __restrict__ ws, u16* __restrict__ out) {
    __shared__ __attribute__((aligned(16))) float red[4 * 64 * RED_LD];
    const int bid = blockIdx.x;
    const int xcd = bid & 7, idx = bid >> 3;
    const int mt = (xcd >> 1) * 8 + (idx & 7);
    const int nt = (xcd & 1) * 4 + (idx >> 3);
    const int t = threadIdx.x, lane = t & 63, w = t >> 6;

    const u16* A; const u16* B; int nsteps, kstot, ks0;
    if (w == 0) { A = (const u16*)(ws + WS_O); B = (const u16*)(ws + WS_WO); nsteps = KS_O; kstot = KS_O; ks0 = 0; }
    else { A = (const u16*)(ws + WS_H); B = (const u16*)(ws + WS_WD); nsteps = 18; kstot = KS_H; ks0 = 18 * (w - 1); }
    const u16* ap = A + ((size_t)(mt * 4) * kstot + ks0) * 512 + lane * 8;
    const u16* bp = B + ((size_t)(nt * 5) * kstot + ks0) * 512 + lane * 8;
    const size_t fstride = (size_t)kstot * 512;

    f32x4 acc[4][5];
#pragma unroll
    for (int i = 0; i < 4; ++i)
#pragma unroll
        for (int j = 0; j < 5; ++j) acc[i][j] = (f32x4){0.f, 0.f, 0.f, 0.f};

    bf16x8 a0[4], b0[5], a1[4], b1[5];
#pragma unroll
    for (int i = 0; i < 4; ++i) a0[i] = *(const bf16x8*)(ap + i * fstride);
#pragma unroll
    for (int j = 0; j < 5; ++j) b0[j] = *(const bf16x8*)(bp + j * fstride);
    for (int s = 0; s < nsteps; s += 2) {
#pragma unroll
        for (int i = 0; i < 4; ++i) a1[i] = *(const bf16x8*)(ap + i * fstride + (s + 1) * 512);
#pragma unroll
        for (int j = 0; j < 5; ++j) b1[j] = *(const bf16x8*)(bp + j * fstride + (s + 1) * 512);
#pragma unroll
        for (int i = 0; i < 4; ++i)
#pragma unroll
            for (int j = 0; j < 5; ++j)
                acc[i][j] = __builtin_amdgcn_mfma_f32_16x16x32_bf16(b0[j], a0[i], acc[i][j], 0, 0, 0);
        if (s + 2 < nsteps) {
#pragma unroll
            for (int i = 0; i < 4; ++i) a0[i] = *(const bf16x8*)(ap + i * fstride + (s + 2) * 512);
#pragma unroll
            for (int j = 0; j < 5; ++j) b0[j] = *(const bf16x8*)(bp + j * fstride + (s + 2) * 512);
        }
#pragma unroll
        for (int i = 0; i < 4; ++i)
#pragma unroll
            for (int j = 0; j < 5; ++j)
                acc[i][j] = __builtin_amdgcn_mfma_f32_16x16x32_bf16(b1[j], a1[i], acc[i][j], 0, 0, 0);
    }
    {
        const int fr = lane & 15, g = lane >> 4;
        float* rw = red + w * 64 * RED_LD;
#pragma unroll
        for (int i = 0; i < 4; ++i)
#pragma unroll
            for (int j = 0; j < 5; ++j)
                *(f32x4*)(rw + (i * 16 + fr) * RED_LD + j * 16 + g * 4) = acc[i][j];
    }
    __syncthreads();
    {
        const int m = t >> 2, c0 = (t & 3) * 20;
        const float gg = ((const float*)(ws + WS_G))[mt * 64 + m];
        const float omg = rbf(1.f - gg);
        u16* dst = out + (size_t)(mt * 64 + m) * D_ + nt * 80 + c0;
#pragma unroll
        for (int c = 0; c < 20; c += 4) {
            f32x4 va = *(const f32x4*)(red + (0 * 64 + m) * RED_LD + c0 + c);
            f32x4 v1 = *(const f32x4*)(red + (1 * 64 + m) * RED_LD + c0 + c);
            f32x4 v2 = *(const f32x4*)(red + (2 * 64 + m) * RED_LD + c0 + c);
            f32x4 v3 = *(const f32x4*)(red + (3 * 64 + m) * RED_LD + c0 + c);
            float o[4];
#pragma unroll
            for (int e = 0; e < 4; ++e) {
                const float a = rbf(va[e]);
                const float b = rbf(v1[e] + v2[e] + v3[e]);
                o[e] = rbf(gg * a) + rbf(omg * b);
            }
            u32x2 pk = {pack_bf16(o[0], o[1]), pack_bf16(o[2], o[3])};
            *(u32x2*)(dst + c) = pk;
        }
    }
}

extern "C" void kernel_launch(void* const* d_in, const int* in_sizes, int n_in,
                              void* d_out, int out_size, void* d_ws, size_t ws_size,
                              hipStream_t stream) {
    const u16* X = (const u16*)d_in[0];
    const u16* Wqkv = (const u16*)d_in[1];
    const u16* Wo = (const u16*)d_in[2];
    const u16* Wg = (const u16*)d_in[4];
    const u16* Wu = (const u16*)d_in[5];
    const u16* Wd = (const u16*)d_in[6];
    const u16* gw = (const u16*)d_in[7];
    char* ws = (char*)d_ws;
    k1_gemm<<<256, 512, 0, stream>>>(X, Wqkv, Wg, Wu, Wo, Wd, gw, ws);
    k2_attn<<<512, 256, 0, stream>>>(ws);
    k3_gemm<<<256, 256, 0, stream>>>(ws, (u16*)d_out);
}
```

```cpp
#include <hip/hip_runtime.h>
#include <hip/hip_bf16.h>
#include <stdint.h>

typedef unsigned short u16;
typedef short bf16x8 __attribute__((ext_vector_type(8)));
typedef short s16x4 __attribute__((ext_vector_type(4)));
typedef float f32x4 __attribute__((ext_vector_type(4)));
typedef float f32x2 __attribute__((ext_vector_type(2)));
typedef unsigned u32x4 __attribute__((ext_vector_type(4)));
typedef unsigned u32x2 __attribute__((ext_vector_type(2)));
typedef __bf16 bf2_t __attribute__((ext_vector_type(2)));

#define LDS_AS __attribute__((address_space(3)))

constexpr int S_ = 2048, D_ = 640, F_ = 1728, H_ = 32, DH_ = 20, NQKV = 1920;
constexpr int KS_O = D_ / 32;
constexpr int KS_H = F_ / 32;

constexpr size_t WS_Q = 0;
constexpr size_t QKV_BYTES = (size_t)H_ * S_ * DH_ * 2;
constexpr size_t WS_K = WS_Q + QKV_BYTES;
constexpr size_t WS_V = WS_K + QKV_BYTES;
constexpr size_t WS_H = WS_V + QKV_BYTES + 4096;
constexpr size_t WS_O = WS_H + (size_t)S_ * F_ * 2;
constexpr size_t WS_WO = WS_O + (size_t)S_ * D_ * 2;
constexpr size_t WS_WD = WS_WO + (size_t)D_ * D_ * 2;
constexpr size_t WS_G = WS_WD + (size_t)F_ * D_ * 2;

__device__ __forceinline__ unsigned pack_bf16(float a, float b) {
    bf2_t v = {(__bf16)a, (__bf16)b};
    return __builtin_bit_cast(unsigned, v);
}
__device__ __forceinline__ float rbf(float x) { return (float)(__bf16)x; }
__device__ __forceinline__ float bf2f(u16 v) { return __uint_as_float(((unsigned)v) << 16); }

__device__ __forceinline__ s16x4 tr_read(const char* lds_ptr) {
    return __builtin_amdgcn_ds_read_tr16_b64_v4i16((LDS_AS s16x4*)(lds_ptr));
}
__device__ __forceinline__ bf16x8 cat4(s16x4 a, s16x4 b) {
    bf16x8 r;
    r[0] = a[0]; r[1] = a[1]; r[2] = a[2]; r[3] = a[3];
    r[4] = b[0]; r[5] = b[1]; r[6] = b[2]; r[7] = b[3];
    return r;
}
__device__ __forceinline__ void glds16(const void* g, void* l) {
    __builtin_amdgcn_global_load_lds((const unsigned*)g, (LDS_AS unsigned*)l, 16, 0, 0);
}
__device__ __forceinline__ void glds16_asm(const void* gsrc, unsigned lds_dst) {
    unsigned keep;
    asm volatile("s_mov_b32 %0, m0\n\ts_mov_b32 m0, %2\n\ts_nop 0\n\tglobal_load_lds_dwordx4 %1, off\n\ts_mov_b32 m0, %0"
                 : "=&s"(keep) : "v"(gsrc), "s"(lds_dst) : "memory");
}
__device__ __forceinline__ void glds4_asm(const void* gsrc, unsigned lds_dst) {
    unsigned keep;
    asm volatile("s_mov_b32 %0, m0\n\ts_mov_b32 m0, %2\n\ts_nop 0\n\tglobal_load_lds_dword %1, off\n\ts_mov_b32 m0, %0"
                 : "=&s"(keep) : "v"(gsrc), "s"(lds_dst) : "memory");
}
__device__ __forceinline__ unsigned lds_addr(const void* p) { return (unsigned)(size_t)(LDS_AS const char*)p; }
#define WAIT_VM(n) asm volatile("s_waitcnt vmcnt(" #n ")" ::: "memory")
#define CBAR() asm volatile("" ::: "memory")
#define STAMP(kid, i) do {} while (0)
#define STAMPK(kid, i) do { if ((i) == 3) STAMP(kid, i); } while (0)

#define K1UNR 2
constexpr int K1_ABYTES = 256 * 64 * 2;
constexpr int K1_BBYTES = 64 * 192 * 2;
constexpr int K1_BOFF = 3 * K1_ABYTES;
constexpr int K1_AUX = K1_BOFF + 2 * K1_BBYTES;
constexpr int K1_JUNK = K1_AUX + 3 * 4096;
constexpr int K1_LDS = K1_JUNK + 256;

template <int NJ>
__device__ __forceinline__ void k1_epilogue(f32x4 (&acc)[4][NJ], char* __restrict__ ws, char* smem, int m0, int c0, int nt) {
    int t = threadIdx.x;
    asm volatile("" : "+v"(t));
    const int lane = t & 63, wid = t >> 6;
    const int wr = wid >> 1, wc = wid & 1;
    const int fr = lane & 15, g = lane >> 4;
    if (NJ == 5) {
        __syncthreads();
#pragma unroll
        for (int j = 0; j < NJ; ++j) {
            const int n = wc * 80 + j * 16 + g * 4;
            const int hh = n / DH_, d = n % DH_;
            char* dst = smem + hh * 10240 + (wr * 64 + fr) * 40 + d * 2;
#pragma unroll
            for (int i = 0; i < 4; ++i) {
                u32x2 a = {pack_bf16(acc[i][j][0], acc[i][j][1]), pack_bf16(acc[i][j][2], acc[i][j][3])};
                *(u32x2*)(dst + i * 640) = a;
            }
        }
        __syncthreads();
        const int which = nt >> 2;
        const int h0 = (nt & 3) * 8;
        char* dbase = ws + WS_Q + (size_t)which * QKV_BYTES + ((size_t)h0 * S_ + m0) * 40;
#pragma unroll
        for (int it = 0; it < 10; ++it) {
            const int e = it * 512 + t;
            const int hh = e / 640, r = e % 640;
            const u32x4 v = *(const u32x4*)(smem + e * 16);
            *(u32x4*)(dbase + (size_t)hh * (S_ * 40) + r * 16) = v;
        }
    } else {
        u16* hF = (u16*)(ws + WS_H);
#pragma unroll
        for (int j = 0; j < 3; ++j) {
            const int f = c0 + wc * 48 + j * 16 + g * 4;
            const int ks = f >> 5;
            const int lq = (f & 31) >> 3;
            const int hf = f & 4;
#pragma unroll
            for (int i = 0; i < 4; ++i) {
                const int m = m0 + wr * 64 + i * 16 + fr;
                unsigned pk[2];
#pragma unroll
                for (int r = 0; r < 2; ++r) {
                    const f32x2 gt = {acc[i][j][2 * r], acc[i][j][2 * r + 1]};
                    const f32x2 up = {acc[i][(j + 3) % NJ][2 * r], acc[i][(j + 3) % NJ][2 * r + 1]};
                    f32x2 e = gt * -1.4426950408889634f;
                    e = (f32x2){__builtin_amdgcn_exp2f(e[0]), __builtin_amdgcn_exp2f(e[1])};
                    e = e + 1.f;
                    const f32x2 rc = {__builtin_amdgcn_rcpf(e[0]), __builtin_amdgcn_rcpf(e[1])};
                    const f32x2 hv = (gt * up) * rc;
                    pk[r] = pack_bf16(hv[0], hv[1]);
                }
                u32x2 a = {pk[0], pk[1]};
                u16* dst = hF + ((size_t)((m >> 4) * KS_H + ks) * 64 + (m & 15) + 16 * lq) * 8 + hf;
                *(u32x2*)dst = a;
            }
        }
    }
}

template <int NJ>
__device__ __forceinline__ void k1_tile(const u16* __restrict__ X, const u16* __restrict__ W0, const u16* __restrict__ W1,
                                        char* __restrict__ ws, char* smem, int mt, int nt, int xs, int xcnt) {
    constexpr int BN = 32 * NJ;
    constexpr int ROWB = BN * 2;
    constexpr int UPR = BN / 8;
    constexpr int LDB = (NJ == 5) ? NQKV : F_;
    const int t = threadIdx.x;
    const int lane = t & 63, wid = t >> 6;
    const int m0 = mt * 256;
    const int c0 = (NJ == 5) ? nt * 160 : nt * 96;
    const int uw = __builtin_amdgcn_readfirstlane(wid);
    const unsigned sb0 = lds_addr(smem);

    if (uw == 10) {
        const u16* pp[3]; unsigned ps[3];
#pragma unroll
        for (int i = 0; i < 3; ++i) {
            constexpr int SPR = BN / 32;
            constexpr int NWS = 16 * SPR;
            const int xr = (256 + xcnt - 1) / xcnt;
            int e = i * 64 + lane;
            if (e < NWS) {
                const int kr = (mt & 3) * 16 + e / SPR, u = e % SPR;
                if (NJ == 5) pp[i] = W0 + (size_t)kr * LDB + c0 + u * 32;
                else pp[i] = ((u < 3) ? W0 : W1) + (size_t)kr * LDB + c0 + (u % 3) * 32;
                ps[i] = 64 * LDB;
            } else {
                e -= NWS;
                int r = xs * xr + (e >> 1);
                if ((e >> 1) >= xr || r > 255) { r = xs * xr; }
                pp[i] = X + (size_t)(m0 + r) * D_ + (e & 1) * 32;
                ps[i] = 64;
            }
        }
        const unsigned junk = sb0 + K1_JUNK;
        auto pf = [&](int kt) {
#pragma unroll
            for (int i = 0; i < 3; ++i) glds4_asm(pp[i] + (size_t)kt * ps[i], junk);
        };
        pf(1);
        for (int kt = 0; kt < 10; ++kt) {
            __builtin_amdgcn_s_barrier();
            if (kt < 2) { pf(2 * kt + 2); pf(2 * kt + 3); }
            else if (kt + 4 < 10) pf(kt + 4);
        }
        WAIT_VM(0);
        return;
    }
    if (uw >= 8) {
        const int dw = uw - 8;
        constexpr int NBI = 2 * NJ;
        const u16* pa = X + (size_t)(m0 + dw * 128 + (lane >> 3)) * D_ + (((lane & 7) ^ (lane >> 3)) << 3);
        const u16* pb[NBI];
#pragma unroll
        for (int i = 0; i < NBI; ++i) {
            const int p = (dw * NBI + i) * 64 + lane;
            const int k = p / UPR, u = p % UPR;
            const int sw = (NJ == 5) ? ((k >> 3) & 1) : (((k >> 1) & 1) | (((k >> 3) & 1) << 1));
            const int c32 = (u >> 1) ^ sw, half = u & 1;
            if (NJ == 5) pb[i] = W0 + (size_t)k * LDB + c0 + c32 * 16 + half * 8;
            else pb[i] = ((c32 < 6) ? W0 : W1) + (size_t)k * LDB + c0 + (c32 < 6 ? c32 : c32 - 6) * 16 + half * 8;
        }
        auto loadA = [&](int kt) {
            const unsigned base = sb0 + (kt % 3) * K1_ABYTES + dw * 16384;
#pragma unroll
            for (int i = 0; i < 16; ++i) glds16_asm(pa + (size_t)i * 8 * D_ + kt * 64, base + i * 1024);
        };
        auto loadB = [&](int kt) {
            const unsigned base = sb0 + K1_BOFF + (kt & 1) * K1_BBYTES + dw * NBI * 1024;
#pragma unroll
            for (int i = 0; i < NBI; ++i) glds16_asm(pb[i] + (size_t)kt * 64 * LDB, base + i * 1024);
        };
        STAMP(0, 0);
        loadB(0); loadA(1);
        WAIT_VM(16);
        for (int kt = 0; kt < 10; ++kt) {
            STAMPK(0, 2 + kt * 3);
            __builtin_amdgcn_s_barrier();
            STAMPK(0, 3 + kt * 3);
            if (kt + 1 < 10) loadB(kt + 1);
            if (kt + 2 < 10) { loadA(kt + 2); STAMPK(0, 4 + kt * 3); WAIT_VM(16); } else { STAMPK(0, 4 + kt * 3); WAIT_VM(0); }
        }
        STAMP(0, 40); STAMP(0, 41); STAMP(0, 42);
        return;
    }

    STAMP(0, 0);
    {
        const u16* pa = X + (size_t)(m0 + uw * 32 + (lane >> 3)) * D_ + (((lane & 7) ^ (lane >> 3)) << 3);
#pragma unroll
        for (int i = 0; i < 4; ++i) glds16_asm(pa + (size_t)i * 8 * D_, sb0 + uw * 4096 + i * 1024);
    }
    const int wr = wid >> 1, wc = wid & 1;
    const int fr = lane & 15, g = lane >> 4;
    f32x4 acc[4][NJ];
#pragma unroll
    for (int i = 0; i < 4; ++i)
#pragma unroll
        for (int j = 0; j < NJ; ++j) acc[i][j] = (f32x4){0.f, 0.f, 0.f, 0.f};

    int aoff[2];
#pragma unroll
    for (int ks = 0; ks < 2; ++ks) {
        const int row = wr * 64 + fr;
        aoff[ks] = row * 128 + (((ks * 4 + g) ^ (row & 7)) << 4);
    }
    const int q4 = (lane & 15) >> 2, p4 = lane & 3;
    const int swl = (NJ == 5) ? (g & 1) : (((q4 >> 1) & 1) | ((g & 1) << 1));
    int boff[NJ];
#pragma unroll
    for (int j = 0; j < NJ; ++j) {
        const int c = (NJ == 5) ? (5 * wc + j) : (j < 3 ? 3 * wc + j : 6 + 3 * wc + (j - 3));
        boff[j] = K1_BOFF + (8 * g + q4) * ROWB + ((c ^ swl) << 5) + p4 * 8;
    }
    WAIT_VM(0);
    int abuf = 0;
#pragma unroll K1UNR
    for (int kt = 0; kt < 10; ++kt) {
        STAMPK(0, 1 + kt * 3);
        STAMPK(0, 2 + kt * 3);
        asm volatile("s_waitcnt lgkmcnt(0)" ::: "memory");
        __builtin_amdgcn_s_barrier();
        CBAR();
        STAMPK(0, 3 + kt * 3);
        const char* baseA = smem + abuf * K1_ABYTES;
        const char* baseB = smem + (kt & 1) * K1_BBYTES;
        abuf = (abuf == 2) ? 0 : abuf + 1;
#pragma unroll
        for (int ks = 0; ks < 2; ++ks) {
            bf16x8 af[4], bfr[NJ];
#pragma unroll
            for (int j = 0; j < NJ; ++j) {
                s16x4 lo = tr_read(baseB + boff[j] + ks * (32 * ROWB));
                s16x4 hi = tr_read(baseB + boff[j] + ks * (32 * ROWB) + 4 * ROWB);
                bfr[j] = cat4(lo, hi);
            }
#pragma unroll
            for (int i = 0; i < 4; ++i) af[i] = *(const bf16x8*)(baseA + aoff[ks] + i * 2048);
#pragma unroll
            for (int i = 0; i < 4; ++i)
#pragma unroll
                for (int j = 0; j < NJ; ++j)
                    acc[i][j] = __builtin_amdgcn_mfma_f32_16x16x32_bf16(bfr[j], af[i], acc[i][j], 0, 0, 0);
        }
    }

    STAMP(0, 40);
    k1_epilogue<NJ>(acc, ws, smem, m0, c0, nt);
    STAMP(0, 41);
    STAMP(0, 42);
}

__device__ __forceinline__ void k1_aux(const u16* __restrict__ X, const u16* __restrict__ Wo, const u16* __restrict__ Wd,
                                       const u16* __restrict__ gate_w, char* __restrict__ ws, char* smem, int lin) {
    const int lane = threadIdx.x & 63;
    float* gout = (float*)(ws + WS_G);
    u16* woF = (u16*)(ws + WS_WO);
    u16* wdF = (u16*)(ws + WS_WD);
    const int q4 = (lane & 15) >> 2, p4 = lane & 3, g = lane >> 4;
    char* wl = smem + K1_AUX;
    const unsigned wla = lds_addr(wl);
    auto issue = [&](int unit, int slot) {
        const int ksg = unit / 10, cc = unit % 10;
        const u16* src = (ksg < KS_O) ? (Wo + (size_t)ksg * 32 * D_) : (Wd + (size_t)(ksg - KS_O) * 32 * D_);
#pragma unroll
        for (int i = 0; i < 4; ++i)
            glds16_asm(src + (size_t)(i * 8 + (lane >> 3)) * D_ + cc * 64 + (lane & 7) * 8, wla + slot * 4096 + i * 1024);
    };
    auto emit = [&](int unit, int slot) {
        const int ksg = unit / 10, cc = unit % 10;
#pragma unroll
        for (int j = 0; j < 4; ++j) {
            const char* a = wl + slot * 4096 + (8 * g + q4) * 128 + j * 32 + p4 * 8;
            s16x4 lo = tr_read(a);
            s16x4 hi = tr_read(a + 4 * 128);
            bf16x8 v = cat4(lo, hi);
            const int nt = cc * 4 + j;
            u16* dst = (ksg < KS_O) ? (woF + ((size_t)(nt * KS_O + ksg) * 64 + lane) * 8)
                                    : (wdF + ((size_t)(nt * KS_H + (ksg - KS_O)) * 64 + lane) * 8);
            *(bf16x8*)dst = v;
        }
    };
    __builtin_amdgcn_s_barrier();
    issue(lin, 0); issue(lin + 240, 1); issue(lin + 480, 2);
    __builtin_amdgcn_s_barrier();
    __builtin_amdgcn_s_barrier();
    WAIT_VM(0);
    emit(lin, 0); emit(lin + 240, 1); emit(lin + 480, 2);
    asm volatile("s_waitcnt lgkmcnt(0)" ::: "memory");
    const bool extra = lin + 720 < 740;
    if (extra) issue(lin + 720, 0);
    __builtin_amdgcn_s_barrier();
    u32x4 w0 = *(const u32x4*)(gate_w + lane * 8);
    u32x4 w1 = {0, 0, 0, 0};
    if (lane < 16) w1 = *(const u32x4*)(gate_w + 512 + lane * 8);
    u32x4 x0[9], x1[9];
#pragma unroll
    for (int r = 0; r < 9; ++r) {
        int row = lin + 240 * r;
        row = row < S_ ? row : lin;
        x0[r] = *(const u32x4*)(X + (size_t)row * D_ + lane * 8);
        x1[r] = (u32x4){0, 0, 0, 0};
        if (lane < 16) x1[r] = *(const u32x4*)(X + (size_t)row * D_ + 512 + lane * 8);
    }
    __builtin_amdgcn_s_barrier();
    __builtin_amdgcn_s_barrier();
#pragma unroll
    for (int r = 0; r < 9; ++r) {
        float sacc = 0.f;
#pragma unroll
        for (int e = 0; e < 4; ++e) {
            sacc += rbf(__uint_as_float(x0[r][e] << 16) * __uint_as_float(w0[e] << 16));
            sacc += rbf(__uint_as_float(x0[r][e] & 0xffff0000u) * __uint_as_float(w0[e] & 0xffff0000u));
            sacc += rbf(__uint_as_float(x1[r][e] << 16) * __uint_as_float(w1[e] << 16));
            sacc += rbf(__uint_as_float(x1[r][e] & 0xffff0000u) * __uint_as_float(w1[e] & 0xffff0000u));
        }
#pragma unroll
        for (int o = 32; o >= 1; o >>= 1) sacc += __shfl_xor(sacc, o);
        const int row = lin + 240 * r;
        if (lane == 0 && row < S_) {
            float z = rbf(sacc);
            gout[row] = rbf(1.f / (1.f + __expf(-z)));
        }
    }
    __builtin_amdgcn_s_barrier();
    WAIT_VM(0);
    if (extra) emit(lin + 720, 0);
    __builtin_amdgcn_s_barrier();
    __builtin_amdgcn_s_barrier();
    __builtin_amdgcn_s_barrier();
}

__global__ __launch_bounds__(768) void k1_gemm(const u16* __restrict__ X, const u16* __restrict__ Wqkv,
                                               const u16* __restrict__ Wg, const u16* __restrict__ Wu,
                                               const u16* __restrict__ Wo, const u16* __restrict__ Wd,
                                               const u16* __restrict__ gate_w, char* __restrict__ ws) {
    __shared__ __attribute__((aligned(16))) char smem[K1_LDS];
    const int bid = blockIdx.x;
    const int xcd = bid & 7, idx = bid >> 3;
    const int ng = xcd >> 1, mg = xcd & 1;
    const int cnt = (ng < 2) ? 8 : 7;
    const int nstart = (ng < 2) ? ng * 8 : 16 + (ng - 2) * 7;
    if (idx >= 4 * cnt) return;
    if (__builtin_amdgcn_readfirstlane(threadIdx.x >> 6) == 11) {
        const int lin = (ng < 2) ? xcd * 32 + idx : 128 + (xcd - 4) * 28 + idx;
        k1_aux(X, Wo, Wd, gate_w, ws, smem, lin);
        return;
    }
    const int mt = mg * 4 + (idx & 3);
    const int nt = nstart + (idx >> 2);
    if (nt < 12) k1_tile<5>(X, Wqkv, Wqkv, ws, smem, mt, nt, idx >> 2, cnt);
    else k1_tile<6>(X, Wg, Wu, ws, smem, mt, nt - 12, idx >> 2, cnt);
}

__global__ __launch_bounds__(256, 2) void k2_attn(char* __restrict__ ws) {
    __shared__ __attribute__((aligned(16))) char smem[2 * 15360 + 64];
    const int bid = blockIdx.x;
    const int h = (bid & 7) + 8 * ((bid >> 3) & 3);
    const int qb = bid >> 5;
    const int q0 = qb * 128;
    const int t = threadIdx.x, lane = t & 63, w = t >> 6;
    const char* Qb = ws + WS_Q + (size_t)h * S_ * 40;
    const char* Kb = ws + WS_K + (size_t)h * S_ * 40;
    const char* Vb = ws + WS_V + (size_t)h * S_ * 40;
    char* sK = smem;
    char* sV = smem + 15360;

#pragma unroll
    for (int i = 0; i < 4; ++i) {
        const int p = i * 256 + t;
        if (i < 3 || w < 3) {
            int off = (q0 - 256) * 40 + p * 16;
            off = off < 0 ? 0 : off;
            glds16(Kb + off, sK + p * 16);
            glds16(Vb + off, sV + p * 16);
        }
    }
    if (t < 4) *(u32x4*)(smem + 2 * 15360 + t * 16) = (u32x4){0u, 0u, 0u, 0u};
    const int fr = lane & 15, g = lane >> 4;
    const int qw = q0 + 32 * w;
    bf16x8 qf[2];
#pragma unroll
    for (int qt = 0; qt < 2; ++qt) {
        const char* qp = Qb + (size_t)(qw + qt * 16 + fr) * 40 + (g < 3 ? g : 2) * 16;
        u32x2 lo = *(const u32x2*)qp;
        u32x2 hi = *(const u32x2*)(qp + (g < 2 ? 8 : 0));
        if (g >= 2) hi = (u32x2){0u, 0u};
        if (g == 3) lo = (u32x2){0u, 0u};
        u32x4 v = {lo[0], lo[1], hi[0], hi[1]};
        qf[qt] = __builtin_bit_cast(bf16x8, v);
    }
    __syncthreads();

    const int bstart = (qw >= 256) ? 0 : (8 - (qw >> 5));
    const float NEG = -__builtin_inff();
    f32x4 sc[9][2][2];
    float mx[2] = {NEG, NEG};
#pragma unroll
    for (int b = 0; b < 9; ++b) {
        if (b >= bstart) {
#pragma unroll
            for (int kt = 0; kt < 2; ++kt) {
                const int row = 32 * (w + b) + 16 * kt + fr;
                const char* kp = sK + row * 40 + (g < 3 ? g : 2) * 16;
                const u32x2 klo = *(const u32x2*)kp, khi = *(const u32x2*)(kp + 8);
                const u32x4 kv = {klo[0], klo[1], khi[0], khi[1]};
                const bf16x8 kf = __builtin_bit_cast(bf16x8, kv);
#pragma unroll
                for (int qt = 0; qt < 2; ++qt) {
                    f32x4 z = {0.f, 0.f, 0.f, 0.f};
                    f32x4 s = __builtin_amdgcn_mfma_f32_16x16x32_bf16(kf, qf[qt], z, 0, 0, 0);
                    if (b == 0) {
#pragma unroll
                        for (int r = 0; r < 4; ++r)
                            if (16 * qt + fr >= 16 * kt + 4 * g + r) s[r] = NEG;
                    }
                    if (b == 8) {
#pragma unroll
                        for (int r = 0; r < 4; ++r)
                            if (16 * kt + 4 * g + r > 16 * qt + fr) s[r] = NEG;
                    }
                    sc[b][kt][qt] = s;
                    mx[qt] = fmaxf(mx[qt], fmaxf(fmaxf(s[0], s[1]), fmaxf(s[2], s[3])));
                }
            }
        }
    }
    constexpr float CSC = 0.22360679774997896f * 1.4426950408889634f;
    float mc[2];
#pragma unroll
    for (int qt = 0; qt < 2; ++qt) {
        float m = mx[qt];
        m = fmaxf(m, __shfl_xor(m, 16));
        m = fmaxf(m, __shfl_xor(m, 32));
        mc[qt] = -m * CSC;
    }
    f32x4 oacc[2][3];
#pragma unroll
    for (int qt = 0; qt < 2; ++qt)
#pragma unroll
        for (int dt = 0; dt < 3; ++dt) oacc[qt][dt] = (f32x4){0.f, 0.f, 0.f, 0.f};
    const bf16x8 ones = {0x3f80, 0x3f80, 0x3f80, 0x3f80, 0x3f80, 0x3f80, 0x3f80, 0x3f80};
    const int q4 = (lane & 15) >> 2, p4 = lane & 3;
#pragma unroll
    for (int b = 0; b < 9; ++b) {
        if (b >= bstart) {
            bf16x8 vf[2];
#pragma unroll
            for (int dt = 0; dt < 2; ++dt) {
                const int r0 = 32 * (w + b) + 4 * g + q4;
                s16x4 lo = tr_read(sV + r0 * 40 + dt * 32 + p4 * 8);
                s16x4 hi = tr_read(sV + (r0 + 16) * 40 + dt * 32 + p4 * 8);
                vf[dt] = cat4(lo, hi);
            }
#pragma unroll
            for (int qt = 0; qt < 2; ++qt) {
                float pv[8];
#pragma unroll
                for (int kt = 0; kt < 2; ++kt)
#pragma unroll
                    for (int r = 0; r < 4; ++r)
                        pv[kt * 4 + r] = __builtin_amdgcn_exp2f(__builtin_fmaf(sc[b][kt][qt][r], CSC, mc[qt]));
                u32x4 pk = {pack_bf16(pv[0], pv[1]), pack_bf16(pv[2], pv[3]), pack_bf16(pv[4], pv[5]), pack_bf16(pv[6], pv[7])};
                const bf16x8 pf = __builtin_bit_cast(bf16x8, pk);
#pragma unroll
                for (int dt = 0; dt < 2; ++dt)
                    oacc[qt][dt] = __builtin_amdgcn_mfma_f32_16x16x32_bf16(vf[dt], pf, oacc[qt][dt], 0, 0, 0);
                oacc[qt][2] = __builtin_amdgcn_mfma_f32_16x16x32_bf16(ones, pf, oacc[qt][2], 0, 0, 0);
            }
        }
    }
    u16* oF = (u16*)(ws + WS_O);
#pragma unroll
    for (int qt = 0; qt < 2; ++qt) {
        const float inv = 1.f / oacc[qt][2][0];
        const int m = qw + qt * 16 + fr;
        {
            const int k0 = h * DH_ + 4 * g;
            u32x2 a = {pack_bf16(oacc[qt][0][0] * inv, oacc[qt][0][1] * inv), pack_bf16(oacc[qt][0][2] * inv, oacc[qt][0][3] * inv)};
            u16* dst = oF + ((size_t)((m >> 4) * KS_O + (k0 >> 5)) * 64 + (m & 15) + 16 * ((k0 & 31) >> 3)) * 8 + (k0 & 4);
            *(u32x2*)dst = a;
        }
        if (g == 0) {
            const int k0 = h * DH_ + 16;
            u32x2 a = {pack_bf16(oacc[qt][1][0] * inv, oacc[qt][1][1] * inv), pack_bf16(oacc[qt][1][2] * inv, oacc[qt][1][3] * inv)};
            u16* dst = oF + ((size_t)((m >> 4) * KS_O + (k0 >> 5)) * 64 + (m & 15) + 16 * ((k0 & 31) >> 3)) * 8 + (k0 & 4);
            *(u32x2*)dst = a;
        }
    }
}

constexpr int RED_LD = 84;
__global__ __launch_bounds__(256, 1) void k3_gemm(const char* __restrict__ ws, u16* __restrict__ out) {
    __shared__ __attribute__((aligned(16))) float red[4 * 64 * RED_LD];
    const int bid = blockIdx.x;
    const int xcd = bid & 7, idx = bid >> 3;
    const int mt = (xcd >> 1) * 8 + (idx & 7);
    const int nt = (xcd & 1) * 4 + (idx >> 3);
    const int t = threadIdx.x, lane = t & 63, w = t >> 6;

    const u16* A; const u16* B; int nsteps, kstot, ks0;
    if (w == 0) { A = (const u16*)(ws + WS_O); B = (const u16*)(ws + WS_WO); nsteps = KS_O; kstot = KS_O; ks0 = 0; }
    else { A = (const u16*)(ws + WS_H); B = (const u16*)(ws + WS_WD); nsteps = 18; kstot = KS_H; ks0 = 18 * (w - 1); }
    const u16* ap = A + ((size_t)(mt * 4) * kstot + ks0) * 512 + lane * 8;
    const u16* bp = B + ((size_t)(nt * 5) * kstot + ks0) * 512 + lane * 8;
    const size_t fstride = (size_t)kstot * 512;

    f32x4 acc[4][5];
#pragma unroll
    for (int i = 0; i < 4; ++i)
#pragma unroll
        for (int j = 0; j < 5; ++j) acc[i][j] = (f32x4){0.f, 0.f, 0.f, 0.f};

    bf16x8 a0[4], b0[5], a1[4], b1[5];
#pragma unroll
    for (int i = 0; i < 4; ++i) a0[i] = *(const bf16x8*)(ap + i * fstride);
#pragma unroll
    for (int j = 0; j < 5; ++j) b0[j] = *(const bf16x8*)(bp + j * fstride);
    for (int s = 0; s < nsteps; s += 2) {
#pragma unroll
        for (int i = 0; i < 4; ++i) a1[i] = *(const bf16x8*)(ap + i * fstride + (s + 1) * 512);
#pragma unroll
        for (int j = 0; j < 5; ++j) b1[j] = *(const bf16x8*)(bp + j * fstride + (s + 1) * 512);
#pragma unroll
        for (int i = 0; i < 4; ++i)
#pragma unroll
            for (int j = 0; j < 5; ++j)
                acc[i][j] = __builtin_amdgcn_mfma_f32_16x16x32_bf16(b0[j], a0[i], acc[i][j], 0, 0, 0);
        if (s + 2 < nsteps) {
#pragma unroll
            for (int i = 0; i < 4; ++i) a0[i] = *(const bf16x8*)(ap + i * fstride + (s + 2) * 512);
#pragma unroll
            for (int j = 0; j < 5; ++j) b0[j] = *(const bf16x8*)(bp + j * fstride + (s + 2) * 512);
        }
#pragma unroll
        for (int i = 0; i < 4; ++i)
#pragma unroll
            for (int j = 0; j < 5; ++j)
                acc[i][j] = __builtin_amdgcn_mfma_f32_16x16x32_bf16(b1[j], a1[i], acc[i][j], 0, 0, 0);
    }
    {
        const int fr = lane & 15, g = lane >> 4;
        float* rw = red + w * 64 * RED_LD;
#pragma unroll
        for (int i = 0; i < 4; ++i)
#pragma unroll
            for (int j = 0; j < 5; ++j)
                *(f32x4*)(rw + (i * 16 + fr) * RED_LD + j * 16 + g * 4) = acc[i][j];
    }
    __syncthreads();
    {
        const int m = t >> 2, c0 = (t & 3) * 20;
        const float gg = ((const float*)(ws + WS_G))[mt * 64 + m];
        const float omg = rbf(1.f - gg);
        u16* dst = out + (size_t)(mt * 64 + m) * D_ + nt * 80 + c0;
#pragma unroll
        for (int c = 0; c < 20; c += 4) {
            f32x4 va = *(const f32x4*)(red + (0 * 64 + m) * RED_LD + c0 + c);
            f32x4 v1 = *(const f32x4*)(red + (1 * 64 + m) * RED_LD + c0 + c);
            f32x4 v2 = *(const f32x4*)(red + (2 * 64 + m) * RED_LD + c0 + c);
            f32x4 v3 = *(const f32x4*)(red + (3 * 64 + m) * RED_LD + c0 + c);
            float o[4];
#pragma unroll
            for (int e = 0; e < 4; ++e) {
                const float a = rbf(va[e]);
                const float b = rbf(v1[e] + v2[e] + v3[e]);
                o[e] = rbf(gg * a) + rbf(omg * b);
            }
            u32x2 pk = {pack_bf16(o[0], o[1]), pack_bf16(o[2], o[3])};
            *(u32x2*)(dst + c) = pk;
        }
    }
}

extern "C" void kernel_launch(void* const* d_in, const int* in_sizes, int n_in,
                              void* d_out, int out_size, void* d_ws, size_t ws_size,
                              hipStream_t stream) {
    const u16* X = (const u16*)d_in[0];
    const u16* Wqkv = (const u16*)d_in[1];
    const u16* Wo = (const u16*)d_in[2];
    const u16* Wg = (const u16*)d_in[4];
    const u16* Wu = (const u16*)d_in[5];
    const u16* Wd = (const u16*)d_in[6];
    const u16* gw = (const u16*)d_in[7];
    char* ws = (char*)d_ws;
    k1_gemm<<<256, 768, 0, stream>>>(X, Wqkv, Wg, Wu, Wo, Wd, gw, ws);
    k2_attn<<<512, 256, 0, stream>>>(ws);
    k3_gemm<<<256, 256, 0, stream>>>(ws, (u16*)d_out);
}
```

```cpp
#include <hip/hip_runtime.h>
#include <hip/hip_bf16.h>
#include <stdint.h>

typedef unsigned short u16;
typedef short bf16x8 __attribute__((ext_vector_type(8)));
typedef short s16x4 __attribute__((ext_vector_type(4)));
typedef float f32x4 __attribute__((ext_vector_type(4)));
typedef float f32x2 __attribute__((ext_vector_type(2)));
typedef unsigned u32x4 __attribute__((ext_vector_type(4)));
typedef unsigned u32x2 __attribute__((ext_vector_type(2)));
typedef __bf16 bf2_t __attribute__((ext_vector_type(2)));

#define LDS_AS __attribute__((address_space(3)))

constexpr int S_ = 2048, D_ = 640, F_ = 1728, H_ = 32, DH_ = 20, NQKV = 1920;
constexpr int KS_O = D_ / 32;
constexpr int KS_H = F_ / 32;

constexpr size_t WS_Q = 0;
constexpr size_t QKV_BYTES = (size_t)H_ * S_ * DH_ * 2;
constexpr size_t WS_K = WS_Q + QKV_BYTES;
constexpr size_t WS_V = WS_K + QKV_BYTES;
constexpr size_t WS_H = WS_V + QKV_BYTES + 4096;
constexpr size_t WS_O = WS_H + (size_t)S_ * F_ * 2;
constexpr size_t WS_WO = WS_O + (size_t)S_ * D_ * 2;
constexpr size_t WS_WD = WS_WO + (size_t)D_ * D_ * 2;
constexpr size_t WS_G = WS_WD + (size_t)F_ * D_ * 2;
constexpr size_t WS_FLAG = WS_G + (size_t)S_ * 4;

__device__ __forceinline__ unsigned pack_bf16(float a, float b) {
    bf2_t v = {(__bf16)a, (__bf16)b};
    return __builtin_bit_cast(unsigned, v);
}
__device__ __forceinline__ float rbf(float x) { return (float)(__bf16)x; }
__device__ __forceinline__ float bf2f(u16 v) { return __uint_as_float(((unsigned)v) << 16); }

template <int CTRL> __device__ __forceinline__ float dpp_add(float x) {
    return x + __builtin_bit_cast(float, __builtin_amdgcn_update_dpp(0, __builtin_bit_cast(int, x), CTRL, 0xf, 0xf, false));
}
__device__ __forceinline__ float wave_sum(float x) {
    x = dpp_add<0xB1>(x); x = dpp_add<0x4E>(x); x = dpp_add<0x141>(x); x = dpp_add<0x140>(x);
    const int xi = __builtin_bit_cast(int, x);
    return (__builtin_bit_cast(float, __builtin_amdgcn_readlane(xi, 0)) + __builtin_bit_cast(float, __builtin_amdgcn_readlane(xi, 16))) +
           (__builtin_bit_cast(float, __builtin_amdgcn_readlane(xi, 32)) + __builtin_bit_cast(float, __builtin_amdgcn_readlane(xi, 48)));
}
__device__ __forceinline__ s16x4 tr_read(const char* lds_ptr) {
    return __builtin_amdgcn_ds_read_tr16_b64_v4i16((LDS_AS s16x4*)(lds_ptr));
}
__device__ __forceinline__ bf16x8 cat4(s16x4 a, s16x4 b) {
    bf16x8 r;
    r[0] = a[0]; r[1] = a[1]; r[2] = a[2]; r[3] = a[3];
    r[4] = b[0]; r[5] = b[1]; r[6] = b[2]; r[7] = b[3];
    return r;
}
__device__ __forceinline__ void glds16(const void* g, void* l) {
    __builtin_amdgcn_global_load_lds((const unsigned*)g, (LDS_AS unsigned*)l, 16, 0, 0);
}
__device__ __forceinline__ void glds16_asm(const void* gsrc, unsigned lds_dst) {
    unsigned keep;
    asm volatile("s_mov_b32 %0, m0\n\ts_mov_b32 m0, %2\n\ts_nop 0\n\tglobal_load_lds_dwordx4 %1, off\n\ts_mov_b32 m0, %0"
                 : "=&s"(keep) : "v"(gsrc), "s"(lds_dst) : "memory");
}
__device__ __forceinline__ void glds4_asm(const void* gsrc, unsigned lds_dst) {
    unsigned keep;
    asm volatile("s_mov_b32 %0, m0\n\ts_mov_b32 m0, %2\n\ts_nop 0\n\tglobal_load_lds_dword %1, off\n\ts_mov_b32 m0, %0"
                 : "=&s"(keep) : "v"(gsrc), "s"(lds_dst) : "memory");
}
__device__ __forceinline__ unsigned lds_addr(const void* p) { return (unsigned)(size_t)(LDS_AS const char*)p; }
#define WAIT_VM(n) asm volatile("s_waitcnt vmcnt(%0)" :: "n"(n) : "memory")
#define CBAR() asm volatile("" ::: "memory")
#define STAMP(kid, i) do {} while (0)
#define STAMPK(kid, i) do { if ((i) == 3) STAMP(kid, i); } while (0)

#define K1UNR 1
constexpr int K1_ABYTES = 256 * 64 * 2;
constexpr int K1_BBYTES = 64 * 192 * 2;
constexpr int K1_BOFF = 3 * K1_ABYTES;
constexpr int K1_AUX = K1_BOFF + 2 * K1_BBYTES;
constexpr int K1_JUNK = K1_AUX + 3 * 4096;
constexpr int K1_LDS = K1_JUNK + 256;

template <int NJ>
__device__ __forceinline__ void k1_epilogue(f32x4 (&acc)[4][NJ], char* __restrict__ ws, char* smem, int m0, int c0, int nt) {
    int t = threadIdx.x;
    asm volatile("" : "+v"(t));
    const int lane = t & 63, wid = t >> 6;
    const int wr = wid >> 1, wc = wid & 1;
    const int fr = lane & 15, g = lane >> 4;
    if (NJ == 5) {
        __syncthreads();
#pragma unroll
        for (int j = 0; j < NJ; ++j) {
            const int n = wc * 80 + j * 16 + g * 4;
            const int hh = n / DH_, d = n % DH_;
            char* dst = smem + hh * 10240 + (wr * 64 + fr) * 40 + d * 2;
#pragma unroll
            for (int i = 0; i < 4; ++i) {
                u32x2 a = {pack_bf16(acc[i][j][0], acc[i][j][1]), pack_bf16(acc[i][j][2], acc[i][j][3])};
                *(u32x2*)(dst + i * 640) = a;
            }
        }
        __syncthreads();
        const int which = nt >> 2;
        const int h0 = (nt & 3) * 8;
        char* dbase = ws + WS_Q + (size_t)which * QKV_BYTES + ((size_t)h0 * S_ + m0) * 40;
#pragma unroll
        for (int it = 0; it < 10; ++it) {
            const int e = it * 512 + t;
            const int hh = e / 640, r = e % 640;
            const u32x4 v = *(const u32x4*)(smem + e * 16);
            *(u32x4*)(dbase + (size_t)hh * (S_ * 40) + r * 16) = v;
        }
    } else {
        u16* hF = (u16*)(ws + WS_H);
#pragma unroll
        for (int j = 0; j < 3; ++j) {
            const int f = c0 + wc * 48 + j * 16 + g * 4;
            const int ks = f >> 5;
            const int lq = (f & 31) >> 3;
            const int hf = f & 4;
#pragma unroll
            for (int i = 0; i < 4; ++i) {
                const int m = m0 + wr * 64 + i * 16 + fr;
                unsigned pk[2];
#pragma unroll
                for (int r = 0; r < 2; ++r) {
                    const f32x2 gt = {acc[i][j][2 * r], acc[i][j][2 * r + 1]};
                    const f32x2 up = {acc[i][(j + 3) % NJ][2 * r], acc[i][(j + 3) % NJ][2 * r + 1]};
                    f32x2 e = gt * -1.4426950408889634f;
                    e = (f32x2){__builtin_amdgcn_exp2f(e[0]), __builtin_amdgcn_exp2f(e[1])};
                    e = e + 1.f;
                    const f32x2 rc = {__builtin_amdgcn_rcpf(e[0]), __builtin_amdgcn_rcpf(e[1])};
                    const f32x2 hv = (gt * up) * rc;
                    pk[r] = pack_bf16(hv[0], hv[1]);
                }
                u32x2 a = {pk[0], pk[1]};
                u16* dst = hF + ((size_t)((m >> 4) * KS_H + ks) * 64 + (m & 15) + 16 * lq) * 8 + hf;
                *(u32x2*)dst = a;
            }
        }
    }
}

template <int NJ>
__device__ __forceinline__ void k1_tile(const u16* __restrict__ X, const u16* __restrict__ W0, const u16* __restrict__ W1,
                                        char* __restrict__ ws, char* smem, int mt, int nt, int xs, int xcnt) {
    constexpr int BN = 32 * NJ;
    constexpr int ROWB = BN * 2;
    constexpr int UPR = BN / 8;
    constexpr int LDB = (NJ == 5) ? NQKV : F_;
    const int t = threadIdx.x;
    const int lane = t & 63, wid = t >> 6;
    const int m0 = mt * 256;
    const int c0 = (NJ == 5) ? nt * 160 : nt * 96;
    const int uw = __builtin_amdgcn_readfirstlane(wid);
    const unsigned sb0 = lds_addr(smem);

    if (uw == 10) {
        const u16* pp[3]; unsigned ps[3];
#pragma unroll
        for (int i = 0; i < 3; ++i) {
            constexpr int SPR = BN / 32;
            constexpr int NWS = 16 * SPR;
            const int xr = (256 + xcnt - 1) / xcnt;
            int e = i * 64 + lane;
            if (e < NWS) {
                const int kr = (mt & 3) * 16 + e / SPR, u = e % SPR;
                if (NJ == 5) pp[i] = W0 + (size_t)kr * LDB + c0 + u * 32;
                else pp[i] = ((u < 3) ? W0 : W1) + (size_t)kr * LDB + c0 + (u % 3) * 32;
                ps[i] = 64 * LDB;
            } else {
                e -= NWS;
                int r = xs * xr + (e >> 1);
                if ((e >> 1) >= xr || r > 255) { r = xs * xr; }
                pp[i] = X + (size_t)(m0 + r) * D_ + (e & 1) * 32;
                ps[i] = 64;
            }
        }
        const unsigned junk = sb0 + K1_JUNK;
        auto pf = [&](int kt) {
#pragma unroll
            for (int i = 0; i < 3; ++i) glds4_asm(pp[i] + (size_t)kt * ps[i], junk);
        };
        pf(0); pf(1); pf(2);
        WAIT_VM(6);
        __builtin_amdgcn_s_barrier();
        pf(3);
        for (int kt = 0; kt < 10; ++kt) {
            __builtin_amdgcn_s_barrier();
            if (kt + 4 < 10) pf(kt + 4);
        }
        WAIT_VM(0);
        return;
    }
    if (uw >= 8) {
        const int dw = uw - 8;
        constexpr int NBI = 2 * NJ;
        const u16* pa = X + (size_t)(m0 + dw * 128 + (lane >> 3)) * D_ + (((lane & 7) ^ (lane >> 3)) << 3);
        const u16* pb[NBI];
#pragma unroll
        for (int i = 0; i < NBI; ++i) {
            const int p = (dw * NBI + i) * 64 + lane;
            const int k = p / UPR, u = p % UPR;
            const int sw = (NJ == 5) ? ((k >> 3) & 1) : (((k >> 1) & 1) | (((k >> 3) & 1) << 1));
            const int c32 = (u >> 1) ^ sw, half = u & 1;
            if (NJ == 5) pb[i] = W0 + (size_t)k * LDB + c0 + c32 * 16 + half * 8;
            else pb[i] = ((c32 < 6) ? W0 : W1) + (size_t)k * LDB + c0 + (c32 < 6 ? c32 : c32 - 6) * 16 + half * 8;
        }
        auto loadA = [&](int kt) {
            const unsigned base = sb0 + (kt % 3) * K1_ABYTES + dw * 16384;
#pragma unroll
            for (int i = 0; i < 16; ++i) glds16_asm(pa + (size_t)i * 8 * D_ + kt * 64, base + i * 1024);
        };
        auto loadB = [&](int kt) {
            const unsigned base = sb0 + K1_BOFF + (kt & 1) * K1_BBYTES + dw * NBI * 1024;
#pragma unroll
            for (int i = 0; i < NBI; ++i) glds16_asm(pb[i] + (size_t)kt * 64 * LDB, base + i * 1024);
        };
        STAMP(0, 0);
        __builtin_amdgcn_s_barrier();
        loadB(0); loadB(1); loadA(1);
        WAIT_VM(16 + NBI);
        for (int kt = 0; kt < 10; ++kt) {
            STAMPK(0, 2 + kt * 3);
            __builtin_amdgcn_s_barrier();
            STAMPK(0, 3 + kt * 3);
            if (kt > 0 && kt + 1 < 10) loadB(kt + 1);
            if (kt + 2 < 10) { loadA(kt + 2); STAMPK(0, 4 + kt * 3); WAIT_VM(16); } else { STAMPK(0, 4 + kt * 3); WAIT_VM(0); }
        }
        STAMP(0, 40); STAMP(0, 41); STAMP(0, 42);
        return;
    }

    STAMP(0, 0);
    __builtin_amdgcn_s_barrier();
    {
        const u16* pa = X + (size_t)(m0 + uw * 32 + (lane >> 3)) * D_ + (((lane & 7) ^ (lane >> 3)) << 3);
#pragma unroll
        for (int i = 0; i < 4; ++i) glds16_asm(pa + (size_t)i * 8 * D_, sb0 + uw * 4096 + i * 1024);
    }
    const int wr = wid >> 1, wc = wid & 1;
    const int fr = lane & 15, g = lane >> 4;
    f32x4 acc[4][NJ];
#pragma unroll
    for (int i = 0; i < 4; ++i)
#pragma unroll
        for (int j = 0; j < NJ; ++j) acc[i][j] = (f32x4){0.f, 0.f, 0.f, 0.f};

    int aoff[2];
#pragma unroll
    for (int ks = 0; ks < 2; ++ks) {
        const int row = wr * 64 + fr;
        aoff[ks] = row * 128 + (((ks * 4 + g) ^ (row & 7)) << 4);
    }
    const int q4 = (lane & 15) >> 2, p4 = lane & 3;
    const int swl = (NJ == 5) ? (g & 1) : (((q4 >> 1) & 1) | ((g & 1) << 1));
    int boff[NJ];
#pragma unroll
    for (int j = 0; j < NJ; ++j) {
        const int c = (NJ == 5) ? (5 * wc + j) : (j < 3 ? 3 * wc + j : 6 + 3 * wc + (j - 3));
        boff[j] = K1_BOFF + (8 * g + q4) * ROWB + ((c ^ swl) << 5) + p4 * 8;
    }
    WAIT_VM(0);
    __builtin_amdgcn_s_barrier();
    CBAR();
    STAMP(0, 3);
    bf16x8 a0[4], a1[4], bq[NJ];
    auto rdB = [&](const char* baseB, int ks, int j) {
        s16x4 lo = tr_read(baseB + boff[j] + ks * (32 * ROWB));
        s16x4 hi = tr_read(baseB + boff[j] + ks * (32 * ROWB) + 4 * ROWB);
        return cat4(lo, hi);
    };
#pragma unroll
    for (int i = 0; i < 4; ++i) a0[i] = *(const bf16x8*)(smem + aoff[0] + i * 2048);
    __builtin_amdgcn_sched_barrier(0);
#pragma unroll
    for (int j = 0; j < NJ; ++j) { bq[j] = rdB(smem, 0, j); __builtin_amdgcn_sched_barrier(0); }
    int abuf = 0;
#pragma unroll 1
    for (int kt = 0; kt < 10; ++kt) {
        const char* baseA = smem + abuf * K1_ABYTES;
        const char* baseB = smem + (kt & 1) * K1_BBYTES;
        abuf = (abuf == 2) ? 0 : abuf + 1;
        const char* nextA = smem + abuf * K1_ABYTES;
        const char* nextB = smem + ((kt + 1) & 1) * K1_BBYTES;
#pragma unroll
        for (int i = 0; i < 4; ++i) a1[i] = *(const bf16x8*)(baseA + aoff[1] + i * 2048);
#pragma unroll
        for (int j = 0; j < NJ; ++j) {
#pragma unroll
            for (int i = 0; i < 4; ++i)
                acc[i][j] = __builtin_amdgcn_mfma_f32_16x16x32_bf16(bq[j], a0[i], acc[i][j], 0, 0, 0);
            bq[j] = rdB(baseB, 1, j);
            __builtin_amdgcn_sched_barrier(0);
        }
        asm volatile("s_waitcnt lgkmcnt(0)" ::: "memory");
        if (kt < 9) __builtin_amdgcn_s_barrier();
        CBAR();
#pragma unroll
        for (int i = 0; i < 4; ++i) a0[i] = *(const bf16x8*)(nextA + aoff[0] + i * 2048);
#pragma unroll
        for (int j = 0; j < NJ; ++j) {
#pragma unroll
            for (int i = 0; i < 4; ++i)
                acc[i][j] = __builtin_amdgcn_mfma_f32_16x16x32_bf16(bq[j], a1[i], acc[i][j], 0, 0, 0);
            bq[j] = rdB(nextB, 0, j);
            __builtin_amdgcn_sched_barrier(0);
        }
    }

    STAMP(0, 40);
    k1_epilogue<NJ>(acc, ws, smem, m0, c0, nt);
    STAMP(0, 41);
    STAMP(0, 42);
}

__device__ __forceinline__ void k1_aux(const u16* __restrict__ X, const u16* __restrict__ Wo, const u16* __restrict__ Wd,
                                       const u16* __restrict__ gate_w, char* __restrict__ ws, char* smem, int lin) {
    const int lane = threadIdx.x & 63;
    float* gout = (float*)(ws + WS_G);
    u16* woF = (u16*)(ws + WS_WO);
    u16* wdF = (u16*)(ws + WS_WD);
    const int q4 = (lane & 15) >> 2, p4 = lane & 3, g = lane >> 4;
    char* wl = smem + K1_AUX;
    const unsigned wla = lds_addr(wl);
    auto issue = [&](int unit, int slot) {
        const int ksg = unit / 10, cc = unit % 10;
        const u16* src = (ksg < KS_O) ? (Wo + (size_t)ksg * 32 * D_) : (Wd + (size_t)(ksg - KS_O) * 32 * D_);
#pragma unroll
        for (int i = 0; i < 4; ++i)
            glds16_asm(src + (size_t)(i * 8 + (lane >> 3)) * D_ + cc * 64 + (lane & 7) * 8, wla + slot * 4096 + i * 1024);
    };
    auto emit = [&](int unit, int slot) {
        const int ksg = unit / 10, cc = unit % 10;
#pragma unroll
        for (int j = 0; j < 4; ++j) {
            const char* a = wl + slot * 4096 + (8 * g + q4) * 128 + j * 32 + p4 * 8;
            s16x4 lo = tr_read(a);
            s16x4 hi = tr_read(a + 4 * 128);
            bf16x8 v = cat4(lo, hi);
            const int nt = cc * 4 + j;
            u16* dst = (ksg < KS_O) ? (woF + ((size_t)(nt * KS_O + ksg) * 64 + lane) * 8)
                                    : (wdF + ((size_t)(nt * KS_H + (ksg - KS_O)) * 64 + lane) * 8);
            *(bf16x8*)dst = v;
        }
    };
    __builtin_amdgcn_s_barrier();
    __builtin_amdgcn_s_barrier();
    issue(lin, 0); issue(lin + 240, 1); issue(lin + 480, 2);
    __builtin_amdgcn_s_barrier();
    __builtin_amdgcn_s_barrier();
    WAIT_VM(0);
    emit(lin, 0); emit(lin + 240, 1); emit(lin + 480, 2);
    asm volatile("s_waitcnt lgkmcnt(0)" ::: "memory");
    const bool extra = lin + 720 < 740;
    if (extra) issue(lin + 720, 0);
    __builtin_amdgcn_s_barrier();
    const u32x4 w0 = *(const u32x4*)(gate_w + lane * 8);
    u32x4 w1 = *(const u32x4*)(gate_w + 512 + (lane & 15) * 8);
    u32x4 x0[9], x1[9];
#pragma unroll
    for (int r = 0; r < 9; ++r) {
        int row = lin + 240 * r;
        row = row < S_ ? row : lin;
        x0[r] = *(const u32x4*)(X + (size_t)row * D_ + lane * 8);
        x1[r] = *(const u32x4*)(X + (size_t)row * D_ + 512 + (lane & 15) * 8);
    }
    CBAR();
    __builtin_amdgcn_s_barrier();
    __builtin_amdgcn_s_barrier();
    CBAR();
    if (lane >= 16) w1 = (u32x4){0u, 0u, 0u, 0u};
    float sacc[9];
#pragma unroll
    for (int r = 0; r < 9; ++r) {
        float a = 0.f;
#pragma unroll
        for (int e = 0; e < 4; ++e) {
            const unsigned xa = x0[r][e], wa = w0[e], xb = x1[r][e], wb = w1[e];
            a = __builtin_amdgcn_fdot2_f32_bf16(__builtin_bit_cast(bf2_t, xa), __builtin_bit_cast(bf2_t, wa), a, false);
            a = __builtin_amdgcn_fdot2_f32_bf16(__builtin_bit_cast(bf2_t, xb), __builtin_bit_cast(bf2_t, wb), a, false);
        }
        sacc[r] = a;
    }
    __builtin_amdgcn_s_barrier();
    {
        float z = 0.f;
#pragma unroll
        for (int r = 0; r < 9; ++r) {
            const float tot = wave_sum(sacc[r]);
            z = (lane == r) ? tot : z;
        }
        const int row = lin + 240 * lane;
        z = rbf(z);
        if (lane < 9 && row < S_) gout[row] = rbf(1.f / (1.f + __expf(-z)));
    }
    __builtin_amdgcn_s_barrier();
    WAIT_VM(0);
    if (extra) emit(lin + 720, 0);
    __builtin_amdgcn_s_barrier();
    __builtin_amdgcn_s_barrier();
}

__global__ __launch_bounds__(768) void k1_gemm(const u16* __restrict__ X, const u16* __restrict__ Wqkv,
                                               const u16* __restrict__ Wg, const u16* __restrict__ Wu,
                                               const u16* __restrict__ Wo, const u16* __restrict__ Wd,
                                               const u16* __restrict__ gate_w, char* __restrict__ ws) {
    __shared__ __attribute__((aligned(16))) char smem[K1_LDS];
    const int bid = blockIdx.x;
    const int xcd = bid & 7, idx = bid >> 3;
    const int ng = xcd >> 1, mg = xcd & 1;
    const int cnt = (ng < 2) ? 8 : 7;
    const int nstart = (ng < 2) ? ng * 8 : 16 + (ng - 2) * 7;
    if (idx >= 4 * cnt) {
        if (threadIdx.x < 32)
            *(u32x4*)(ws + WS_FLAG + (size_t)((xcd - 4) * 4 + (idx - 28)) * 512 + threadIdx.x * 16) = (u32x4){0u, 0u, 0u, 0u};
        return;
    }
    if (__builtin_amdgcn_readfirstlane(threadIdx.x >> 6) == 11) {
        const int lin = (ng < 2) ? xcd * 32 + idx : 128 + (xcd - 4) * 28 + idx;
        k1_aux(X, Wo, Wd, gate_w, ws, smem, lin);
        return;
    }
    const int mt = mg * 4 + (idx & 3);
    const int nt = nstart + (idx >> 2);
    if (nt < 12) k1_tile<5>(X, Wqkv, Wqkv, ws, smem, mt, nt, idx >> 2, cnt);
    else k1_tile<6>(X, Wg, Wu, ws, smem, mt, nt - 12, idx >> 2, cnt);
}

typedef unsigned long long u64;
typedef __amdgpu_buffer_rsrc_t rsrc_t;
constexpr int AUX_AGENT = 16;
__device__ __forceinline__ void st_agent(void* p, u32x2 v) {
    __hip_atomic_store((u64*)p, __builtin_bit_cast(u64, v), __ATOMIC_RELAXED, __HIP_MEMORY_SCOPE_AGENT);
}
template <int AUX> __device__ __forceinline__ bf16x8 bld(rsrc_t r, int voff, int soff) {
    return __builtin_bit_cast(bf16x8, __builtin_amdgcn_raw_buffer_load_b128(r, voff, soff, AUX));
}

template <bool EDGE>
__device__ __forceinline__ void k2_attn_pass(char* __restrict__ oF, const char* sK, const char* sV, bf16x8 qq, float gate,
                                             int h, int qs, int rbase, int lane) {
    const int fr = lane & 15, g = lane >> 4;
    const int q4 = (lane & 15) >> 2, p4 = lane & 3;
    const float NEG = -__builtin_inff();
    constexpr float CSC = 0.22360679774997896f * 1.4426950408889634f;
    const bf16x8 ones = {0x3f80, 0x3f80, 0x3f80, 0x3f80, 0x3f80, 0x3f80, 0x3f80, 0x3f80};
    const int tstart = 16 - (qs >> 4);
    const char* kb = sK + (rbase + fr) * 40 + (g < 3 ? g : 2) * 16;
    const char* vb = sV + (rbase + 4 * g + q4) * 40 + p4 * 8;
    f32x4 sc[17];
    float mx = NEG;
#pragma unroll
    for (int tt = 0; tt < 17; ++tt) {
        const u32x2 klo = *(const u32x2*)(kb + tt * 640), khi = *(const u32x2*)(kb + tt * 640 + 8);
        const u32x4 kv = {klo[0], klo[1], khi[0], khi[1]};
        const f32x4 z = {0.f, 0.f, 0.f, 0.f};
        f32x4 s = __builtin_amdgcn_mfma_f32_16x16x32_bf16(__builtin_bit_cast(bf16x8, kv), qq, z, 0, 0, 0);
        if (tt == 0) {
#pragma unroll
            for (int r = 0; r < 4; ++r) if (fr >= 4 * g + r) s[r] = NEG;
        }
        if (tt == 16) {
#pragma unroll
            for (int r = 0; r < 4; ++r) if (4 * g + r > fr) s[r] = NEG;
        }
        if (EDGE && tt < 16) {
#pragma unroll
            for (int r = 0; r < 4; ++r) s[r] = (tt < tstart) ? NEG : s[r];
        }
        mx = fmaxf(mx, fmaxf(fmaxf(s[0], s[1]), fmaxf(s[2], s[3])));
        sc[tt] = s;
    }
    mx = fmaxf(mx, __shfl_xor(mx, 16));
    mx = fmaxf(mx, __shfl_xor(mx, 32));
    const float mc = -mx * CSC;
    f32x4 oacc[3];
#pragma unroll
    for (int dt = 0; dt < 3; ++dt) oacc[dt] = (f32x4){0.f, 0.f, 0.f, 0.f};
#pragma unroll
    for (int pp = 0; pp < 9; ++pp) {
        bf16x8 vf[2];
#pragma unroll
        for (int dt = 0; dt < 2; ++dt) {
            s16x4 lo = tr_read(vb + (2 * pp) * 640 + dt * 32);
            s16x4 hi = (pp < 8) ? tr_read(vb + (2 * pp + 1) * 640 + dt * 32) : lo;
            vf[dt] = cat4(lo, hi);
        }
        unsigned pk[4];
#pragma unroll
        for (int hh = 0; hh < 2; ++hh) {
            if (2 * pp + hh < 17) {
                const f32x4 s = sc[(2 * pp + hh) < 17 ? (2 * pp + hh) : 16];
                f32x2 e0 = {s[0], s[1]}, e1 = {s[2], s[3]};
                e0 = __builtin_elementwise_fma(e0, (f32x2){CSC, CSC}, (f32x2){mc, mc});
                e1 = __builtin_elementwise_fma(e1, (f32x2){CSC, CSC}, (f32x2){mc, mc});
                pk[2 * hh] = pack_bf16(__builtin_amdgcn_exp2f(e0[0]), __builtin_amdgcn_exp2f(e0[1]));
                pk[2 * hh + 1] = pack_bf16(__builtin_amdgcn_exp2f(e1[0]), __builtin_amdgcn_exp2f(e1[1]));
            } else {
                pk[2 * hh] = 0u; pk[2 * hh + 1] = 0u;
            }
        }
        const u32x4 pv = {pk[0], pk[1], pk[2], pk[3]};
        const bf16x8 pf = __builtin_bit_cast(bf16x8, pv);
#pragma unroll
        for (int dt = 0; dt < 2; ++dt)
            oacc[dt] = __builtin_amdgcn_mfma_f32_16x16x32_bf16(vf[dt], pf, oacc[dt], 0, 0, 0);
        oacc[2] = __builtin_amdgcn_mfma_f32_16x16x32_bf16(ones, pf, oacc[2], 0, 0, 0);
    }
    const float inv = gate / oacc[2][0];
    const int m = qs + fr;
    {
        const int k0 = h * DH_ + 4 * g;
        u32x2 a = {pack_bf16(oacc[0][0] * inv, oacc[0][1] * inv), pack_bf16(oacc[0][2] * inv, oacc[0][3] * inv)};
        st_agent(oF + ((size_t)((m >> 4) * KS_O + (k0 >> 5)) * 64 + (m & 15) + 16 * ((k0 & 31) >> 3)) * 16 + (k0 & 4) * 2, a);
    }
    if (g == 0) {
        const int k0 = h * DH_ + 16;
        u32x2 a = {pack_bf16(oacc[1][0] * inv, oacc[1][1] * inv), pack_bf16(oacc[1][2] * inv, oacc[1][3] * inv)};
        st_agent(oF + ((size_t)((m >> 4) * KS_O + (k0 >> 5)) * 64 + (m & 15) + 16 * ((k0 & 31) >> 3)) * 16 + (k0 & 4) * 2, a);
    }
}

__device__ __forceinline__ void k2_attn(char* __restrict__ ws, char* smem, int bid) {
    const int h = (bid & 7) + 8 * ((bid >> 3) & 3);
    const int qb = bid >> 5;
    const int q0 = qb * 128;
    const int t = threadIdx.x, lane = t & 63;
    const int w = __builtin_amdgcn_readfirstlane(t >> 6);
    const char* Qb = ws + WS_Q + (size_t)h * S_ * 40;
    const char* Kb = ws + WS_K + (size_t)h * S_ * 40;
    const char* Vb = ws + WS_V + (size_t)h * S_ * 40;
    char* sK = smem;
    char* sV = smem + 15360;

#pragma unroll
    for (int i = 0; i < 4; ++i) {
        const int p = i * 256 + t;
        if (i < 3 || w < 3) {
            int off = (q0 - 256) * 40 + p * 16;
            off = off < 0 ? 0 : off;
            glds16(Kb + off, sK + p * 16);
            glds16(Vb + off, sV + p * 16);
        }
    }
    if (t < 4) *(u32x4*)(smem + 2 * 15360 + t * 16) = (u32x4){0u, 0u, 0u, 0u};
    const int fr = lane & 15, g = lane >> 4;
    const int qw = q0 + 32 * w;
    bf16x8 qf[2];
    float gg[2];
#pragma unroll
    for (int qt = 0; qt < 2; ++qt) {
        const char* qp = Qb + (size_t)(qw + qt * 16 + fr) * 40 + (g < 3 ? g : 2) * 16;
        u32x2 lo = *(const u32x2*)qp;
        u32x2 hi = *(const u32x2*)(qp + (g < 2 ? 8 : 0));
        if (g >= 2) hi = (u32x2){0u, 0u};
        if (g == 3) lo = (u32x2){0u, 0u};
        u32x4 v = {lo[0], lo[1], hi[0], hi[1]};
        qf[qt] = __builtin_bit_cast(bf16x8, v);
        gg[qt] = ((const float*)(ws + WS_G))[qw + qt * 16 + fr];
    }
    __syncthreads();
    char* oF = ws + WS_O;
    if (qb >= 2) {
#pragma unroll 1
        for (int qt = 0; qt < 2; ++qt)
            k2_attn_pass<false>(oF, sK, sV, qt ? qf[1] : qf[0], qt ? gg[1] : gg[0], h, qw + 16 * qt, 32 * w + 16 * qt, lane);
    } else {
#pragma unroll 1
        for (int qt = 0; qt < 2; ++qt)
            k2_attn_pass<true>(oF, sK, sV, qt ? qf[1] : qf[0], qt ? gg[1] : gg[0], h, qw + 16 * qt, 32 * w + 16 * qt, lane);
    }
    asm volatile("s_waitcnt vmcnt(0)" ::: "memory");
    if (lane == 0)
        __hip_atomic_store((unsigned*)(ws + WS_FLAG) + (qw >> 6) * 64 + h * 2 + ((qw >> 5) & 1), 1u, __ATOMIC_RELAXED, __HIP_MEMORY_SCOPE_AGENT);
}

constexpr int RED_LD = 84;
__device__ __forceinline__ void k2_gemm(char* __restrict__ ws, u16* __restrict__ out, char* smem, int bid) {
    float* red = (float*)smem;
    const int xcd = bid & 7, idx = bid >> 3;
    const int mt = (xcd >> 1) * 8 + (idx & 7);
    const int nt = (xcd & 1) * 4 + (idx >> 3);
    const int t = threadIdx.x, lane = t & 63;
    const int w = __builtin_amdgcn_readfirstlane(t >> 6);
    const int fr = lane & 15, g = lane >> 4;
    const int nsteps = (w < 3) ? 14 : 12;
    const rsrc_t rs = __builtin_amdgcn_make_buffer_rsrc(ws, 0, (int)WS_FLAG, 0x00020000);
    const int vo = lane * 16;
    int cur = (((idx & 7) + 2 * (idx >> 3)) & 7) * nsteps >> 3;
    const int ap = (int)WS_H + ((mt * 4) * KS_H + 14 * w) * 1024;
    const int bp = (int)WS_WD + ((nt * 5) * KS_H + 14 * w) * 1024;

    f32x4 acc[4][5];
#pragma unroll
    for (int i = 0; i < 4; ++i)
#pragma unroll
        for (int j = 0; j < 5; ++j) acc[i][j] = (f32x4){0.f, 0.f, 0.f, 0.f};
    bf16x8 a0[4], b0[5], a1[4], b1[5];
    auto ld = [&](bf16x8 (&a)[4], bf16x8 (&b)[5]) {
#pragma unroll
        for (int i = 0; i < 4; ++i) a[i] = bld<0>(rs, vo, ap + (i * KS_H + cur) * 1024);
#pragma unroll
        for (int j = 0; j < 5; ++j) b[j] = bld<0>(rs, vo, bp + (j * KS_H + cur) * 1024);
        cur = (cur + 1 == nsteps) ? 0 : cur + 1;
    };
    auto mm = [&](bf16x8 (&a)[4], bf16x8 (&b)[5]) {
#pragma unroll
        for (int i = 0; i < 4; ++i)
#pragma unroll
            for (int j = 0; j < 5; ++j)
                acc[i][j] = __builtin_amdgcn_mfma_f32_16x16x32_bf16(b[j], a[i], acc[i][j], 0, 0, 0);
    };
    ld(a0, b0);
    ld(a1, b1);
    float omg[4];
#pragma unroll
    for (int i = 0; i < 4; ++i) omg[i] = ((const float*)(ws + WS_G))[mt * 64 + i * 16 + fr];
#pragma unroll 1
    for (int s = 0; s < nsteps - 2; s += 2) {
        mm(a0, b0); ld(a0, b0);
        mm(a1, b1); ld(a1, b1);
    }
    const int op = (int)WS_O + ((mt * 4) * KS_O + 5 * w) * 1024;
    const int wp = (int)WS_WO + ((nt * 5) * KS_O + 5 * w) * 1024;
    auto ldw = [&](bf16x8 (&b)[5], int s) {
#pragma unroll
        for (int j = 0; j < 5; ++j) b[j] = bld<0>(rs, vo, wp + (j * KS_O + s) * 1024);
    };
    auto ldo = [&](bf16x8 (&a)[4], int s) {
#pragma unroll
        for (int i = 0; i < 4; ++i) a[i] = bld<AUX_AGENT>(rs, vo, op + (i * KS_O + s) * 1024);
    };
#define SB() __builtin_amdgcn_sched_barrier(0)
    SB(); mm(a0, b0); SB(); ldw(b0, 0);
    SB(); mm(a1, b1); SB(); ldw(b1, 1);
    SB();
#pragma unroll
    for (int i = 0; i < 4; ++i) {
        const float sc = rbf(1.f - omg[i]);
#pragma unroll
        for (int j = 0; j < 5; ++j) acc[i][j] = acc[i][j] * sc;
        asm volatile("" : "+v"(acc[i][0]), "+v"(acc[i][1]), "+v"(acc[i][2]), "+v"(acc[i][3]), "+v"(acc[i][4]));
    }
    {
        const unsigned* fp = (const unsigned*)(ws + WS_FLAG) + mt * 64 + lane;
        while (true) {
            const unsigned f = __hip_atomic_load(fp, __ATOMIC_RELAXED, __HIP_MEMORY_SCOPE_AGENT);
            if (__builtin_amdgcn_ballot_w64(f != 1u) == 0ull) break;
            __builtin_amdgcn_s_sleep(4);
        }
        asm volatile("" ::: "memory");
    }
    ldo(a0, 0);
    ldo(a1, 1);
    SB(); mm(a0, b0); SB(); ldo(a0, 2); ldw(b0, 2);
    SB(); mm(a1, b1); SB(); ldo(a1, 3); ldw(b1, 3);
    SB(); mm(a0, b0); SB(); ldo(a0, 4); ldw(b0, 4);
    SB(); mm(a1, b1);
    SB(); mm(a0, b0);
    SB();
    float* rw = red + (w & 1) * 64 * RED_LD + fr * RED_LD + g * 4;
    if (w >= 2) {
#pragma unroll
        for (int i = 0; i < 4; ++i)
#pragma unroll
            for (int j = 0; j < 5; ++j) *(f32x4*)(rw + i * 16 * RED_LD + j * 16) = acc[i][j];
    }
    __syncthreads();
    if (w < 2) {
#pragma unroll
        for (int i = 0; i < 4; ++i)
#pragma unroll
            for (int j = 0; j < 5; ++j) {
                float* q = rw + i * 16 * RED_LD + j * 16;
                *(f32x4*)q = acc[i][j] + *(const f32x4*)q;
            }
    }
    __syncthreads();
    {
        const int m = t >> 2, c0 = (t & 3) * 20;
        u16* dst = out + (size_t)(mt * 64 + m) * D_ + nt * 80 + c0;
#pragma unroll
        for (int c = 0; c < 20; c += 4) {
            const f32x4 v = *(const f32x4*)(red + m * RED_LD + c0 + c) + *(const f32x4*)(red + (64 + m) * RED_LD + c0 + c);
            u32x2 pk = {pack_bf16(v[0], v[1]), pack_bf16(v[2], v[3])};
            *(u32x2*)(dst + c) = pk;
        }
    }
}

__global__ __launch_bounds__(256, 3) void k2_mix(char* __restrict__ ws, u16* __restrict__ out) {
    __shared__ __attribute__((aligned(16))) char smem[2 * 64 * RED_LD * 4];
    if (blockIdx.x < 256) k2_gemm(ws, out, smem, blockIdx.x);
    else k2_attn(ws, smem, blockIdx.x - 256);
}

extern "C" void kernel_launch(void* const* d_in, const int* in_sizes, int n_in,
                              void* d_out, int out_size, void* d_ws, size_t ws_size,
                              hipStream_t stream) {
    const u16* X = (const u16*)d_in[0];
    const u16* Wqkv = (const u16*)d_in[1];
    const u16* Wo = (const u16*)d_in[2];
    const u16* Wg = (const u16*)d_in[4];
    const u16* Wu = (const u16*)d_in[5];
    const u16* Wd = (const u16*)d_in[6];
    const u16* gw = (const u16*)d_in[7];
    char* ws = (char*)d_ws;
    k1_gemm<<<256, 768, 0, stream>>>(X, Wqkv, Wg, Wu, Wo, Wd, gw, ws);
    k2_mix<<<768, 256, 0, stream>>>(ws, (u16*)d_out);
}
```

```cpp
#include <hip/hip_runtime.h>
#include <hip/hip_bf16.h>
#include <stdint.h>

typedef unsigned short u16;
typedef short bf16x8 __attribute__((ext_vector_type(8)));
typedef short s16x4 __attribute__((ext_vector_type(4)));
typedef float f32x4 __attribute__((ext_vector_type(4)));
typedef float f32x2 __attribute__((ext_vector_type(2)));
typedef unsigned u32x4 __attribute__((ext_vector_type(4)));
typedef unsigned u32x2 __attribute__((ext_vector_type(2)));
typedef __bf16 bf2_t __attribute__((ext_vector_type(2)));

#define LDS_AS __attribute__((address_space(3)))

constexpr int S_ = 2048, D_ = 640, F_ = 1728, H_ = 32, DH_ = 20, NQKV = 1920;
constexpr int KS_O = D_ / 32;
constexpr int KS_H = F_ / 32;

constexpr size_t WS_Q = 0;
constexpr size_t QKV_BYTES = (size_t)H_ * S_ * DH_ * 2;
constexpr size_t WS_K = WS_Q + QKV_BYTES;
constexpr size_t WS_V = WS_K + QKV_BYTES;
constexpr size_t WS_H = WS_V + QKV_BYTES + 4096;
constexpr size_t WS_O = WS_H + (size_t)S_ * F_ * 2;
constexpr size_t WS_WO = WS_O + (size_t)S_ * D_ * 2;
constexpr size_t WS_WD = WS_WO + (size_t)D_ * D_ * 2;
constexpr size_t WS_G = WS_WD + (size_t)F_ * D_ * 2;
constexpr size_t WS_FLAG = WS_G + (size_t)S_ * 4;
constexpr int FLAG_LD = 4352;

__device__ __forceinline__ unsigned pack_bf16(float a, float b) {
    bf2_t v = {(__bf16)a, (__bf16)b};
    return __builtin_bit_cast(unsigned, v);
}
__device__ __forceinline__ float rbf(float x) { return (float)(__bf16)x; }
__device__ __forceinline__ float bf2f(u16 v) { return __uint_as_float(((unsigned)v) << 16); }

template <int CTRL> __device__ __forceinline__ float dpp_add(float x) {
    return x + __builtin_bit_cast(float, __builtin_amdgcn_update_dpp(0, __builtin_bit_cast(int, x), CTRL, 0xf, 0xf, false));
}
__device__ __forceinline__ float wave_sum(float x) {
    x = dpp_add<0xB1>(x); x = dpp_add<0x4E>(x); x = dpp_add<0x141>(x); x = dpp_add<0x140>(x);
    const int xi = __builtin_bit_cast(int, x);
    return (__builtin_bit_cast(float, __builtin_amdgcn_readlane(xi, 0)) + __builtin_bit_cast(float, __builtin_amdgcn_readlane(xi, 16))) +
           (__builtin_bit_cast(float, __builtin_amdgcn_readlane(xi, 32)) + __builtin_bit_cast(float, __builtin_amdgcn_readlane(xi, 48)));
}
__device__ __forceinline__ s16x4 tr_read(const char* lds_ptr) {
    return __builtin_amdgcn_ds_read_tr16_b64_v4i16((LDS_AS s16x4*)(lds_ptr));
}
__device__ __forceinline__ bf16x8 cat4(s16x4 a, s16x4 b) {
    bf16x8 r;
    r[0] = a[0]; r[1] = a[1]; r[2] = a[2]; r[3] = a[3];
    r[4] = b[0]; r[5] = b[1]; r[6] = b[2]; r[7] = b[3];
    return r;
}
__device__ __forceinline__ void glds16(const void* g, void* l) {
    __builtin_amdgcn_global_load_lds((const unsigned*)g, (LDS_AS unsigned*)l, 16, 0, 0);
}
__device__ __forceinline__ void glds16_asm(const void* gsrc, unsigned lds_dst) {
    unsigned keep;
    asm volatile("s_mov_b32 %0, m0\n\ts_mov_b32 m0, %2\n\ts_nop 0\n\tglobal_load_lds_dwordx4 %1, off\n\ts_mov_b32 m0, %0"
                 : "=&s"(keep) : "v"(gsrc), "s"(lds_dst) : "memory");
}
__device__ __forceinline__ void glds4_asm(const void* gsrc, unsigned lds_dst) {
    unsigned keep;
    asm volatile("s_mov_b32 %0, m0\n\ts_mov_b32 m0, %2\n\ts_nop 0\n\tglobal_load_lds_dword %1, off\n\ts_mov_b32 m0, %0"
                 : "=&s"(keep) : "v"(gsrc), "s"(lds_dst) : "memory");
}
__device__ __forceinline__ unsigned lds_addr(const void* p) { return (unsigned)(size_t)(LDS_AS const char*)p; }
#define WAIT_VM(n) asm volatile("s_waitcnt vmcnt(%0)" :: "n"(n) : "memory")
#define CBAR() asm volatile("" ::: "memory")
#define STAMP(kid, i) do {} while (0)
#define STAMPK(kid, i) do { if ((i) == 3) STAMP(kid, i); } while (0)

#define K1UNR 1
constexpr int K1_ABYTES = 256 * 64 * 2;
constexpr int K1_BBYTES = 64 * 192 * 2;
constexpr int K1_BOFF = 3 * K1_ABYTES;
constexpr int K1_AUX = K1_BOFF + 2 * K1_BBYTES;
constexpr int K1_JUNK = K1_AUX + 3 * 4096;
constexpr int K1_LDS = K1_JUNK + 256;

template <int NJ>
__device__ __forceinline__ void k1_epilogue(f32x4 (&acc)[4][NJ], char* __restrict__ ws, char* smem, int m0, int c0, int nt) {
    int t = threadIdx.x;
    asm volatile("" : "+v"(t));
    const int lane = t & 63, wid = t >> 6;
    const int wr = wid >> 1, wc = wid & 1;
    const int fr = lane & 15, g = lane >> 4;
    if (NJ == 5) {
        __syncthreads();
        const float qs = (nt < 4) ? 0.22360679774997896f * 1.4426950408889634f : 1.f;
#pragma unroll
        for (int j = 0; j < NJ; ++j) {
            const int n = wc * 80 + j * 16 + g * 4;
            const int hh = n / DH_, d = n % DH_;
            char* dst = smem + hh * 10240 + (wr * 64 + fr) * 40 + d * 2;
#pragma unroll
            for (int i = 0; i < 4; ++i) {
                u32x2 a = {pack_bf16(acc[i][j][0] * qs, acc[i][j][1] * qs), pack_bf16(acc[i][j][2] * qs, acc[i][j][3] * qs)};
                *(u32x2*)(dst + i * 640) = a;
            }
        }
        __syncthreads();
        const int which = nt >> 2;
        const int h0 = (nt & 3) * 8;
        char* dbase = ws + WS_Q + (size_t)which * QKV_BYTES + ((size_t)h0 * S_ + m0) * 40;
#pragma unroll
        for (int it = 0; it < 10; ++it) {
            const int e = it * 512 + t;
            const int hh = e / 640, r = e % 640;
            const u32x4 v = *(const u32x4*)(smem + e * 16);
            *(u32x4*)(dbase + (size_t)hh * (S_ * 40) + r * 16) = v;
        }
    } else {
        u16* hF = (u16*)(ws + WS_H);
#pragma unroll
        for (int j = 0; j < 3; ++j) {
            const int f = c0 + wc * 48 + j * 16 + g * 4;
            const int ks = f >> 5;
            const int lq = (f & 31) >> 3;
            const int hf = f & 4;
#pragma unroll
            for (int i = 0; i < 4; ++i) {
                const int m = m0 + wr * 64 + i * 16 + fr;
                unsigned pk[2];
#pragma unroll
                for (int r = 0; r < 2; ++r) {
                    const f32x2 gt = {acc[i][j][2 * r], acc[i][j][2 * r + 1]};
                    const f32x2 up = {acc[i][(j + 3) % NJ][2 * r], acc[i][(j + 3) % NJ][2 * r + 1]};
                    f32x2 e = gt * -1.4426950408889634f;
                    e = (f32x2){__builtin_amdgcn_exp2f(e[0]), __builtin_amdgcn_exp2f(e[1])};
                    e = e + 1.f;
                    const f32x2 rc = {__builtin_amdgcn_rcpf(e[0]), __builtin_amdgcn_rcpf(e[1])};
                    const f32x2 hv = (gt * up) * rc;
                    pk[r] = pack_bf16(hv[0], hv[1]);
                }
                u32x2 a = {pk[0], pk[1]};
                u16* dst = hF + ((size_t)((m >> 4) * KS_H + ks) * 64 + (m & 15) + 16 * lq) * 8 + hf;
                *(u32x2*)dst = a;
            }
        }
    }
}

template <int NJ>
__device__ __forceinline__ void k1_tile(const u16* __restrict__ X, const u16* __restrict__ W0, const u16* __restrict__ W1,
                                        char* __restrict__ ws, char* smem, int mt, int nt, int xs, int xcnt) {
    constexpr int BN = 32 * NJ;
    constexpr int ROWB = BN * 2;
    constexpr int UPR = BN / 8;
    constexpr int LDB = (NJ == 5) ? NQKV : F_;
    const int t = threadIdx.x;
    const int lane = t & 63, wid = t >> 6;
    const int m0 = mt * 256;
    const int c0 = (NJ == 5) ? nt * 160 : nt * 96;
    const int uw = __builtin_amdgcn_readfirstlane(wid);
    const unsigned sb0 = lds_addr(smem);

    if (uw == 10) {
        const u16* pp[3]; unsigned ps[3];
#pragma unroll
        for (int i = 0; i < 3; ++i) {
            constexpr int SPR = BN / 32;
            constexpr int NWS = 16 * SPR;
            const int xr = (256 + xcnt - 1) / xcnt;
            int e = i * 64 + lane;
            if (e < NWS) {
                const int kr = (mt & 3) * 16 + e / SPR, u = e % SPR;
                if (NJ == 5) pp[i] = W0 + (size_t)kr * LDB + c0 + u * 32;
                else pp[i] = ((u < 3) ? W0 : W1) + (size_t)kr * LDB + c0 + (u % 3) * 32;
                ps[i] = 64 * LDB;
            } else {
                e -= NWS;
                int r = xs * xr + (e >> 1);
                if ((e >> 1) >= xr || r > 255) { r = xs * xr; }
                pp[i] = X + (size_t)(m0 + r) * D_ + (e & 1) * 32;
                ps[i] = 64;
            }
        }
        const unsigned junk = sb0 + K1_JUNK;
        auto pf = [&](int kt) {
#pragma unroll
            for (int i = 0; i < 3; ++i) glds4_asm(pp[i] + (size_t)kt * ps[i], junk);
        };
        pf(0); pf(1); pf(2);
        WAIT_VM(6);
        __builtin_amdgcn_s_barrier();
        pf(3);
        for (int kt = 0; kt < 10; ++kt) {
            __builtin_amdgcn_s_barrier();
            if (kt + 4 < 10) pf(kt + 4);
        }
        WAIT_VM(0);
        return;
    }
    if (uw >= 8) {
        const int dw = uw - 8;
        constexpr int NBI = 2 * NJ;
        const u16* pa = X + (size_t)(m0 + dw * 128 + (lane >> 3)) * D_ + (((lane & 7) ^ (lane >> 3)) << 3);
        const u16* pb[NBI];
#pragma unroll
        for (int i = 0; i < NBI; ++i) {
            const int p = (dw * NBI + i) * 64 + lane;
            const int k = p / UPR, u = p % UPR;
            const int sw = (NJ == 5) ? ((k >> 3) & 1) : (((k >> 1) & 1) | (((k >> 3) & 1) << 1));
            const int c32 = (u >> 1) ^ sw, half = u & 1;
            if (NJ == 5) pb[i] = W0 + (size_t)k * LDB + c0 + c32 * 16 + half * 8;
            else pb[i] = ((c32 < 6) ? W0 : W1) + (size_t)k * LDB + c0 + (c32 < 6 ? c32 : c32 - 6) * 16 + half * 8;
        }
        auto loadA = [&](int kt) {
            const unsigned base = sb0 + (kt % 3) * K1_ABYTES + dw * 16384;
#pragma unroll
            for (int i = 0; i < 16; ++i) glds16_asm(pa + (size_t)i * 8 * D_ + kt * 64, base + i * 1024);
        };
        auto loadB = [&](int kt) {
            const unsigned base = sb0 + K1_BOFF + (kt & 1) * K1_BBYTES + dw * NBI * 1024;
#pragma unroll
            for (int i = 0; i < NBI; ++i) glds16_asm(pb[i] + (size_t)kt * 64 * LDB, base + i * 1024);
        };
        STAMP(0, 0);
        __builtin_amdgcn_s_barrier();
        loadB(0); loadB(1); loadA(1);
        WAIT_VM(16 + NBI);
        for (int kt = 0; kt < 10; ++kt) {
            STAMPK(0, 2 + kt * 3);
            __builtin_amdgcn_s_barrier();
            STAMPK(0, 3 + kt * 3);
            if (kt > 0 && kt + 1 < 10) loadB(kt + 1);
            if (kt + 2 < 10) { loadA(kt + 2); STAMPK(0, 4 + kt * 3); WAIT_VM(16); } else { STAMPK(0, 4 + kt * 3); WAIT_VM(0); }
        }
        STAMP(0, 40); STAMP(0, 41); STAMP(0, 42);
        return;
    }

    STAMP(0, 0);
    __builtin_amdgcn_s_barrier();
    {
        const u16* pa = X + (size_t)(m0 + uw * 32 + (lane >> 3)) * D_ + (((lane & 7) ^ (lane >> 3)) << 3);
#pragma unroll
        for (int i = 0; i < 4; ++i) glds16_asm(pa + (size_t)i * 8 * D_, sb0 + uw * 4096 + i * 1024);
    }
    const int wr = wid >> 1, wc = wid & 1;
    const int fr = lane & 15, g = lane >> 4;
    f32x4 acc[4][NJ];
#pragma unroll
    for (int i = 0; i < 4; ++i)
#pragma unroll
        for (int j = 0; j < NJ; ++j) acc[i][j] = (f32x4){0.f, 0.f, 0.f, 0.f};

    int aoff[2];
#pragma unroll
    for (int ks = 0; ks < 2; ++ks) {
        const int row = wr * 64 + fr;
        aoff[ks] = row * 128 + (((ks * 4 + g) ^ (row & 7)) << 4);
    }
    const int q4 = (lane & 15) >> 2, p4 = lane & 3;
    const int swl = (NJ == 5) ? (g & 1) : (((q4 >> 1) & 1) | ((g & 1) << 1));
    int boff[NJ];
#pragma unroll
    for (int j = 0; j < NJ; ++j) {
        const int c = (NJ == 5) ? (5 * wc + j) : (j < 3 ? 3 * wc + j : 6 + 3 * wc + (j - 3));
        boff[j] = K1_BOFF + (8 * g + q4) * ROWB + ((c ^ swl) << 5) + p4 * 8;
    }
    WAIT_VM(0);
    __builtin_amdgcn_s_barrier();
    CBAR();
    STAMP(0, 3);
    bf16x8 a0[4], a1[4], bq[NJ];
    auto rdB = [&](const char* baseB, int ks, int j) {
        s16x4 lo = tr_read(baseB + boff[j] + ks * (32 * ROWB));
        s16x4 hi = tr_read(baseB + boff[j] + ks * (32 * ROWB) + 4 * ROWB);
        return cat4(lo, hi);
    };
#pragma unroll
    for (int i = 0; i < 4; ++i) a0[i] = *(const bf16x8*)(smem + aoff[0] + i * 2048);
    __builtin_amdgcn_sched_barrier(0);
#pragma unroll
    for (int j = 0; j < NJ; ++j) { bq[j] = rdB(smem, 0, j); __builtin_amdgcn_sched_barrier(0); }
    int abuf = 0;
#pragma unroll 1
    for (int kt = 0; kt < 10; ++kt) {
        const char* baseA = smem + abuf * K1_ABYTES;
        const char* baseB = smem + (kt & 1) * K1_BBYTES;
        abuf = (abuf == 2) ? 0 : abuf + 1;
        const char* nextA = smem + abuf * K1_ABYTES;
        const char* nextB = smem + ((kt + 1) & 1) * K1_BBYTES;
#pragma unroll
        for (int i = 0; i < 4; ++i) a1[i] = *(const bf16x8*)(baseA + aoff[1] + i * 2048);
#pragma unroll
        for (int j = 0; j < NJ; ++j) {
#pragma unroll
            for (int i = 0; i < 4; ++i)
                acc[i][j] = __builtin_amdgcn_mfma_f32_16x16x32_bf16(bq[j], a0[i], acc[i][j], 0, 0, 0);
            bq[j] = rdB(baseB, 1, j);
            __builtin_amdgcn_sched_barrier(0);
        }
        asm volatile("s_waitcnt lgkmcnt(0)" ::: "memory");
        if (kt < 9) __builtin_amdgcn_s_barrier();
        CBAR();
#pragma unroll
        for (int i = 0; i < 4; ++i) a0[i] = *(const bf16x8*)(nextA + aoff[0] + i * 2048);
#pragma unroll
        for (int j = 0; j < NJ; ++j) {
#pragma unroll
            for (int i = 0; i < 4; ++i)
                acc[i][j] = __builtin_amdgcn_mfma_f32_16x16x32_bf16(bq[j], a1[i], acc[i][j], 0, 0, 0);
            bq[j] = rdB(nextB, 0, j);
            __builtin_amdgcn_sched_barrier(0);
        }
    }

    STAMP(0, 40);
    k1_epilogue<NJ>(acc, ws, smem, m0, c0, nt);
    STAMP(0, 41);
    STAMP(0, 42);
}

__device__ __forceinline__ void k1_aux(const u16* __restrict__ X, const u16* __restrict__ Wo, const u16* __restrict__ Wd,
                                       const u16* __restrict__ gate_w, char* __restrict__ ws, char* smem, int lin) {
    const int lane = threadIdx.x & 63;
    float* gout = (float*)(ws + WS_G);
    u16* woF = (u16*)(ws + WS_WO);
    u16* wdF = (u16*)(ws + WS_WD);
    const int q4 = (lane & 15) >> 2, p4 = lane & 3, g = lane >> 4;
    char* wl = smem + K1_AUX;
    const unsigned wla = lds_addr(wl);
    auto issue = [&](int unit, int slot) {
        const int ksg = unit / 10, cc = unit % 10;
        const u16* src = (ksg < KS_O) ? (Wo + (size_t)ksg * 32 * D_) : (Wd + (size_t)(ksg - KS_O) * 32 * D_);
#pragma unroll
        for (int i = 0; i < 4; ++i)
            glds16_asm(src + (size_t)(i * 8 + (lane >> 3)) * D_ + cc * 64 + (lane & 7) * 8, wla + slot * 4096 + i * 1024);
    };
    auto emit = [&](int unit, int slot) {
        const int ksg = unit / 10, cc = unit % 10;
#pragma unroll
        for (int j = 0; j < 4; ++j) {
            const char* a = wl + slot * 4096 + (8 * g + q4) * 128 + j * 32 + p4 * 8;
            s16x4 lo = tr_read(a);
            s16x4 hi = tr_read(a + 4 * 128);
            bf16x8 v = cat4(lo, hi);
            const int nt = cc * 4 + j;
            u16* dst = (ksg < KS_O) ? (woF + ((size_t)(nt * KS_O + ksg) * 64 + lane) * 8)
                                    : (wdF + ((size_t)(nt * KS_H + (ksg - KS_O)) * 64 + lane) * 8);
            *(bf16x8*)dst = v;
        }
    };
    __builtin_amdgcn_s_barrier();
    __builtin_amdgcn_s_barrier();
    issue(lin, 0); issue(lin + 240, 1); issue(lin + 480, 2);
    __builtin_amdgcn_s_barrier();
    __builtin_amdgcn_s_barrier();
    WAIT_VM(0);
    emit(lin, 0); emit(lin + 240, 1); emit(lin + 480, 2);
    asm volatile("s_waitcnt lgkmcnt(0)" ::: "memory");
    const bool extra = lin + 720 < 740;
    if (extra) issue(lin + 720, 0);
    __builtin_amdgcn_s_barrier();
    const u32x4 w0 = *(const u32x4*)(gate_w + lane * 8);
    u32x4 w1 = *(const u32x4*)(gate_w + 512 + (lane & 15) * 8);
    u32x4 x0[9], x1[9];
#pragma unroll
    for (int r = 0; r < 9; ++r) {
        int row = lin + 240 * r;
        row = row < S_ ? row : lin;
        x0[r] = *(const u32x4*)(X + (size_t)row * D_ + lane * 8);
        x1[r] = *(const u32x4*)(X + (size_t)row * D_ + 512 + (lane & 15) * 8);
    }
    CBAR();
    __builtin_amdgcn_s_barrier();
    __builtin_amdgcn_s_barrier();
    CBAR();
    if (lane >= 16) w1 = (u32x4){0u, 0u, 0u, 0u};
    float sacc[9];
#pragma unroll
    for (int r = 0; r < 9; ++r) {
        float a = 0.f;
#pragma unroll
        for (int e = 0; e < 4; ++e) {
            const unsigned xa = x0[r][e], wa = w0[e], xb = x1[r][e], wb = w1[e];
            a = __builtin_amdgcn_fdot2_f32_bf16(__builtin_bit_cast(bf2_t, xa), __builtin_bit_cast(bf2_t, wa), a, false);
            a = __builtin_amdgcn_fdot2_f32_bf16(__builtin_bit_cast(bf2_t, xb), __builtin_bit_cast(bf2_t, wb), a, false);
        }
        sacc[r] = a;
    }
    __builtin_amdgcn_s_barrier();
    {
        float z = 0.f;
#pragma unroll
        for (int r = 0; r < 9; ++r) {
            const float tot = wave_sum(sacc[r]);
            z = (lane == r) ? tot : z;
        }
        const int row = lin + 240 * lane;
        z = rbf(z);
        if (lane < 9 && row < S_) gout[row] = rbf(1.f / (1.f + __expf(-z)));
    }
    __builtin_amdgcn_s_barrier();
    WAIT_VM(0);
    if (extra) emit(lin + 720, 0);
    __builtin_amdgcn_s_barrier();
    __builtin_amdgcn_s_barrier();
}

__global__ __launch_bounds__(768) void k1_gemm(const u16* __restrict__ X, const u16* __restrict__ Wqkv,
                                               const u16* __restrict__ Wg, const u16* __restrict__ Wu,
                                               const u16* __restrict__ Wo, const u16* __restrict__ Wd,
                                               const u16* __restrict__ gate_w, char* __restrict__ ws) {
    __shared__ __attribute__((aligned(16))) char smem[K1_LDS];
    const int bid = blockIdx.x;
    const int xcd = bid & 7, idx = bid >> 3;
    const int ng = xcd >> 1, mg = xcd & 1;
    const int cnt = (ng < 2) ? 8 : 7;
    const int nstart = (ng < 2) ? ng * 8 : 16 + (ng - 2) * 7;
    if (idx >= 4 * cnt) {
        if (threadIdx.x < 32)
            *(u32x4*)(ws + WS_FLAG + (size_t)(((xcd - 4) * 4 + (idx - 28)) * 2 + (threadIdx.x >> 4)) * FLAG_LD + (threadIdx.x & 15) * 16) = (u32x4){0u, 0u, 0u, 0u};
        return;
    }
    if (__builtin_amdgcn_readfirstlane(threadIdx.x >> 6) == 11) {
        const int lin = (ng < 2) ? xcd * 32 + idx : 128 + (xcd - 4) * 28 + idx;
        k1_aux(X, Wo, Wd, gate_w, ws, smem, lin);
        return;
    }
    const int mt = mg * 4 + (idx & 3);
    const int nt = nstart + (idx >> 2);
    if (nt < 12) k1_tile<5>(X, Wqkv, Wqkv, ws, smem, mt, nt, idx >> 2, cnt);
    else k1_tile<6>(X, Wg, Wu, ws, smem, mt, nt - 12, idx >> 2, cnt);
}

typedef unsigned long long u64;
#define GDELAY 4
#define PF2 0
#define ACQ 0
#define ROT 0
#define ROLL 0
#define GPRIO 0
#define POLLSLP 4
#define APRIO 2
#define ST2(i) do {} while (0)
#define ST2ID() do {} while (0)
typedef __amdgpu_buffer_rsrc_t rsrc_t;
constexpr int AUX_AGENT = 16;
__device__ __forceinline__ void st_agent(void* p, u32x2 v) {
    __hip_atomic_store((u64*)p, __builtin_bit_cast(u64, v), __ATOMIC_RELAXED, __HIP_MEMORY_SCOPE_AGENT);
}
template <int AUX> __device__ __forceinline__ bf16x8 bld(rsrc_t r, int voff, int soff) {
    return __builtin_bit_cast(bf16x8, __builtin_amdgcn_raw_buffer_load_b128(r, voff, soff, AUX));
}

__device__ __forceinline__ float fmx(float a, float b) { return __builtin_elementwise_maximum(a, b); }

template <bool EDGE>
__device__ __forceinline__ void k2_attn_wave(char* __restrict__ oF, const char* sK, const char* sV, const bf16x8 (&qf)[2],
                                             const float (&gg)[2], int h, int qw, int rbase, int lane) {
    const int fr = lane & 15, g = lane >> 4;
    const int q4 = (lane & 15) >> 2, p4 = lane & 3;
    const float NEG = -__builtin_inff();
    const bf16x8 ones = {0x3f80, 0x3f80, 0x3f80, 0x3f80, 0x3f80, 0x3f80, 0x3f80, 0x3f80};
    const int ts = 16 - (qw >> 4);
    const char* kb = sK + (rbase + fr) * 40 + (g < 3 ? g : 2) * 16;
    const char* vb = sV + (rbase + 4 * g + q4) * 40 + p4 * 8;
    f32x4 cLo, cHi;
#pragma unroll
    for (int r = 0; r < 4; ++r) {
        cLo[r] = (fr >= 4 * g + r) ? NEG : 0.f;
        cHi[r] = (4 * g + r > fr) ? NEG : 0.f;
    }
    auto rdK = [&](int tt) {
        const u32x2 klo = *(const u32x2*)(kb + tt * 640), khi = *(const u32x2*)(kb + tt * 640 + 8);
        const u32x4 kv = {klo[0], klo[1], khi[0], khi[1]};
        return __builtin_bit_cast(bf16x8, kv);
    };
    const f32x4 zero = {0.f, 0.f, 0.f, 0.f};
#define SB0() __builtin_amdgcn_sched_barrier(0)
    float mx[2] = {NEG, NEG};
    {
        bf16x8 kf[3];
        f32x4 s[2][2];
        kf[0] = rdK(0); kf[1] = rdK(1);
#pragma unroll
        for (int tt = 0; tt < 19; ++tt) {
            if (tt + 2 < 18) kf[(tt + 2) % 3] = rdK(tt + 2);
#pragma unroll
            for (int qt = 0; qt < 2; ++qt) {
                const int u = tt - qt;
                if (tt < 18 && u >= 0 && u <= 16)
                    s[tt & 1][qt] = __builtin_amdgcn_mfma_f32_16x16x32_bf16(kf[tt % 3], qf[qt], u == 0 ? cLo : (u == 16 ? cHi : zero), 0, 0, 0);
                SB0();
                const int tp = tt - 1, up = tp - qt;
                if (tp >= 0 && up >= 0 && up <= 16) {
                    const f32x4 p = s[tp & 1][qt];
                    if (EDGE && tp < 16) {
                        const float m = fmx(fmx(p[0], p[1]), fmx(p[2], p[3]));
                        mx[qt] = fmx(mx[qt], (tp < ts) ? NEG : m);
                    } else {
                        mx[qt] = fmx(fmx(mx[qt], p[0]), p[1]);
                        mx[qt] = fmx(fmx(mx[qt], p[2]), p[3]);
                    }
                }
                SB0();
            }
        }
    }
    CBAR();
    bf16x8 kf[2][2], vf[2][2];
    auto rdV = [&](int pp, bf16x8 (&v)[2]) {
#pragma unroll
        for (int dt = 0; dt < 2; ++dt) {
            s16x4 lo = tr_read(vb + (2 * pp) * 640 + dt * 32);
            s16x4 hi = tr_read(vb + (2 * pp + 1) * 640 + dt * 32);
            v[dt] = cat4(lo, hi);
        }
    };
    kf[0][0] = rdK(0); kf[0][1] = rdK(1);
    kf[1][0] = rdK(2); kf[1][1] = rdK(3);
    rdV(0, vf[0]);
    f32x4 cm[2], cl[2], ch[2];
#pragma unroll
    for (int qt = 0; qt < 2; ++qt) {
        float m = mx[qt];
        {
            const unsigned mi = __builtin_bit_cast(unsigned, m);
            auto r = __builtin_amdgcn_permlane32_swap(mi, mi, false, false);
            m = fmx(__builtin_bit_cast(float, (unsigned)r[0]), __builtin_bit_cast(float, (unsigned)r[1]));
        }
        {
            const unsigned mi = __builtin_bit_cast(unsigned, m);
            auto r = __builtin_amdgcn_permlane16_swap(mi, mi, false, false);
            m = fmx(__builtin_bit_cast(float, (unsigned)r[0]), __builtin_bit_cast(float, (unsigned)r[1]));
        }
        m = -m;
        cm[qt] = (f32x4){m, m, m, m};
#pragma unroll
        for (int r = 0; r < 4; ++r) {
            cl[qt][r] = (fr >= 4 * g + r) ? NEG : m;
            ch[qt][r] = (4 * g + r > fr) ? NEG : m;
        }
    }
    f32x4 oacc[2][3];
#pragma unroll
    for (int qt = 0; qt < 2; ++qt)
#pragma unroll
        for (int dt = 0; dt < 3; ++dt) oacc[qt][dt] = zero;
    f32x4 s[2][2][2];
    u32x4 pk[2];
    auto valid = [](int tt, int qt) { return tt - qt >= 0 && tt - qt <= 16; };
    auto QK = [&](int pp, int hh, int qt) {
        const int tt = 2 * pp + hh, u = tt - qt;
        if (pp < 9 && valid(tt, qt))
            s[pp & 1][hh][qt] = __builtin_amdgcn_mfma_f32_16x16x32_bf16(kf[pp & 1][hh], qf[qt], u == 0 ? cl[qt] : (u == 16 ? ch[qt] : cm[qt]), 0, 0, 0);
        SB0();
    };
    auto EX = [&](int pp, int hh, int qt, int half) {
        const int tt = 2 * pp + hh;
        if (pp < 9) {
            unsigned v = 0u;
            if (valid(tt, qt)) {
                const f32x4 x = s[pp & 1][hh][qt];
                v = pack_bf16(__builtin_amdgcn_exp2f(x[2 * half]), __builtin_amdgcn_exp2f(x[2 * half + 1]));
                if (EDGE && tt < 16) v = (tt < ts) ? 0u : v;
            }
            pk[qt][2 * hh + half] = v;
        }
        SB0();
    };
    auto PV = [&](int pp, int qt, int dt) {
        if (pp >= 0 && pp < 9)
            oacc[qt][dt] = __builtin_amdgcn_mfma_f32_16x16x32_bf16(dt < 2 ? vf[pp & 1][dt] : ones, __builtin_bit_cast(bf16x8, pk[qt]), oacc[qt][dt], 0, 0, 0);
        SB0();
    };
    SB0();
    QK(0, 0, 0); QK(0, 1, 0); QK(0, 0, 1); QK(0, 1, 1);
#pragma unroll
    for (int pp = 0; pp < 10; ++pp) {
        PV(pp - 1, 1, 0); EX(pp, 0, 0, 0);
        PV(pp - 1, 1, 1); EX(pp, 0, 0, 1);
        PV(pp - 1, 1, 2);
        if (pp + 1 < 9) rdV(pp + 1, vf[(pp + 1) & 1]);
        QK(pp + 1, 0, 0); EX(pp, 1, 0, 0);
        QK(pp + 1, 1, 0); EX(pp, 1, 0, 1);
        PV(pp, 0, 0); EX(pp, 0, 1, 0);
        PV(pp, 0, 1); EX(pp, 0, 1, 1);
        PV(pp, 0, 2);
        QK(pp + 1, 0, 1); EX(pp, 1, 1, 0);
        QK(pp + 1, 1, 1);
        if (pp + 2 < 9) { kf[pp & 1][0] = rdK(2 * pp + 4); kf[pp & 1][1] = rdK(2 * pp + 5); }
        EX(pp, 1, 1, 1);
    }
#pragma unroll
    for (int qt = 0; qt < 2; ++qt) {
        const float inv = gg[qt] / oacc[qt][2][0];
        const int m = qw + 16 * qt + fr;
        {
            const int k0 = h * DH_ + 4 * g;
            u32x2 a = {pack_bf16(oacc[qt][0][0] * inv, oacc[qt][0][1] * inv), pack_bf16(oacc[qt][0][2] * inv, oacc[qt][0][3] * inv)};
            st_agent(oF + ((size_t)((m >> 4) * KS_O + (k0 >> 5)) * 64 + (m & 15) + 16 * ((k0 & 31) >> 3)) * 16 + (k0 & 4) * 2, a);
        }
        if (g == 0) {
            const int k0 = h * DH_ + 16;
            u32x2 a = {pack_bf16(oacc[qt][1][0] * inv, oacc[qt][1][1] * inv), pack_bf16(oacc[qt][1][2] * inv, oacc[qt][1][3] * inv)};
            st_agent(oF + ((size_t)((m >> 4) * KS_O + (k0 >> 5)) * 64 + (m & 15) + 16 * ((k0 & 31) >> 3)) * 16 + (k0 & 4) * 2, a);
        }
    }
}

__device__ __forceinline__ void k2_attn(char* __restrict__ ws, char* smem, int bid) {
    const int h = (bid & 7) + 8 * ((bid >> 3) & 3);
    const int qb = bid >> 5;
    const int q0 = qb * 128;
    const int t = threadIdx.x, lane = t & 63;
    const int w = __builtin_amdgcn_readfirstlane(t >> 6);
    const char* Qb = ws + WS_Q + (size_t)h * S_ * 40;
    const char* Kb = ws + WS_K + (size_t)h * S_ * 40;
    const char* Vb = ws + WS_V + (size_t)h * S_ * 40;
    char* sK = smem;
    char* sV = smem + 15360;

    ST2(0); ST2ID();
    if (APRIO) __builtin_amdgcn_s_setprio(APRIO);
#pragma unroll
    for (int i = 0; i < 4; ++i) {
        const int p = i * 256 + t;
        if (i < 3 || w < 3) {
            int off = (q0 - 256) * 40 + p * 16;
            off = off < 0 ? 0 : off;
            glds16(Kb + off, sK + p * 16);
            glds16(Vb + off, sV + p * 16);
        }
    }
    if (t < 4) *(u32x4*)(smem + 2 * 15360 + t * 16) = (u32x4){0u, 0u, 0u, 0u};
    const int fr = lane & 15, g = lane >> 4;
    const int qw = q0 + 32 * w;
    bf16x8 qf[2];
    float gg[2];
#pragma unroll
    for (int qt = 0; qt < 2; ++qt) {
        const char* qp = Qb + (size_t)(qw + qt * 16 + fr) * 40 + (g < 3 ? g : 2) * 16;
        u32x2 lo = *(const u32x2*)qp;
        u32x2 hi = *(const u32x2*)(qp + (g < 2 ? 8 : 0));
        if (g >= 2) hi = (u32x2){0u, 0u};
        if (g == 3) lo = (u32x2){0u, 0u};
        u32x4 v = {lo[0], lo[1], hi[0], hi[1]};
        qf[qt] = __builtin_bit_cast(bf16x8, v);
        gg[qt] = ((const float*)(ws + WS_G))[qw + qt * 16 + fr];
    }
    __syncthreads();
    ST2(1);
    char* oF = ws + WS_O;
    if (qb >= 2) k2_attn_wave<false>(oF, sK, sV, qf, gg, h, qw, 32 * w, lane);
    else k2_attn_wave<true>(oF, sK, sV, qf, gg, h, qw, 32 * w, lane);
    ST2(2);
    asm volatile("s_waitcnt vmcnt(0)" ::: "memory");
    ST2(3);
    if (lane == 0)
        __hip_atomic_store((unsigned*)(ws + WS_FLAG + (qw >> 6) * FLAG_LD) + h * 2 + ((qw >> 5) & 1), 1u, __ATOMIC_RELAXED, __HIP_MEMORY_SCOPE_AGENT);
}

constexpr int RED_LD = 84;
__device__ __forceinline__ void k2_gemm(char* __restrict__ ws, u16* __restrict__ out, char* smem, int bid) {
    float* red = (float*)smem;
    const int xcd = bid & 7, idx = bid >> 3;
    const int mt = (xcd >> 1) * 8 + (idx & 7);
    const int nt = (xcd & 1) * 4 + (idx >> 3);
    const int t = threadIdx.x, lane = t & 63;
    const int w = __builtin_amdgcn_readfirstlane(t >> 6);
    const int fr = lane & 15, g = lane >> 4;
    const int nsteps = (w < 3) ? 14 : 12;
    const rsrc_t rs = __builtin_amdgcn_make_buffer_rsrc(ws, 0, (int)WS_FLAG, 0x00020000);
    const int vo = lane * 16;
    int cur = ROT ? (((idx & 7) + 2 * (idx >> 3)) & 7) * nsteps >> 3 : 0;
    const int ap = (int)WS_H + ((mt * 4) * KS_H + 14 * w) * 1024;
    const int bp = (int)WS_WD + ((nt * 5) * KS_H + 14 * w) * 1024;

    f32x4 acc[4][5];
#pragma unroll
    for (int i = 0; i < 4; ++i)
#pragma unroll
        for (int j = 0; j < 5; ++j) acc[i][j] = (f32x4){0.f, 0.f, 0.f, 0.f};
    bf16x8 a0[4], b0[5], a1[4], b1[5];
    auto ld = [&](bf16x8 (&a)[4], bf16x8 (&b)[5]) {
#pragma unroll
        for (int i = 0; i < 4; ++i) a[i] = bld<0>(rs, vo, ap + (i * KS_H + cur) * 1024);
#pragma unroll
        for (int j = 0; j < 5; ++j) b[j] = bld<0>(rs, vo, bp + (j * KS_H + cur) * 1024);
        cur = (cur + 1 == nsteps) ? 0 : cur + 1;
    };
    auto mm = [&](bf16x8 (&a)[4], bf16x8 (&b)[5]) {
#pragma unroll
        for (int i = 0; i < 4; ++i)
#pragma unroll
            for (int j = 0; j < 5; ++j)
                acc[i][j] = __builtin_amdgcn_mfma_f32_16x16x32_bf16(b[j], a[i], acc[i][j], 0, 0, 0);
    };
    ST2(0); ST2ID();
    if (GPRIO) __builtin_amdgcn_s_setprio(GPRIO);
#pragma unroll 1
    for (int i = 0; i < GDELAY; ++i) __builtin_amdgcn_s_sleep(8);
    ld(a0, b0);
    ld(a1, b1);
    const float gl = ((const float*)(ws + WS_G))[mt * 64 + lane];
    auto mmld = [&](bf16x8 (&a)[4], bf16x8 (&b)[5]) {
#pragma unroll
        for (int j = 0; j < 5; ++j) {
#pragma unroll
            for (int i = 0; i < 4; ++i) {
                acc[i][j] = __builtin_amdgcn_mfma_f32_16x16x32_bf16(b[j], a[i], acc[i][j], 0, 0, 0);
                if (j == 4) { __builtin_amdgcn_sched_barrier(0); a[i] = bld<0>(rs, vo, ap + (i * KS_H + cur) * 1024); __builtin_amdgcn_sched_barrier(0); }
            }
            __builtin_amdgcn_sched_barrier(0);
            b[j] = bld<0>(rs, vo, bp + (j * KS_H + cur) * 1024);
            __builtin_amdgcn_sched_barrier(0);
        }
        cur = (cur + 1 == nsteps) ? 0 : cur + 1;
    };
#pragma unroll 1
    for (int s = 0; s < nsteps - 2; s += 2) {
        if (ROLL) { mmld(a0, b0); mmld(a1, b1); }
        else {
            mm(a0, b0); ld(a0, b0);
            mm(a1, b1); ld(a1, b1);
        }
    }
    ST2(1);
    const int op = (int)WS_O + ((mt * 4) * KS_O + 5 * w) * 1024;
    const int wp = (int)WS_WO + ((nt * 5) * KS_O + 5 * w) * 1024;
    auto ldw = [&](bf16x8 (&b)[5], int s) {
#pragma unroll
        for (int j = 0; j < 5; ++j) b[j] = bld<0>(rs, vo, wp + (j * KS_O + s) * 1024);
    };
    auto ldo = [&](bf16x8 (&a)[4], int s) {
#pragma unroll
        for (int i = 0; i < 4; ++i) a[i] = bld<ACQ ? 0 : AUX_AGENT>(rs, vo, op + (i * KS_O + s) * 1024);
    };
#define SB() __builtin_amdgcn_sched_barrier(0)
    SB(); mm(a0, b0); SB(); ldw(b0, 0);
    SB(); mm(a1, b1); SB(); ldw(b1, 1);
    SB();
#pragma unroll
    for (int i = 0; i < 4; ++i) {
        const float sc = rbf(1.f - __builtin_bit_cast(float, __builtin_amdgcn_ds_bpermute((i * 16 + fr) * 4, __builtin_bit_cast(int, gl))));
#pragma unroll
        for (int j = 0; j < 5; ++j) acc[i][j] = acc[i][j] * sc;
        asm volatile("" : "+v"(acc[i][0]), "+v"(acc[i][1]), "+v"(acc[i][2]), "+v"(acc[i][3]), "+v"(acc[i][4]));
    }
    ST2(2);
    {
        if (w == 0) {
            const unsigned* fp = (const unsigned*)(ws + WS_FLAG + mt * FLAG_LD) + lane;
            while (true) {
                const unsigned f = __hip_atomic_load(fp, __ATOMIC_RELAXED, __HIP_MEMORY_SCOPE_AGENT);
                if (__builtin_amdgcn_ballot_w64(f != 1u) == 0ull) break;
                __builtin_amdgcn_s_sleep(POLLSLP);
            }
        }
        asm volatile("" ::: "memory");
        __builtin_amdgcn_s_barrier();
        asm volatile("" ::: "memory");
    }
    ST2(3);
    ldo(a0, 0);
    ldo(a1, 1);
    SB(); mm(a0, b0); SB(); ldo(a0, 2); ldw(b0, 2);
    SB(); mm(a1, b1); SB(); ldo(a1, 3); ldw(b1, 3);
    SB(); mm(a0, b0); SB(); ldo(a0, 4); ldw(b0, 4);
    SB(); mm(a1, b1);
    SB(); mm(a0, b0);
    SB();
    ST2(4);
    float* rw = red + (w & 1) * 64 * RED_LD + fr * RED_LD + g * 4;
    if (w >= 2) {
#pragma unroll
        for (int i = 0; i < 4; ++i)
#pragma unroll
            for (int j = 0; j < 5; ++j) *(f32x4*)(rw + i * 16 * RED_LD + j * 16) = acc[i][j];
    }
    __syncthreads();
    if (w < 2) {
#pragma unroll
        for (int i = 0; i < 4; ++i)
#pragma unroll
            for (int j = 0; j < 5; ++j) {
                float* q = rw + i * 16 * RED_LD + j * 16;
                *(f32x4*)q = acc[i][j] + *(const f32x4*)q;
            }
    }
    __syncthreads();
    {
        const int m = t >> 2, c0 = (t & 3) * 20;
        u16* dst = out + (size_t)(mt * 64 + m) * D_ + nt * 80 + c0;
#pragma unroll
        for (int c = 0; c < 20; c += 4) {
            const f32x4 v = *(const f32x4*)(red + m * RED_LD + c0 + c) + *(const f32x4*)(red + (64 + m) * RED_LD + c0 + c);
            u32x2 pk = {pack_bf16(v[0], v[1]), pack_bf16(v[2], v[3])};
            *(u32x2*)(dst + c) = pk;
        }
    }
    ST2(5);
}

__global__ __launch_bounds__(256, 3) void k2_mix(char* __restrict__ ws, u16* __restrict__ out) {
    __shared__ __attribute__((aligned(16))) char smem[2 * 64 * RED_LD * 4];
    if (blockIdx.x < 256) k2_gemm(ws, out, smem, blockIdx.x);
    else k2_attn(ws, smem, blockIdx.x - 256);
}

extern "C" void kernel_launch(void* const* d_in, const int* in_sizes, int n_in,
                              void* d_out, int out_size, void* d_ws, size_t ws_size,
                              hipStream_t stream) {
    const u16* X = (const u16*)d_in[0];
    const u16* Wqkv = (const u16*)d_in[1];
    const u16* Wo = (const u16*)d_in[2];
    const u16* Wg = (const u16*)d_in[4];
    const u16* Wu = (const u16*)d_in[5];
    const u16* Wd = (const u16*)d_in[6];
    const u16* gw = (const u16*)d_in[7];
    char* ws = (char*)d_ws;
    k1_gemm<<<256, 768, 0, stream>>>(X, Wqkv, Wg, Wu, Wo, Wd, gw, ws);
    k2_mix<<<768, 256, 0, stream>>>(ws, (u16*)d_out);
}
```

```cpp
#define ESC "sc1"
#define WSC1
#define OSC 0
#include <hip/hip_runtime.h>
#include <hip/hip_bf16.h>
#include <stdint.h>

typedef unsigned short u16;
typedef short bf16x8 __attribute__((ext_vector_type(8)));
typedef short s16x4 __attribute__((ext_vector_type(4)));
typedef float f32x4 __attribute__((ext_vector_type(4)));
typedef float f32x2 __attribute__((ext_vector_type(2)));
typedef unsigned u32x4 __attribute__((ext_vector_type(4)));
typedef unsigned u32x2 __attribute__((ext_vector_type(2)));
typedef __bf16 bf2_t __attribute__((ext_vector_type(2)));

#define LDS_AS __attribute__((address_space(3)))

constexpr int S_ = 2048, D_ = 640, F_ = 1728, H_ = 32, DH_ = 20, NQKV = 1920;
constexpr int KS_O = D_ / 32;
constexpr int KS_H = F_ / 32;

constexpr size_t WS_Q = 0;
constexpr size_t QKV_BYTES = (size_t)H_ * S_ * DH_ * 2;
constexpr size_t WS_K = WS_Q + QKV_BYTES;
constexpr size_t WS_V = WS_K + QKV_BYTES;
constexpr size_t WS_H = WS_V + QKV_BYTES + 4096;
constexpr size_t WS_O = WS_H + (size_t)S_ * F_ * 2;
constexpr size_t WS_WO = WS_O + (size_t)S_ * D_ * 2;
constexpr size_t WS_WD = WS_WO + (size_t)D_ * D_ * 2;
constexpr size_t WS_G = WS_WD + (size_t)F_ * D_ * 2;
constexpr size_t WS_FLAG = WS_G + (size_t)S_ * 4;
constexpr int FLAG_LD = 4352;

__device__ __forceinline__ unsigned pack_bf16(float a, float b) {
    bf2_t v = {(__bf16)a, (__bf16)b};
    return __builtin_bit_cast(unsigned, v);
}
__device__ __forceinline__ float rbf(float x) { return (float)(__bf16)x; }
__device__ __forceinline__ float bf2f(u16 v) { return __uint_as_float(((unsigned)v) << 16); }

template <int CTRL> __device__ __forceinline__ float dpp_add(float x) {
    return x + __builtin_bit_cast(float, __builtin_amdgcn_update_dpp(0, __builtin_bit_cast(int, x), CTRL, 0xf, 0xf, false));
}
__device__ __forceinline__ float wave_sum(float x) {
    x = dpp_add<0xB1>(x); x = dpp_add<0x4E>(x); x = dpp_add<0x141>(x); x = dpp_add<0x140>(x);
    const int xi = __builtin_bit_cast(int, x);
    return (__builtin_bit_cast(float, __builtin_amdgcn_readlane(xi, 0)) + __builtin_bit_cast(float, __builtin_amdgcn_readlane(xi, 16))) +
           (__builtin_bit_cast(float, __builtin_amdgcn_readlane(xi, 32)) + __builtin_bit_cast(float, __builtin_amdgcn_readlane(xi, 48)));
}
__device__ __forceinline__ s16x4 tr_read(const char* lds_ptr) {
    return __builtin_amdgcn_ds_read_tr16_b64_v4i16((LDS_AS s16x4*)(lds_ptr));
}
__device__ __forceinline__ bf16x8 cat4(s16x4 a, s16x4 b) {
    bf16x8 r;
    r[0] = a[0]; r[1] = a[1]; r[2] = a[2]; r[3] = a[3];
    r[4] = b[0]; r[5] = b[1]; r[6] = b[2]; r[7] = b[3];
    return r;
}
__device__ __forceinline__ void glds16(const void* g, void* l) {
    __builtin_amdgcn_global_load_lds((const unsigned*)g, (LDS_AS unsigned*)l, 16, 0, 0);
}
__device__ __forceinline__ void glds16_asm(const void* gsrc, unsigned lds_dst) {
    unsigned keep;
    asm volatile("s_mov_b32 %0, m0\n\ts_mov_b32 m0, %2\n\ts_nop 0\n\tglobal_load_lds_dwordx4 %1, off\n\ts_mov_b32 m0, %0"
                 : "=&s"(keep) : "v"(gsrc), "s"(lds_dst) : "memory");
}
__device__ __forceinline__ void glds4_asm(const void* gsrc, unsigned lds_dst) {
    unsigned keep;
    asm volatile("s_mov_b32 %0, m0\n\ts_mov_b32 m0, %2\n\ts_nop 0\n\tglobal_load_lds_dword %1, off\n\ts_mov_b32 m0, %0"
                 : "=&s"(keep) : "v"(gsrc), "s"(lds_dst) : "memory");
}
__device__ __forceinline__ unsigned lds_addr(const void* p) { return (unsigned)(size_t)(LDS_AS const char*)p; }
typedef __amdgpu_buffer_rsrc_t rsrc_t;
constexpr int AUX_AGENT = 16;
__device__ __forceinline__ rsrc_t mk_rsrc(const void* p, unsigned bytes) { return __builtin_amdgcn_make_buffer_rsrc((void*)p, 0, (int)bytes, 0x00020000); }
template <int AUX> __device__ __forceinline__ void bst4(rsrc_t r, int voff, u32x4 v) { __builtin_amdgcn_raw_buffer_store_b128(v, r, voff, 0, AUX); }
template <int AUX> __device__ __forceinline__ void bst2(rsrc_t r, int voff, u32x2 v) { __builtin_amdgcn_raw_buffer_store_b64(v, r, voff, 0, AUX); }
#define WAIT_VM(n) asm volatile("s_waitcnt vmcnt(%0)" :: "n"(n) : "memory")
#define CBAR() asm volatile("" ::: "memory")
#define STAMP(kid, i) do {} while (0)
#define STAMPK(kid, i) do { if ((i) == 3) STAMP(kid, i); } while (0)

#define WPG 64
#define K1UNR 1
constexpr int K1_ABYTES = 256 * 64 * 2;
constexpr int K1_BBYTES = 64 * 192 * 2;
constexpr int K1_BOFF = 3 * K1_ABYTES;
constexpr int K1_AUX = K1_BOFF + 2 * K1_BBYTES;
constexpr int K1_JUNK = K1_AUX + 3 * 4096;
constexpr int K1_LDS = K1_JUNK + 256;

template <int NJ>
__device__ __forceinline__ void k1_epilogue(f32x4 (&acc)[4][NJ], char* __restrict__ ws, char* smem, int m0, int c0, int nt) {
    int t = threadIdx.x;
    asm volatile("" : "+v"(t));
    const int lane = t & 63, wid = t >> 6;
    const int wr = wid >> 1, wc = wid & 1;
    const int fr = lane & 15, g = lane >> 4;
    const rsrc_t rw = mk_rsrc(ws, (unsigned)WS_FLAG);
    if (NJ == 5) {
        __syncthreads();
        const float qs = (nt < 4) ? 0.22360679774997896f * 1.4426950408889634f : 1.f;
#pragma unroll
        for (int j = 0; j < NJ; ++j) {
            const int n = wc * 80 + j * 16 + g * 4;
            const int hh = n / DH_, d = n % DH_;
            char* dst = smem + hh * 10240 + (wr * 64 + fr) * 40 + d * 2;
#pragma unroll
            for (int i = 0; i < 4; ++i) {
                u32x2 a = {pack_bf16(acc[i][j][0] * qs, acc[i][j][1] * qs), pack_bf16(acc[i][j][2] * qs, acc[i][j][3] * qs)};
                *(u32x2*)(dst + i * 640) = a;
            }
        }
        __syncthreads();
        const int which = nt >> 2;
        const int h0 = (nt & 3) * 8;
        char* dbase = ws + WS_Q + (size_t)which * QKV_BYTES + ((size_t)h0 * S_ + m0) * 40;
        const int dofs = (int)(WS_Q + (size_t)which * QKV_BYTES + ((size_t)h0 * S_ + m0) * 40);
#pragma unroll
        for (int it = 0; it < 10; ++it) {
            const int e = it * 512 + t;
            const int hh = e / 640, r = e % 640;
            const u32x4 v = *(const u32x4*)(smem + e * 16);
            bst4<AUX_AGENT>(rw, dofs + hh * (S_ * 40) + r * 16, v);
        }
    } else {
        u16* hF = (u16*)(ws + WS_H);
#pragma unroll
        for (int j = 0; j < 3; ++j) {
            const int f = c0 + wc * 48 + j * 16 + g * 4;
            const int ks = f >> 5;
            const int lq = (f & 31) >> 3;
            const int hf = f & 4;
#pragma unroll
            for (int i = 0; i < 4; ++i) {
                const int m = m0 + wr * 64 + i * 16 + fr;
                unsigned pk[2];
#pragma unroll
                for (int r = 0; r < 2; ++r) {
                    const f32x2 gt = {acc[i][j][2 * r], acc[i][j][2 * r + 1]};
                    const f32x2 up = {acc[i][(j + 3) % NJ][2 * r], acc[i][(j + 3) % NJ][2 * r + 1]};
                    f32x2 e = gt * -1.4426950408889634f;
                    e = (f32x2){__builtin_amdgcn_exp2f(e[0]), __builtin_amdgcn_exp2f(e[1])};
                    e = e + 1.f;
                    const f32x2 rc = {__builtin_amdgcn_rcpf(e[0]), __builtin_amdgcn_rcpf(e[1])};
                    const f32x2 hv = (gt * up) * rc;
                    pk[r] = pack_bf16(hv[0], hv[1]);
                }
                u32x2 a = {pk[0], pk[1]};
                u16* dst = hF + ((size_t)((m >> 4) * KS_H + ks) * 64 + (m & 15) + 16 * lq) * 8 + hf;
                bst2<AUX_AGENT>(rw, (int)WS_H + ((((m >> 4) * KS_H + ks) * 64 + (m & 15) + 16 * lq) * 8 + hf) * 2, a);
            }
        }
    }
}

template <int NJ>
__device__ __forceinline__ void k1_tile(const u16* __restrict__ X, const u16* __restrict__ W0, const u16* __restrict__ W1,
                                        char* __restrict__ ws, char* smem, int mt, int nt, int xs, int xcnt) {
    constexpr int BN = 32 * NJ;
    constexpr int ROWB = BN * 2;
    constexpr int UPR = BN / 8;
    constexpr int LDB = (NJ == 5) ? NQKV : F_;
    const int t = threadIdx.x;
    const int lane = t & 63, wid = t >> 6;
    const int m0 = mt * 256;
    const int c0 = (NJ == 5) ? nt * 160 : nt * 96;
    const int uw = __builtin_amdgcn_readfirstlane(wid);
    const unsigned sb0 = lds_addr(smem);

    if (uw == 10) {
        const u16* pp[3]; unsigned ps[3];
#pragma unroll
        for (int i = 0; i < 3; ++i) {
            constexpr int SPR = BN / 32;
            constexpr int NWS = 16 * SPR;
            const int xr = (256 + xcnt - 1) / xcnt;
            int e = i * 64 + lane;
            if (e < NWS) {
                const int kr = (mt & 3) * 16 + e / SPR, u = e % SPR;
                if (NJ == 5) pp[i] = W0 + (size_t)kr * LDB + c0 + u * 32;
                else pp[i] = ((u < 3) ? W0 : W1) + (size_t)kr * LDB + c0 + (u % 3) * 32;
                ps[i] = 64 * LDB;
            } else {
                e -= NWS;
                int r = xs * xr + (e >> 1);
                if ((e >> 1) >= xr || r > 255) { r = xs * xr; }
                pp[i] = X + (size_t)(m0 + r) * D_ + (e & 1) * 32;
                ps[i] = 64;
            }
        }
        const unsigned junk = sb0 + K1_JUNK;
        auto pf = [&](int kt) {
#pragma unroll
            for (int i = 0; i < 3; ++i) glds4_asm(pp[i] + (size_t)kt * ps[i], junk);
        };
        pf(0); pf(1); pf(2);
        WAIT_VM(6);
        __builtin_amdgcn_s_barrier();
        pf(3);
        for (int kt = 0; kt < 10; ++kt) {
            __builtin_amdgcn_s_barrier();
            if (kt + 4 < 10) pf(kt + 4);
        }
        WAIT_VM(0);
        return;
    }
    if (uw >= 8) {
        const int dw = uw - 8;
        constexpr int NBI = 2 * NJ;
        const u16* pa = X + (size_t)(m0 + dw * 128 + (lane >> 3)) * D_ + (((lane & 7) ^ (lane >> 3)) << 3);
        const u16* pb[NBI];
#pragma unroll
        for (int i = 0; i < NBI; ++i) {
            const int p = (dw * NBI + i) * 64 + lane;
            const int k = p / UPR, u = p % UPR;
            const int sw = (NJ == 5) ? ((k >> 3) & 1) : (((k >> 1) & 1) | (((k >> 3) & 1) << 1));
            const int c32 = (u >> 1) ^ sw, half = u & 1;
            if (NJ == 5) pb[i] = W0 + (size_t)k * LDB + c0 + c32 * 16 + half * 8;
            else pb[i] = ((c32 < 6) ? W0 : W1) + (size_t)k * LDB + c0 + (c32 < 6 ? c32 : c32 - 6) * 16 + half * 8;
        }
        auto loadA = [&](int kt) {
            const unsigned base = sb0 + (kt % 3) * K1_ABYTES + dw * 16384;
#pragma unroll
            for (int i = 0; i < 16; ++i) glds16_asm(pa + (size_t)i * 8 * D_ + kt * 64, base + i * 1024);
        };
        auto loadB = [&](int kt) {
            const unsigned base = sb0 + K1_BOFF + (kt & 1) * K1_BBYTES + dw * NBI * 1024;
#pragma unroll
            for (int i = 0; i < NBI; ++i) glds16_asm(pb[i] + (size_t)kt * 64 * LDB, base + i * 1024);
        };
        STAMP(0, 0);
        __builtin_amdgcn_s_barrier();
        loadB(0); loadB(1); loadA(1);
        WAIT_VM(16 + NBI);
        for (int kt = 0; kt < 10; ++kt) {
            STAMPK(0, 2 + kt * 3);
            __builtin_amdgcn_s_barrier();
            STAMPK(0, 3 + kt * 3);
            if (kt > 0 && kt + 1 < 10) loadB(kt + 1);
            if (kt + 2 < 10) { loadA(kt + 2); STAMPK(0, 4 + kt * 3); WAIT_VM(16); } else { STAMPK(0, 4 + kt * 3); WAIT_VM(0); }
        }
        STAMP(0, 40); STAMP(0, 41); STAMP(0, 42);
        return;
    }

    STAMP(0, 0);
    __builtin_amdgcn_s_barrier();
    {
        const u16* pa = X + (size_t)(m0 + uw * 32 + (lane >> 3)) * D_ + (((lane & 7) ^ (lane >> 3)) << 3);
#pragma unroll
        for (int i = 0; i < 4; ++i) glds16_asm(pa + (size_t)i * 8 * D_, sb0 + uw * 4096 + i * 1024);
    }
    const int wr = wid >> 1, wc = wid & 1;
    const int fr = lane & 15, g = lane >> 4;
    f32x4 acc[4][NJ];
#pragma unroll
    for (int i = 0; i < 4; ++i)
#pragma unroll
        for (int j = 0; j < NJ; ++j) acc[i][j] = (f32x4){0.f, 0.f, 0.f, 0.f};

    int aoff[2];
#pragma unroll
    for (int ks = 0; ks < 2; ++ks) {
        const int row = wr * 64 + fr;
        aoff[ks] = row * 128 + (((ks * 4 + g) ^ (row & 7)) << 4);
    }
    const int q4 = (lane & 15) >> 2, p4 = lane & 3;
    const int swl = (NJ == 5) ? (g & 1) : (((q4 >> 1) & 1) | ((g & 1) << 1));
    int boff[NJ];
#pragma unroll
    for (int j = 0; j < NJ; ++j) {
        const int c = (NJ == 5) ? (5 * wc + j) : (j < 3 ? 3 * wc + j : 6 + 3 * wc + (j - 3));
        boff[j] = K1_BOFF + (8 * g + q4) * ROWB + ((c ^ swl) << 5) + p4 * 8;
    }
    WAIT_VM(0);
    __builtin_amdgcn_s_barrier();
    CBAR();
    STAMP(0, 3);
    bf16x8 a0[4], a1[4], bq[NJ];
    auto rdB = [&](const char* baseB, int ks, int j) {
        s16x4 lo = tr_read(baseB + boff[j] + ks * (32 * ROWB));
        s16x4 hi = tr_read(baseB + boff[j] + ks * (32 * ROWB) + 4 * ROWB);
        return cat4(lo, hi);
    };
#pragma unroll
    for (int i = 0; i < 4; ++i) a0[i] = *(const bf16x8*)(smem + aoff[0] + i * 2048);
    __builtin_amdgcn_sched_barrier(0);
#pragma unroll
    for (int j = 0; j < NJ; ++j) { bq[j] = rdB(smem, 0, j); __builtin_amdgcn_sched_barrier(0); }
    int abuf = 0;
#pragma unroll 1
    for (int kt = 0; kt < 10; ++kt) {
        const char* baseA = smem + abuf * K1_ABYTES;
        const char* baseB = smem + (kt & 1) * K1_BBYTES;
        abuf = (abuf == 2) ? 0 : abuf + 1;
        const char* nextA = smem + abuf * K1_ABYTES;
        const char* nextB = smem + ((kt + 1) & 1) * K1_BBYTES;
#pragma unroll
        for (int i = 0; i < 4; ++i) a1[i] = *(const bf16x8*)(baseA + aoff[1] + i * 2048);
#pragma unroll
        for (int j = 0; j < NJ; ++j) {
#pragma unroll
            for (int i = 0; i < 4; ++i)
                acc[i][j] = __builtin_amdgcn_mfma_f32_16x16x32_bf16(bq[j], a0[i], acc[i][j], 0, 0, 0);
            bq[j] = rdB(baseB, 1, j);
            __builtin_amdgcn_sched_barrier(0);
        }
        asm volatile("s_waitcnt lgkmcnt(0)" ::: "memory");
        if (kt < 9) __builtin_amdgcn_s_barrier();
        CBAR();
#pragma unroll
        for (int i = 0; i < 4; ++i) a0[i] = *(const bf16x8*)(nextA + aoff[0] + i * 2048);
#pragma unroll
        for (int j = 0; j < NJ; ++j) {
#pragma unroll
            for (int i = 0; i < 4; ++i)
                acc[i][j] = __builtin_amdgcn_mfma_f32_16x16x32_bf16(bq[j], a1[i], acc[i][j], 0, 0, 0);
            bq[j] = rdB(nextB, 0, j);
            __builtin_amdgcn_sched_barrier(0);
        }
    }

    STAMP(0, 40);
    k1_epilogue<NJ>(acc, ws, smem, m0, c0, nt);
    STAMP(0, 41);
    STAMP(0, 42);
}

__device__ __forceinline__ void k1_aux(const u16* __restrict__ X, const u16* __restrict__ Wo, const u16* __restrict__ Wd,
                                       const u16* __restrict__ gate_w, char* __restrict__ ws, char* smem, int lin) {
    const int lane = threadIdx.x & 63;
    float* gout = (float*)(ws + WS_G);
    u16* woF = (u16*)(ws + WS_WO);
    u16* wdF = (u16*)(ws + WS_WD);
    const int q4 = (lane & 15) >> 2, p4 = lane & 3, g = lane >> 4;
    char* wl = smem + K1_AUX;
    const unsigned wla = lds_addr(wl);
    auto issue = [&](int unit, int slot) {
        const int ksg = unit / 10, cc = unit % 10;
        const u16* src = (ksg < KS_O) ? (Wo + (size_t)ksg * 32 * D_) : (Wd + (size_t)(ksg - KS_O) * 32 * D_);
#pragma unroll
        for (int i = 0; i < 4; ++i)
            glds16_asm(src + (size_t)(i * 8 + (lane >> 3)) * D_ + cc * 64 + (lane & 7) * 8, wla + slot * 4096 + i * 1024);
    };
    auto emit = [&](int unit, int slot) {
        const int ksg = unit / 10, cc = unit % 10;
#pragma unroll
        for (int j = 0; j < 4; ++j) {
            const char* a = wl + slot * 4096 + (8 * g + q4) * 128 + j * 32 + p4 * 8;
            s16x4 lo = tr_read(a);
            s16x4 hi = tr_read(a + 4 * 128);
            bf16x8 v = cat4(lo, hi);
            const int nt = cc * 4 + j;
            u16* dst = (ksg < KS_O) ? (woF + ((size_t)(nt * KS_O + ksg) * 64 + lane) * 8)
                                    : (wdF + ((size_t)(nt * KS_H + (ksg - KS_O)) * 64 + lane) * 8);
            bst4<AUX_AGENT>(mk_rsrc(ws, (unsigned)WS_FLAG), (int)((char*)dst - ws), __builtin_bit_cast(u32x4, v));
        }
    };
    __builtin_amdgcn_s_barrier();
    __builtin_amdgcn_s_barrier();
    issue(lin, 0); issue(lin + 240, 1); issue(lin + 480, 2);
    __builtin_amdgcn_s_barrier();
    __builtin_amdgcn_s_barrier();
    WAIT_VM(0);
    emit(lin, 0); emit(lin + 240, 1); emit(lin + 480, 2);
    asm volatile("s_waitcnt lgkmcnt(0)" ::: "memory");
    const bool extra = lin + 720 < 740;
    if (extra) issue(lin + 720, 0);
    __builtin_amdgcn_s_barrier();
    const u32x4 w0 = *(const u32x4*)(gate_w + lane * 8);
    u32x4 w1 = *(const u32x4*)(gate_w + 512 + (lane & 15) * 8);
    u32x4 x0[9], x1[9];
#pragma unroll
    for (int r = 0; r < 9; ++r) {
        int row = lin + 240 * r;
        row = row < S_ ? row : lin;
        x0[r] = *(const u32x4*)(X + (size_t)row * D_ + lane * 8);
        x1[r] = *(const u32x4*)(X + (size_t)row * D_ + 512 + (lane & 15) * 8);
    }
    CBAR();
    __builtin_amdgcn_s_barrier();
    __builtin_amdgcn_s_barrier();
    CBAR();
    if (lane >= 16) w1 = (u32x4){0u, 0u, 0u, 0u};
    float sacc[9];
#pragma unroll
    for (int r = 0; r < 9; ++r) {
        float a = 0.f;
#pragma unroll
        for (int e = 0; e < 4; ++e) {
            const unsigned xa = x0[r][e], wa = w0[e], xb = x1[r][e], wb = w1[e];
            a = __builtin_amdgcn_fdot2_f32_bf16(__builtin_bit_cast(bf2_t, xa), __builtin_bit_cast(bf2_t, wa), a, false);
            a = __builtin_amdgcn_fdot2_f32_bf16(__builtin_bit_cast(bf2_t, xb), __builtin_bit_cast(bf2_t, wb), a, false);
        }
        sacc[r] = a;
    }
    __builtin_amdgcn_s_barrier();
    {
        float z = 0.f;
#pragma unroll
        for (int r = 0; r < 9; ++r) {
            const float tot = wave_sum(sacc[r]);
            z = (lane == r) ? tot : z;
        }
        const int row = lin + 240 * lane;
        z = rbf(z);
        if (lane < 9 && row < S_) gout[row] = rbf(1.f / (1.f + __expf(-z)));
    }
    __builtin_amdgcn_s_barrier();
    WAIT_VM(0);
    if (extra) emit(lin + 720, 0);
    __builtin_amdgcn_s_barrier();
    __builtin_amdgcn_s_barrier();
}

__global__ __launch_bounds__(768) void k1_gemm(const u16* __restrict__ X, const u16* __restrict__ Wqkv,
                                               const u16* __restrict__ Wg, const u16* __restrict__ Wu,
                                               const u16* __restrict__ Wo, const u16* __restrict__ Wd,
                                               const u16* __restrict__ gate_w, char* __restrict__ ws) {
    __shared__ __attribute__((aligned(16))) char smem[K1_LDS];
    const int bid = blockIdx.x;
    const int xcd = bid & 7, idx = bid >> 3;
    const int ng = xcd >> 1, mg = xcd & 1;
    const int cnt = (ng < 2) ? 8 : 7;
    const int nstart = (ng < 2) ? ng * 8 : 16 + (ng - 2) * 7;
    if (idx >= 4 * cnt) {
        if (threadIdx.x < 32)
            *(u32x4*)(ws + WS_FLAG + (size_t)(((xcd - 4) * 4 + (idx - 28)) * 2 + (threadIdx.x >> 4)) * FLAG_LD + (threadIdx.x & 15) * 16) = (u32x4){0u, 0u, 0u, 0u};
        return;
    }
    if (__builtin_amdgcn_readfirstlane(threadIdx.x >> 6) == 11) {
        const int lin = (ng < 2) ? xcd * 32 + idx : 128 + (xcd - 4) * 28 + idx;
        k1_aux(X, Wo, Wd, gate_w, ws, smem, lin);
        return;
    }
    const int mt = mg * 4 + (idx & 3);
    const int nt = nstart + (idx >> 2);
    if (nt < 12) k1_tile<5>(X, Wqkv, Wqkv, ws, smem, mt, nt, idx >> 2, cnt);
    else k1_tile<6>(X, Wg, Wu, ws, smem, mt, nt - 12, idx >> 2, cnt);
}

typedef unsigned long long u64;
#define GDELAY 4
#define PF2 0
#define ACQ 0
#define ROT 0
#define ROLL 0
#define GPRIO 0
#define POLLSLP 4
#define APRIO 2
#define ST2(i) do {} while (0)
#define ST2ID() do {} while (0)
__device__ __forceinline__ void st_agent(void* p, u32x2 v) {
    __hip_atomic_store((u64*)p, __builtin_bit_cast(u64, v), __ATOMIC_RELAXED, __HIP_MEMORY_SCOPE_AGENT);
}
template <int AUX> __device__ __forceinline__ bf16x8 bld(rsrc_t r, int voff, int soff) {
    return __builtin_bit_cast(bf16x8, __builtin_amdgcn_raw_buffer_load_b128(r, voff, soff, AUX));
}

__device__ __forceinline__ float fmx(float a, float b) { return __builtin_elementwise_maximum(a, b); }

template <bool EDGE>
__device__ __forceinline__ void k2_attn_wave(char* __restrict__ oF, const char* sK, const char* sV, const bf16x8 (&qf)[2],
                                             const float (&gg)[2], int h, int qw, int rbase, int lane) {
    const int fr = lane & 15, g = lane >> 4;
    const int q4 = (lane & 15) >> 2, p4 = lane & 3;
    const float NEG = -__builtin_inff();
    const bf16x8 ones = {0x3f80, 0x3f80, 0x3f80, 0x3f80, 0x3f80, 0x3f80, 0x3f80, 0x3f80};
    const int ts = 16 - (qw >> 4);
    const char* kb = sK + (rbase + fr) * 40 + (g < 3 ? g : 2) * 16;
    const char* vb = sV + (rbase + 4 * g + q4) * 40 + p4 * 8;
    f32x4 cLo, cHi;
#pragma unroll
    for (int r = 0; r < 4; ++r) {
        cLo[r] = (fr >= 4 * g + r) ? NEG : 0.f;
        cHi[r] = (4 * g + r > fr) ? NEG : 0.f;
    }
    auto rdK = [&](int tt) {
        const u32x2 klo = *(const u32x2*)(kb + tt * 640), khi = *(const u32x2*)(kb + tt * 640 + 8);
        const u32x4 kv = {klo[0], klo[1], khi[0], khi[1]};
        return __builtin_bit_cast(bf16x8, kv);
    };
    const f32x4 zero = {0.f, 0.f, 0.f, 0.f};
#define SB0() __builtin_amdgcn_sched_barrier(0)
    float mx[2] = {NEG, NEG};
    {
        bf16x8 kf[3];
        f32x4 s[2][2];
        kf[0] = rdK(0); kf[1] = rdK(1);
#pragma unroll
        for (int tt = 0; tt < 19; ++tt) {
            if (tt + 2 < 18) kf[(tt + 2) % 3] = rdK(tt + 2);
#pragma unroll
            for (int qt = 0; qt < 2; ++qt) {
                const int u = tt - qt;
                if (tt < 18 && u >= 0 && u <= 16)
                    s[tt & 1][qt] = __builtin_amdgcn_mfma_f32_16x16x32_bf16(kf[tt % 3], qf[qt], u == 0 ? cLo : (u == 16 ? cHi : zero), 0, 0, 0);
                SB0();
                const int tp = tt - 1, up = tp - qt;
                if (tp >= 0 && up >= 0 && up <= 16) {
                    const f32x4 p = s[tp & 1][qt];
                    if (EDGE && tp < 16) {
                        const float m = fmx(fmx(p[0], p[1]), fmx(p[2], p[3]));
                        mx[qt] = fmx(mx[qt], (tp < ts) ? NEG : m);
                    } else {
                        mx[qt] = fmx(fmx(mx[qt], p[0]), p[1]);
                        mx[qt] = fmx(fmx(mx[qt], p[2]), p[3]);
                    }
                }
                SB0();
            }
        }
    }
    CBAR();
    bf16x8 kf[2][2], vf[2][2];
    auto rdV = [&](int pp, bf16x8 (&v)[2]) {
#pragma unroll
        for (int dt = 0; dt < 2; ++dt) {
            s16x4 lo = tr_read(vb + (2 * pp) * 640 + dt * 32);
            s16x4 hi = tr_read(vb + (2 * pp + 1) * 640 + dt * 32);
            v[dt] = cat4(lo, hi);
        }
    };
    kf[0][0] = rdK(0); kf[0][1] = rdK(1);
    kf[1][0] = rdK(2); kf[1][1] = rdK(3);
    rdV(0, vf[0]);
    f32x4 cm[2], cl[2], ch[2];
#pragma unroll
    for (int qt = 0; qt < 2; ++qt) {
        float m = mx[qt];
        {
            const unsigned mi = __builtin_bit_cast(unsigned, m);
            auto r = __builtin_amdgcn_permlane32_swap(mi, mi, false, false);
            m = fmx(__builtin_bit_cast(float, (unsigned)r[0]), __builtin_bit_cast(float, (unsigned)r[1]));
        }
        {
            const unsigned mi = __builtin_bit_cast(unsigned, m);
            auto r = __builtin_amdgcn_permlane16_swap(mi, mi, false, false);
            m = fmx(__builtin_bit_cast(float, (unsigned)r[0]), __builtin_bit_cast(float, (unsigned)r[1]));
        }
        m = -m;
        cm[qt] = (f32x4){m, m, m, m};
#pragma unroll
        for (int r = 0; r < 4; ++r) {
            cl[qt][r] = (fr >= 4 * g + r) ? NEG : m;
            ch[qt][r] = (4 * g + r > fr) ? NEG : m;
        }
    }
    f32x4 oacc[2][3];
#pragma unroll
    for (int qt = 0; qt < 2; ++qt)
#pragma unroll
        for (int dt = 0; dt < 3; ++dt) oacc[qt][dt] = zero;
    f32x4 s[2][2][2];
    u32x4 pk[2];
    auto valid = [](int tt, int qt) { return tt - qt >= 0 && tt - qt <= 16; };
    auto QK = [&](int pp, int hh, int qt) {
        const int tt = 2 * pp + hh, u = tt - qt;
        if (pp < 9 && valid(tt, qt))
            s[pp & 1][hh][qt] = __builtin_amdgcn_mfma_f32_16x16x32_bf16(kf[pp & 1][hh], qf[qt], u == 0 ? cl[qt] : (u == 16 ? ch[qt] : cm[qt]), 0, 0, 0);
        SB0();
    };
    auto EX = [&](int pp, int hh, int qt, int half) {
        const int tt = 2 * pp + hh;
        if (pp < 9) {
            unsigned v = 0u;
            if (valid(tt, qt)) {
                const f32x4 x = s[pp & 1][hh][qt];
                v = pack_bf16(__builtin_amdgcn_exp2f(x[2 * half]), __builtin_amdgcn_exp2f(x[2 * half + 1]));
                if (EDGE && tt < 16) v = (tt < ts) ? 0u : v;
            }
            pk[qt][2 * hh + half] = v;
        }
        SB0();
    };
    auto PV = [&](int pp, int qt, int dt) {
        if (pp >= 0 && pp < 9)
            oacc[qt][dt] = __builtin_amdgcn_mfma_f32_16x16x32_bf16(dt < 2 ? vf[pp & 1][dt] : ones, __builtin_bit_cast(bf16x8, pk[qt]), oacc[qt][dt], 0, 0, 0);
        SB0();
    };
    SB0();
    QK(0, 0, 0); QK(0, 1, 0); QK(0, 0, 1); QK(0, 1, 1);
#pragma unroll
    for (int pp = 0; pp < 10; ++pp) {
        PV(pp - 1, 1, 0); EX(pp, 0, 0, 0);
        PV(pp - 1, 1, 1); EX(pp, 0, 0, 1);
        PV(pp - 1, 1, 2);
        if (pp + 1 < 9) rdV(pp + 1, vf[(pp + 1) & 1]);
        QK(pp + 1, 0, 0); EX(pp, 1, 0, 0);
        QK(pp + 1, 1, 0); EX(pp, 1, 0, 1);
        PV(pp, 0, 0); EX(pp, 0, 1, 0);
        PV(pp, 0, 1); EX(pp, 0, 1, 1);
        PV(pp, 0, 2);
        QK(pp + 1, 0, 1); EX(pp, 1, 1, 0);
        QK(pp + 1, 1, 1);
        if (pp + 2 < 9) { kf[pp & 1][0] = rdK(2 * pp + 4); kf[pp & 1][1] = rdK(2 * pp + 5); }
        EX(pp, 1, 1, 1);
    }
#pragma unroll
    for (int qt = 0; qt < 2; ++qt) {
        const float inv = gg[qt] / oacc[qt][2][0];
        const int m = qw + 16 * qt + fr;
        {
            const int k0 = h * DH_ + 4 * g;
            u32x2 a = {pack_bf16(oacc[qt][0][0] * inv, oacc[qt][0][1] * inv), pack_bf16(oacc[qt][0][2] * inv, oacc[qt][0][3] * inv)};
            st_agent(oF + ((size_t)((m >> 4) * KS_O + (k0 >> 5)) * 64 + (m & 15) + 16 * ((k0 & 31) >> 3)) * 16 + (k0 & 4) * 2, a);
        }
        if (g == 0) {
            const int k0 = h * DH_ + 16;
            u32x2 a = {pack_bf16(oacc[qt][1][0] * inv, oacc[qt][1][1] * inv), pack_bf16(oacc[qt][1][2] * inv, oacc[qt][1][3] * inv)};
            st_agent(oF + ((size_t)((m >> 4) * KS_O + (k0 >> 5)) * 64 + (m & 15) + 16 * ((k0 & 31) >> 3)) * 16 + (k0 & 4) * 2, a);
        }
    }
}

__device__ __forceinline__ void k2_attn(char* __restrict__ ws, char* smem, int bid) {
    const int h = (bid & 7) + 8 * ((bid >> 3) & 3);
    const int qb = bid >> 5;
    const int q0 = qb * 128;
    const int t = threadIdx.x, lane = t & 63;
    const int w = __builtin_amdgcn_readfirstlane(t >> 6);
    const char* Qb = ws + WS_Q + (size_t)h * S_ * 40;
    const char* Kb = ws + WS_K + (size_t)h * S_ * 40;
    const char* Vb = ws + WS_V + (size_t)h * S_ * 40;
    char* sK = smem;
    char* sV = smem + 15360;

    ST2(0); ST2ID();
    if (APRIO) __builtin_amdgcn_s_setprio(APRIO);
#pragma unroll
    for (int i = 0; i < 4; ++i) {
        const int p = i * 256 + t;
        if (i < 3 || w < 3) {
            int off = (q0 - 256) * 40 + p * 16;
            off = off < 0 ? 0 : off;
            glds16(Kb + off, sK + p * 16);
            glds16(Vb + off, sV + p * 16);
        }
    }
    if (t < 4) *(u32x4*)(smem + 2 * 15360 + t * 16) = (u32x4){0u, 0u, 0u, 0u};
    const int fr = lane & 15, g = lane >> 4;
    const int qw = q0 + 32 * w;
    bf16x8 qf[2];
    float gg[2];
#pragma unroll
    for (int qt = 0; qt < 2; ++qt) {
        const char* qp = Qb + (size_t)(qw + qt * 16 + fr) * 40 + (g < 3 ? g : 2) * 16;
        u32x2 lo = *(const u32x2*)qp;
        u32x2 hi = *(const u32x2*)(qp + (g < 2 ? 8 : 0));
        if (g >= 2) hi = (u32x2){0u, 0u};
        if (g == 3) lo = (u32x2){0u, 0u};
        u32x4 v = {lo[0], lo[1], hi[0], hi[1]};
        qf[qt] = __builtin_bit_cast(bf16x8, v);
        gg[qt] = ((const float*)(ws + WS_G))[qw + qt * 16 + fr];
    }
    __syncthreads();
    ST2(1);
    char* oF = ws + WS_O;
    if (qb >= 2) k2_attn_wave<false>(oF, sK, sV, qf, gg, h, qw, 32 * w, lane);
    else k2_attn_wave<true>(oF, sK, sV, qf, gg, h, qw, 32 * w, lane);
    ST2(2);
    asm volatile("s_waitcnt vmcnt(0)" ::: "memory");
    ST2(3);
    if (lane == 0)
        __hip_atomic_store((unsigned*)(ws + WS_FLAG + (qw >> 6) * FLAG_LD) + h * 2 + ((qw >> 5) & 1), 1u, __ATOMIC_RELAXED, __HIP_MEMORY_SCOPE_AGENT);
}

constexpr int RED_LD = 84;
__device__ __forceinline__ void k2_gemm(char* __restrict__ ws, u16* __restrict__ out, char* smem, int bid) {
    float* red = (float*)smem;
    const int xcd = bid & 7, idx = bid >> 3;
    const int mt = (xcd >> 1) * 8 + (idx & 7);
    const int nt = (xcd & 1) * 4 + (idx >> 3);
    const int t = threadIdx.x, lane = t & 63;
    const int w = __builtin_amdgcn_readfirstlane(t >> 6);
    const int fr = lane & 15, g = lane >> 4;
    const int nsteps = (w < 3) ? 14 : 12;
    const rsrc_t rs = __builtin_amdgcn_make_buffer_rsrc(ws, 0, (int)WS_FLAG, 0x00020000);
    const int vo = lane * 16;
    int cur = ROT ? (((idx & 7) + 2 * (idx >> 3)) & 7) * nsteps >> 3 : 0;
    const int ap = (int)WS_H + ((mt * 4) * KS_H + 14 * w) * 1024;
    const int bp = (int)WS_WD + ((nt * 5) * KS_H + 14 * w) * 1024;

    f32x4 acc[4][5];
#pragma unroll
    for (int i = 0; i < 4; ++i)
#pragma unroll
        for (int j = 0; j < 5; ++j) acc[i][j] = (f32x4){0.f, 0.f, 0.f, 0.f};
    bf16x8 a0[4], b0[5], a1[4], b1[5];
    auto ld = [&](bf16x8 (&a)[4], bf16x8 (&b)[5]) {
#pragma unroll
        for (int i = 0; i < 4; ++i) a[i] = bld<0>(rs, vo, ap + (i * KS_H + cur) * 1024);
#pragma unroll
        for (int j = 0; j < 5; ++j) b[j] = bld<0>(rs, vo, bp + (j * KS_H + cur) * 1024);
        cur = (cur + 1 == nsteps) ? 0 : cur + 1;
    };
    auto mm = [&](bf16x8 (&a)[4], bf16x8 (&b)[5]) {
#pragma unroll
        for (int i = 0; i < 4; ++i)
#pragma unroll
            for (int j = 0; j < 5; ++j)
                acc[i][j] = __builtin_amdgcn_mfma_f32_16x16x32_bf16(b[j], a[i], acc[i][j], 0, 0, 0);
    };
    ST2(0); ST2ID();
    if (GPRIO) __builtin_amdgcn_s_setprio(GPRIO);
#pragma unroll 1
    for (int i = 0; i < GDELAY; ++i) __builtin_amdgcn_s_sleep(8);
    ld(a0, b0);
    ld(a1, b1);
    const float gl = ((const float*)(ws + WS_G))[mt * 64 + lane];
    auto mmld = [&](bf16x8 (&a)[4], bf16x8 (&b)[5]) {
#pragma unroll
        for (int j = 0; j < 5; ++j) {
#pragma unroll
            for (int i = 0; i < 4; ++i) {
                acc[i][j] = __builtin_amdgcn_mfma_f32_16x16x32_bf16(b[j], a[i], acc[i][j], 0, 0, 0);
                if (j == 4) { __builtin_amdgcn_sched_barrier(0); a[i] = bld<0>(rs, vo, ap + (i * KS_H + cur) * 1024); __builtin_amdgcn_sched_barrier(0); }
            }
            __builtin_amdgcn_sched_barrier(0);
            b[j] = bld<0>(rs, vo, bp + (j * KS_H + cur) * 1024);
            __builtin_amdgcn_sched_barrier(0);
        }
        cur = (cur + 1 == nsteps) ? 0 : cur + 1;
    };
#pragma unroll 1
    for (int s = 0; s < nsteps - 2; s += 2) {
        if (ROLL) { mmld(a0, b0); mmld(a1, b1); }
        else {
            mm(a0, b0); ld(a0, b0);
            mm(a1, b1); ld(a1, b1);
        }
    }
    ST2(1);
    const int op = (int)WS_O + ((mt * 4) * KS_O + 5 * w) * 1024;
    const int wp = (int)WS_WO + ((nt * 5) * KS_O + 5 * w) * 1024;
    auto ldw = [&](bf16x8 (&b)[5], int s) {
#pragma unroll
        for (int j = 0; j < 5; ++j) b[j] = bld<0>(rs, vo, wp + (j * KS_O + s) * 1024);
    };
    auto ldo = [&](bf16x8 (&a)[4], int s) {
#pragma unroll
        for (int i = 0; i < 4; ++i) a[i] = bld<ACQ ? 0 : AUX_AGENT>(rs, vo, op + (i * KS_O + s) * 1024);
    };
#define SB() __builtin_amdgcn_sched_barrier(0)
    SB(); mm(a0, b0); SB(); ldw(b0, 0);
    SB(); mm(a1, b1); SB(); ldw(b1, 1);
    SB();
#pragma unroll
    for (int i = 0; i < 4; ++i) {
        const float sc = rbf(1.f - __builtin_bit_cast(float, __builtin_amdgcn_ds_bpermute((i * 16 + fr) * 4, __builtin_bit_cast(int, gl))));
#pragma unroll
        for (int j = 0; j < 5; ++j) acc[i][j] = acc[i][j] * sc;
        asm volatile("" : "+v"(acc[i][0]), "+v"(acc[i][1]), "+v"(acc[i][2]), "+v"(acc[i][3]), "+v"(acc[i][4]));
    }
    ST2(2);
    {
        if (w == 0) {
            const unsigned* fp = (const unsigned*)(ws + WS_FLAG + mt * FLAG_LD) + lane;
            while (true) {
                const unsigned f = __hip_atomic_load(fp, __ATOMIC_RELAXED, __HIP_MEMORY_SCOPE_AGENT);
                if (__builtin_amdgcn_ballot_w64(f != 1u) == 0ull) break;
                __builtin_amdgcn_s_sleep(POLLSLP);
            }
        }
        asm volatile("" ::: "memory");
        __builtin_amdgcn_s_barrier();
        asm volatile("" ::: "memory");
    }
    ST2(3);
    ldo(a0, 0);
    ldo(a1, 1);
    SB(); mm(a0, b0); SB(); ldo(a0, 2); ldw(b0, 2);
    SB(); mm(a1, b1); SB(); ldo(a1, 3); ldw(b1, 3);
    SB(); mm(a0, b0); SB(); ldo(a0, 4); ldw(b0, 4);
    SB(); mm(a1, b1);
    SB(); mm(a0, b0);
    SB();
    ST2(4);
    float* rw = red + (w & 1) * 64 * RED_LD + fr * RED_LD + g * 4;
    if (w >= 2) {
#pragma unroll
        for (int i = 0; i < 4; ++i)
#pragma unroll
            for (int j = 0; j < 5; ++j) *(f32x4*)(rw + i * 16 * RED_LD + j * 16) = acc[i][j];
    }
    __syncthreads();
    if (w < 2) {
#pragma unroll
        for (int i = 0; i < 4; ++i)
#pragma unroll
            for (int j = 0; j < 5; ++j) {
                float* q = rw + i * 16 * RED_LD + j * 16;
                *(f32x4*)q = acc[i][j] + *(const f32x4*)q;
            }
    }
    __syncthreads();
    {
        const int m = t >> 2, c0 = (t & 3) * 20;
        u16* dst = out + (size_t)(mt * 64 + m) * D_ + nt * 80 + c0;
#pragma unroll
        for (int c = 0; c < 20; c += 4) {
            const f32x4 v = *(const f32x4*)(red + m * RED_LD + c0 + c) + *(const f32x4*)(red + (64 + m) * RED_LD + c0 + c);
            u32x2 pk = {pack_bf16(v[0], v[1]), pack_bf16(v[2], v[3])};
            *(u32x2*)(dst + c) = pk;
        }
    }
    ST2(5);
}

__global__ __launch_bounds__(256, 3) void k2_mix(char* __restrict__ ws, u16* __restrict__ out) {
    __shared__ __attribute__((aligned(16))) char smem[2 * 64 * RED_LD * 4];
    if (blockIdx.x < 256) k2_gemm(ws, out, smem, blockIdx.x);
    else k2_attn(ws, smem, blockIdx.x - 256);
}

extern "C" void kernel_launch(void* const* d_in, const int* in_sizes, int n_in,
                              void* d_out, int out_size, void* d_ws, size_t ws_size,
                              hipStream_t stream) {
    const u16* X = (const u16*)d_in[0];
    const u16* Wqkv = (const u16*)d_in[1];
    const u16* Wo = (const u16*)d_in[2];
    const u16* Wg = (const u16*)d_in[4];
    const u16* Wu = (const u16*)d_in[5];
    const u16* Wd = (const u16*)d_in[6];
    const u16* gw = (const u16*)d_in[7];
    char* ws = (char*)d_ws;
    k1_gemm<<<256, 768, 0, stream>>>(X, Wqkv, Wg, Wu, Wo, Wd, gw, ws);
    k2_mix<<<768, 256, 0, stream>>>(ws, (u16*)d_out);
}
```

```cpp
#include <hip/hip_runtime.h>
#include <hip/hip_bf16.h>
#include <stdint.h>

typedef unsigned short u16;
typedef short bf16x8 __attribute__((ext_vector_type(8)));
typedef short s16x4 __attribute__((ext_vector_type(4)));
typedef float f32x4 __attribute__((ext_vector_type(4)));
typedef float f32x2 __attribute__((ext_vector_type(2)));
typedef unsigned u32x4 __attribute__((ext_vector_type(4)));
typedef unsigned u32x2 __attribute__((ext_vector_type(2)));
typedef __bf16 bf2_t __attribute__((ext_vector_type(2)));

#define LDS_AS __attribute__((address_space(3)))

constexpr int S_ = 2048, D_ = 640, F_ = 1728, H_ = 32, DH_ = 20, NQKV = 1920;
constexpr int KS_O = D_ / 32;
constexpr int KS_H = F_ / 32;

constexpr size_t WS_Q = 0;
constexpr size_t QKV_BYTES = (size_t)H_ * S_ * DH_ * 2;
constexpr size_t WS_K = WS_Q + QKV_BYTES;
constexpr size_t WS_V = WS_K + QKV_BYTES;
constexpr size_t WS_H = WS_V + QKV_BYTES + 4096;
constexpr size_t WS_O = WS_H + (size_t)S_ * F_ * 2;
constexpr size_t WS_WO = WS_O + (size_t)S_ * D_ * 2;
constexpr size_t WS_WD = WS_WO + (size_t)D_ * D_ * 2;
constexpr size_t WS_G = WS_WD + (size_t)F_ * D_ * 2;
constexpr size_t WS_FLAG = WS_G + (size_t)S_ * 4;
constexpr int FLAG_LD = 4352;

__device__ __forceinline__ unsigned pack_bf16(float a, float b) {
    bf2_t v = {(__bf16)a, (__bf16)b};
    return __builtin_bit_cast(unsigned, v);
}
__device__ __forceinline__ float rbf(float x) { return (float)(__bf16)x; }
__device__ __forceinline__ float bf2f(u16 v) { return __uint_as_float(((unsigned)v) << 16); }

template <int CTRL> __device__ __forceinline__ float dpp_add(float x) {
    return x + __builtin_bit_cast(float, __builtin_amdgcn_update_dpp(0, __builtin_bit_cast(int, x), CTRL, 0xf, 0xf, false));
}
__device__ __forceinline__ float wave_sum(float x) {
    x = dpp_add<0xB1>(x); x = dpp_add<0x4E>(x); x = dpp_add<0x141>(x); x = dpp_add<0x140>(x);
    const int xi = __builtin_bit_cast(int, x);
    return (__builtin_bit_cast(float, __builtin_amdgcn_readlane(xi, 0)) + __builtin_bit_cast(float, __builtin_amdgcn_readlane(xi, 16))) +
           (__builtin_bit_cast(float, __builtin_amdgcn_readlane(xi, 32)) + __builtin_bit_cast(float, __builtin_amdgcn_readlane(xi, 48)));
}
__device__ __forceinline__ s16x4 tr_read(const char* lds_ptr) {
    return __builtin_amdgcn_ds_read_tr16_b64_v4i16((LDS_AS s16x4*)(lds_ptr));
}
__device__ __forceinline__ bf16x8 cat4(s16x4 a, s16x4 b) {
    bf16x8 r;
    r[0] = a[0]; r[1] = a[1]; r[2] = a[2]; r[3] = a[3];
    r[4] = b[0]; r[5] = b[1]; r[6] = b[2]; r[7] = b[3];
    return r;
}
__device__ __forceinline__ void glds16(const void* g, void* l) {
    __builtin_amdgcn_global_load_lds((const unsigned*)g, (LDS_AS unsigned*)l, 16, 0, 0);
}
__device__ __forceinline__ void glds16_asm(const void* gsrc, unsigned lds_dst) {
    unsigned keep;
    asm volatile("s_mov_b32 %0, m0\n\ts_mov_b32 m0, %2\n\ts_nop 0\n\tglobal_load_lds_dwordx4 %1, off\n\ts_mov_b32 m0, %0"
                 : "=&s"(keep) : "v"(gsrc), "s"(lds_dst) : "memory");
}
__device__ __forceinline__ void glds4_asm(const void* gsrc, unsigned lds_dst) {
    unsigned keep;
    asm volatile("s_mov_b32 %0, m0\n\ts_mov_b32 m0, %2\n\ts_nop 0\n\tglobal_load_lds_dword %1, off\n\ts_mov_b32 m0, %0"
                 : "=&s"(keep) : "v"(gsrc), "s"(lds_dst) : "memory");
}
__device__ __forceinline__ unsigned lds_addr(const void* p) { return (unsigned)(size_t)(LDS_AS const char*)p; }
typedef __amdgpu_buffer_rsrc_t rsrc_t;
constexpr int AUX_AGENT = 16;
__device__ __forceinline__ rsrc_t mk_rsrc(const void* p, unsigned bytes) { return __builtin_amdgcn_make_buffer_rsrc((void*)p, 0, (int)bytes, 0x00020000); }
template <int AUX> __device__ __forceinline__ void bst4(rsrc_t r, int voff, u32x4 v) { __builtin_amdgcn_raw_buffer_store_b128(v, r, voff, 0, AUX); }
template <int AUX> __device__ __forceinline__ void bst2(rsrc_t r, int voff, u32x2 v) { __builtin_amdgcn_raw_buffer_store_b64(v, r, voff, 0, AUX); }
#define WAIT_VM(n) asm volatile("s_waitcnt vmcnt(%0)" :: "n"(n) : "memory")
#define CBAR() asm volatile("" ::: "memory")
#define STAMP(kid, i) do {} while (0)
#define STAMPK(kid, i) do { if ((i) == 3) STAMP(kid, i); } while (0)

#define WPG 64
#define OSC 0
#define ESC
#define WSC1
#define K1SB 0
#define K1P0 0
#define K1UNR 1
constexpr int K1_ABYTES = 256 * 64 * 2;
constexpr int K1_BBYTES = 64 * 192 * 2;
constexpr int K1_BOFF = 3 * K1_ABYTES;
constexpr int K1_AUX = K1_BOFF + 2 * K1_BBYTES;
constexpr int K1_JUNK = K1_AUX + 3 * 4096;
constexpr int K1_LDS = K1_JUNK + 256;

template <int NJ>
__device__ __forceinline__ void k1_epilogue(f32x4 (&acc)[4][NJ], char* __restrict__ ws, char* smem, int m0, int c0, int nt) {
    int t = threadIdx.x;
    asm volatile("" : "+v"(t));
    const int lane = t & 63, wid = t >> 6;
    const int wr = wid >> 1, wc = wid & 1;
    const int fr = lane & 15, g = lane >> 4;
    const rsrc_t rw = mk_rsrc(ws, (unsigned)WS_FLAG);
    if (NJ == 5) {
        __syncthreads();
        const float qs = (nt < 4) ? 0.22360679774997896f * 1.4426950408889634f : 1.f;
#pragma unroll
        for (int j = 0; j < NJ; ++j) {
            const int n = wc * 80 + j * 16 + g * 4;
            const int hh = n / DH_, d = n % DH_;
            char* dst = smem + hh * 10240 + (wr * 64 + fr) * 40 + d * 2;
#pragma unroll
            for (int i = 0; i < 4; ++i) {
                u32x2 a = {pack_bf16(acc[i][j][0] * qs, acc[i][j][1] * qs), pack_bf16(acc[i][j][2] * qs, acc[i][j][3] * qs)};
                *(u32x2*)(dst + i * 640) = a;
            }
        }
        __syncthreads();
        const int which = nt >> 2;
        const int h0 = (nt & 3) * 8;
        char* dbase = ws + WS_Q + (size_t)which * QKV_BYTES + ((size_t)h0 * S_ + m0) * 40;
        const int dofs = (int)(WS_Q + (size_t)which * QKV_BYTES + ((size_t)h0 * S_ + m0) * 40);
#pragma unroll
        for (int it = 0; it < 10; ++it) {
            const int e = it * 512 + t;
            const int hh = e / 640, r = e % 640;
            const u32x4 v = *(const u32x4*)(smem + e * 16);
            bst4<AUX_AGENT>(rw, dofs + hh * (S_ * 40) + r * 16, v);
        }
    } else {
        u16* hF = (u16*)(ws + WS_H);
#pragma unroll
        for (int j = 0; j < 3; ++j) {
            const int f = c0 + wc * 48 + j * 16 + g * 4;
            const int ks = f >> 5;
            const int lq = (f & 31) >> 3;
            const int hf = f & 4;
#pragma unroll
            for (int i = 0; i < 4; ++i) {
                const int m = m0 + wr * 64 + i * 16 + fr;
                unsigned pk[2];
#pragma unroll
                for (int r = 0; r < 2; ++r) {
                    const f32x2 gt = {acc[i][j][2 * r], acc[i][j][2 * r + 1]};
                    const f32x2 up = {acc[i][(j + 3) % NJ][2 * r], acc[i][(j + 3) % NJ][2 * r + 1]};
                    f32x2 e = gt * -1.4426950408889634f;
                    e = (f32x2){__builtin_amdgcn_exp2f(e[0]), __builtin_amdgcn_exp2f(e[1])};
                    e = e + 1.f;
                    const f32x2 rc = {__builtin_amdgcn_rcpf(e[0]), __builtin_amdgcn_rcpf(e[1])};
                    const f32x2 hv = (gt * up) * rc;
                    pk[r] = pack_bf16(hv[0], hv[1]);
                }
                u32x2 a = {pk[0], pk[1]};
                u16* dst = hF + ((size_t)((m >> 4) * KS_H + ks) * 64 + (m & 15) + 16 * lq) * 8 + hf;
                bst2<AUX_AGENT>(rw, (int)WS_H + ((((m >> 4) * KS_H + ks) * 64 + (m & 15) + 16 * lq) * 8 + hf) * 2, a);
            }
        }
    }
}

template <int NJ>
__device__ __forceinline__ void k1_tile(const u16* __restrict__ X, const u16* __restrict__ W0, const u16* __restrict__ W1,
                                        char* __restrict__ ws, char* smem, int mt, int nt, int xs, int xcnt) {
    constexpr int BN = 32 * NJ;
    constexpr int ROWB = BN * 2;
    constexpr int UPR = BN / 8;
    constexpr int LDB = (NJ == 5) ? NQKV : F_;
    const int t = threadIdx.x;
    const int lane = t & 63, wid = t >> 6;
    const int m0 = mt * 256;
    const int c0 = (NJ == 5) ? nt * 160 : nt * 96;
    const int uw = __builtin_amdgcn_readfirstlane(wid);
    const unsigned sb0 = lds_addr(smem);

    if (uw == 10) {
        const u16* pp[3]; unsigned ps[3];
#pragma unroll
        for (int i = 0; i < 3; ++i) {
            constexpr int SPR = BN / 32;
            constexpr int NWS = 16 * SPR;
            const int xr = (256 + xcnt - 1) / xcnt;
            int e = i * 64 + lane;
            if (e < NWS) {
                const int kr = (mt & 3) * 16 + e / SPR, u = e % SPR;
                if (NJ == 5) pp[i] = W0 + (size_t)kr * LDB + c0 + u * 32;
                else pp[i] = ((u < 3) ? W0 : W1) + (size_t)kr * LDB + c0 + (u % 3) * 32;
                ps[i] = 64 * LDB;
            } else {
                e -= NWS;
                int r = xs * xr + (e >> 1);
                if ((e >> 1) >= xr || r > 255) { r = xs * xr; }
                pp[i] = X + (size_t)(m0 + r) * D_ + (e & 1) * 32;
                ps[i] = 64;
            }
        }
        const unsigned junk = sb0 + K1_JUNK;
        auto pf = [&](int kt) {
#pragma unroll
            for (int i = 0; i < 3; ++i) glds4_asm(pp[i] + (size_t)kt * ps[i], junk);
        };
        if (K1P0 <= 0) pf(0);
        if (K1P0 <= 1) pf(1);
        pf(2);
        if (K1SB) { WAIT_VM(6); __builtin_amdgcn_s_barrier(); }
        pf(3);
        for (int kt = 0; kt < 10; ++kt) {
            __builtin_amdgcn_s_barrier();
            if (kt + 4 < 10) pf(kt + 4);
        }
        WAIT_VM(0);
        return;
    }
    if (uw >= 8) {
        const int dw = uw - 8;
        constexpr int NBI = 2 * NJ;
        const u16* pa = X + (size_t)(m0 + dw * 128 + (lane >> 3)) * D_ + (((lane & 7) ^ (lane >> 3)) << 3);
        const u16* pb[NBI];
#pragma unroll
        for (int i = 0; i < NBI; ++i) {
            const int p = (dw * NBI + i) * 64 + lane;
            const int k = p / UPR, u = p % UPR;
            const int sw = (NJ == 5) ? ((k >> 3) & 1) : (((k >> 1) & 1) | (((k >> 3) & 1) << 1));
            const int c32 = (u >> 1) ^ sw, half = u & 1;
            if (NJ == 5) pb[i] = W0 + (size_t)k * LDB + c0 + c32 * 16 + half * 8;
            else pb[i] = ((c32 < 6) ? W0 : W1) + (size_t)k * LDB + c0 + (c32 < 6 ? c32 : c32 - 6) * 16 + half * 8;
        }
        auto loadA = [&](int kt) {
            const unsigned base = sb0 + (kt % 3) * K1_ABYTES + dw * 16384;
#pragma unroll
            for (int i = 0; i < 16; ++i) glds16_asm(pa + (size_t)i * 8 * D_ + kt * 64, base + i * 1024);
        };
        auto loadB = [&](int kt) {
            const unsigned base = sb0 + K1_BOFF + (kt & 1) * K1_BBYTES + dw * NBI * 1024;
#pragma unroll
            for (int i = 0; i < NBI; ++i) glds16_asm(pb[i] + (size_t)kt * 64 * LDB, base + i * 1024);
        };
        STAMP(0, 0);
        if (K1SB) __builtin_amdgcn_s_barrier();
        loadB(0); loadB(1); loadA(1);
        WAIT_VM(16 + NBI);
        for (int kt = 0; kt < 10; ++kt) {
            STAMPK(0, 2 + kt * 3);
            __builtin_amdgcn_s_barrier();
            STAMPK(0, 3 + kt * 3);
            if (kt > 0 && kt + 1 < 10) loadB(kt + 1);
            if (kt + 2 < 10) { loadA(kt + 2); STAMPK(0, 4 + kt * 3); WAIT_VM(16); } else { STAMPK(0, 4 + kt * 3); WAIT_VM(0); }
        }
        STAMP(0, 40); STAMP(0, 41); STAMP(0, 42);
        return;
    }

    STAMP(0, 0);
    if (K1SB) __builtin_amdgcn_s_barrier();
    {
        const u16* pa = X + (size_t)(m0 + uw * 32 + (lane >> 3)) * D_ + (((lane & 7) ^ (lane >> 3)) << 3);
#pragma unroll
        for (int i = 0; i < 4; ++i) glds16_asm(pa + (size_t)i * 8 * D_, sb0 + uw * 4096 + i * 1024);
    }
    const int wr = wid >> 1, wc = wid & 1;
    const int fr = lane & 15, g = lane >> 4;
    f32x4 acc[4][NJ];
#pragma unroll
    for (int i = 0; i < 4; ++i)
#pragma unroll
        for (int j = 0; j < NJ; ++j) acc[i][j] = (f32x4){0.f, 0.f, 0.f, 0.f};

    int aoff[2];
#pragma unroll
    for (int ks = 0; ks < 2; ++ks) {
        const int row = wr * 64 + fr;
        aoff[ks] = row * 128 + (((ks * 4 + g) ^ (row & 7)) << 4);
    }
    const int q4 = (lane & 15) >> 2, p4 = lane & 3;
    const int swl = (NJ == 5) ? (g & 1) : (((q4 >> 1) & 1) | ((g & 1) << 1));
    int boff[NJ];
#pragma unroll
    for (int j = 0; j < NJ; ++j) {
        const int c = (NJ == 5) ? (5 * wc + j) : (j < 3 ? 3 * wc + j : 6 + 3 * wc + (j - 3));
        boff[j] = K1_BOFF + (8 * g + q4) * ROWB + ((c ^ swl) << 5) + p4 * 8;
    }
    WAIT_VM(0);
    __builtin_amdgcn_s_barrier();
    CBAR();
    STAMP(0, 3);
    bf16x8 a0[4], a1[4], bq[NJ];
    auto rdB = [&](const char* baseB, int ks, int j) {
        s16x4 lo = tr_read(baseB + boff[j] + ks * (32 * ROWB));
        s16x4 hi = tr_read(baseB + boff[j] + ks * (32 * ROWB) + 4 * ROWB);
        return cat4(lo, hi);
    };
#pragma unroll
    for (int i = 0; i < 4; ++i) a0[i] = *(const bf16x8*)(smem + aoff[0] + i * 2048);
    __builtin_amdgcn_sched_barrier(0);
#pragma unroll
    for (int j = 0; j < NJ; ++j) { bq[j] = rdB(smem, 0, j); __builtin_amdgcn_sched_barrier(0); }
    int abuf = 0;
#pragma unroll 1
    for (int kt = 0; kt < 10; ++kt) {
        const char* baseA = smem + abuf * K1_ABYTES;
        const char* baseB = smem + (kt & 1) * K1_BBYTES;
        abuf = (abuf == 2) ? 0 : abuf + 1;
        const char* nextA = smem + abuf * K1_ABYTES;
        const char* nextB = smem + ((kt + 1) & 1) * K1_BBYTES;
#pragma unroll
        for (int i = 0; i < 4; ++i) a1[i] = *(const bf16x8*)(baseA + aoff[1] + i * 2048);
#pragma unroll
        for (int j = 0; j < NJ; ++j) {
#pragma unroll
            for (int i = 0; i < 4; ++i)
                acc[i][j] = __builtin_amdgcn_mfma_f32_16x16x32_bf16(bq[j], a0[i], acc[i][j], 0, 0, 0);
            bq[j] = rdB(baseB, 1, j);
            __builtin_amdgcn_sched_barrier(0);
        }
        asm volatile("s_waitcnt lgkmcnt(0)" ::: "memory");
        if (kt < 9) __builtin_amdgcn_s_barrier();
        CBAR();
#pragma unroll
        for (int i = 0; i < 4; ++i) a0[i] = *(const bf16x8*)(nextA + aoff[0] + i * 2048);
#pragma unroll
        for (int j = 0; j < NJ; ++j) {
#pragma unroll
            for (int i = 0; i < 4; ++i)
                acc[i][j] = __builtin_amdgcn_mfma_f32_16x16x32_bf16(bq[j], a1[i], acc[i][j], 0, 0, 0);
            bq[j] = rdB(nextB, 0, j);
            __builtin_amdgcn_sched_barrier(0);
        }
    }

    STAMP(0, 40);
    k1_epilogue<NJ>(acc, ws, smem, m0, c0, nt);
    STAMP(0, 41);
    STAMP(0, 42);
}

__device__ __forceinline__ void k1_aux(const u16* __restrict__ X, const u16* __restrict__ Wo, const u16* __restrict__ Wd,
                                       const u16* __restrict__ gate_w, char* __restrict__ ws, char* smem, int lin) {
    const int lane = threadIdx.x & 63;
    float* gout = (float*)(ws + WS_G);
    u16* woF = (u16*)(ws + WS_WO);
    u16* wdF = (u16*)(ws + WS_WD);
    const int q4 = (lane & 15) >> 2, p4 = lane & 3, g = lane >> 4;
    char* wl = smem + K1_AUX;
    const unsigned wla = lds_addr(wl);
    auto issue = [&](int unit, int slot) {
        const int ksg = unit / 10, cc = unit % 10;
        const u16* src = (ksg < KS_O) ? (Wo + (size_t)ksg * 32 * D_) : (Wd + (size_t)(ksg - KS_O) * 32 * D_);
#pragma unroll
        for (int i = 0; i < 4; ++i)
            glds16_asm(src + (size_t)(i * 8 + (lane >> 3)) * D_ + cc * 64 + (lane & 7) * 8, wla + slot * 4096 + i * 1024);
    };
    auto emit = [&](int unit, int slot) {
        const int ksg = unit / 10, cc = unit % 10;
#pragma unroll
        for (int j = 0; j < 4; ++j) {
            const char* a = wl + slot * 4096 + (8 * g + q4) * 128 + j * 32 + p4 * 8;
            s16x4 lo = tr_read(a);
            s16x4 hi = tr_read(a + 4 * 128);
            bf16x8 v = cat4(lo, hi);
            const int nt = cc * 4 + j;
            u16* dst = (ksg < KS_O) ? (woF + ((size_t)(nt * KS_O + ksg) * 64 + lane) * 8)
                                    : (wdF + ((size_t)(nt * KS_H + (ksg - KS_O)) * 64 + lane) * 8);
            bst4<AUX_AGENT>(mk_rsrc(ws, (unsigned)WS_FLAG), (int)((char*)dst - ws), __builtin_bit_cast(u32x4, v));
        }
    };
    if (K1SB) __builtin_amdgcn_s_barrier();
    __builtin_amdgcn_s_barrier();
    issue(lin, 0); issue(lin + 240, 1); issue(lin + 480, 2);
    __builtin_amdgcn_s_barrier();
    __builtin_amdgcn_s_barrier();
    WAIT_VM(0);
    emit(lin, 0); emit(lin + 240, 1); emit(lin + 480, 2);
    asm volatile("s_waitcnt lgkmcnt(0)" ::: "memory");
    const bool extra = lin + 720 < 740;
    if (extra) issue(lin + 720, 0);
    __builtin_amdgcn_s_barrier();
    const u32x4 w0 = *(const u32x4*)(gate_w + lane * 8);
    u32x4 w1 = *(const u32x4*)(gate_w + 512 + (lane & 15) * 8);
    u32x4 x0[9], x1[9];
#pragma unroll
    for (int r = 0; r < 9; ++r) {
        int row = lin + 240 * r;
        row = row < S_ ? row : lin;
        x0[r] = *(const u32x4*)(X + (size_t)row * D_ + lane * 8);
        x1[r] = *(const u32x4*)(X + (size_t)row * D_ + 512 + (lane & 15) * 8);
    }
    CBAR();
    __builtin_amdgcn_s_barrier();
    __builtin_amdgcn_s_barrier();
    CBAR();
    if (lane >= 16) w1 = (u32x4){0u, 0u, 0u, 0u};
    float sacc[9];
#pragma unroll
    for (int r = 0; r < 9; ++r) {
        float a = 0.f;
#pragma unroll
        for (int e = 0; e < 4; ++e) {
            const unsigned xa = x0[r][e], wa = w0[e], xb = x1[r][e], wb = w1[e];
            a = __builtin_amdgcn_fdot2_f32_bf16(__builtin_bit_cast(bf2_t, xa), __builtin_bit_cast(bf2_t, wa), a, false);
            a = __builtin_amdgcn_fdot2_f32_bf16(__builtin_bit_cast(bf2_t, xb), __builtin_bit_cast(bf2_t, wb), a, false);
        }
        sacc[r] = a;
    }
    __builtin_amdgcn_s_barrier();
    {
        float z = 0.f;
#pragma unroll
        for (int r = 0; r < 9; ++r) {
            const float tot = wave_sum(sacc[r]);
            z = (lane == r) ? tot : z;
        }
        const int row = lin + 240 * lane;
        z = rbf(z);
        if (lane < 9 && row < S_) gout[row] = rbf(1.f / (1.f + __expf(-z)));
    }
    __builtin_amdgcn_s_barrier();
    WAIT_VM(0);
    if (extra) emit(lin + 720, 0);
    __builtin_amdgcn_s_barrier();
    __builtin_amdgcn_s_barrier();
}

__global__ __launch_bounds__(768) void k1_gemm(const u16* __restrict__ X, const u16* __restrict__ Wqkv,
                                               const u16* __restrict__ Wg, const u16* __restrict__ Wu,
                                               const u16* __restrict__ Wo, const u16* __restrict__ Wd,
                                               const u16* __restrict__ gate_w, char* __restrict__ ws) {
    __shared__ __attribute__((aligned(16))) char smem[K1_LDS];
    const int bid = blockIdx.x;
    const int xcd = bid & 7, idx = bid >> 3;
    const int ng = xcd >> 1, mg = xcd & 1;
    const int cnt = (ng < 2) ? 8 : 7;
    const int nstart = (ng < 2) ? ng * 8 : 16 + (ng - 2) * 7;
    if (idx >= 4 * cnt) {
        if (threadIdx.x < 32)
            *(u32x4*)(ws + WS_FLAG + (size_t)(((xcd - 4) * 4 + (idx - 28)) * 2 + (threadIdx.x >> 4)) * FLAG_LD + (threadIdx.x & 15) * 16) = (u32x4){0u, 0u, 0u, 0u};
        return;
    }
    if (__builtin_amdgcn_readfirstlane(threadIdx.x >> 6) == 11) {
        const int lin = (ng < 2) ? xcd * 32 + idx : 128 + (xcd - 4) * 28 + idx;
        k1_aux(X, Wo, Wd, gate_w, ws, smem, lin);
        return;
    }
    const int mt = mg * 4 + (idx & 3);
    const int nt = nstart + (idx >> 2);
    if (nt < 12) k1_tile<5>(X, Wqkv, Wqkv, ws, smem, mt, nt, idx >> 2, cnt);
    else k1_tile<6>(X, Wg, Wu, ws, smem, mt, nt - 12, idx >> 2, cnt);
}

typedef unsigned long long u64;
#define GDELAY 4
#define PF2 0
#define ACQ 0
#define ROT 0
#define ROLL 0
#define GPRIO 0
#define POLLSLP 4
#define APRIO 2
#define ST2(i) do {} while (0)
#define ST2ID() do {} while (0)
__device__ __forceinline__ void st_agent(void* p, u32x2 v) {
    __hip_atomic_store((u64*)p, __builtin_bit_cast(u64, v), __ATOMIC_RELAXED, __HIP_MEMORY_SCOPE_AGENT);
}
template <int AUX> __device__ __forceinline__ bf16x8 bld(rsrc_t r, int voff, int soff) {
    return __builtin_bit_cast(bf16x8, __builtin_amdgcn_raw_buffer_load_b128(r, voff, soff, AUX));
}

__device__ __forceinline__ float fmx(float a, float b) { return __builtin_elementwise_maximum(a, b); }

template <bool EDGE>
__device__ __forceinline__ void k2_attn_wave(char* __restrict__ oF, const char* sK, const char* sV, const bf16x8 (&qf)[2],
                                             const float (&gg)[2], int h, int qw, int rbase, int lane) {
    const int fr = lane & 15, g = lane >> 4;
    const int q4 = (lane & 15) >> 2, p4 = lane & 3;
    const float NEG = -__builtin_inff();
    const bf16x8 ones = {0x3f80, 0x3f80, 0x3f80, 0x3f80, 0x3f80, 0x3f80, 0x3f80, 0x3f80};
    const int ts = 16 - (qw >> 4);
    const char* kb = sK + (rbase + fr) * 40 + (g < 3 ? g : 2) * 16;
    const char* vb = sV + (rbase + 4 * g + q4) * 40 + p4 * 8;
    f32x4 cLo, cHi;
#pragma unroll
    for (int r = 0; r < 4; ++r) {
        cLo[r] = (fr >= 4 * g + r) ? NEG : 0.f;
        cHi[r] = (4 * g + r > fr) ? NEG : 0.f;
    }
    auto rdK = [&](int tt) {
        const u32x2 klo = *(const u32x2*)(kb + tt * 640), khi = *(const u32x2*)(kb + tt * 640 + 8);
        const u32x4 kv = {klo[0], klo[1], khi[0], khi[1]};
        return __builtin_bit_cast(bf16x8, kv);
    };
    const f32x4 zero = {0.f, 0.f, 0.f, 0.f};
#define SB0() __builtin_amdgcn_sched_barrier(0)
    float mx[2] = {NEG, NEG};
    {
        bf16x8 kf[3];
        f32x4 s[2][2];
        kf[0] = rdK(0); kf[1] = rdK(1);
#pragma unroll
        for (int tt = 0; tt < 19; ++tt) {
            if (tt + 2 < 18) kf[(tt + 2) % 3] = rdK(tt + 2);
#pragma unroll
            for (int qt = 0; qt < 2; ++qt) {
                const int u = tt - qt;
                if (tt < 18 && u >= 0 && u <= 16)
                    s[tt & 1][qt] = __builtin_amdgcn_mfma_f32_16x16x32_bf16(kf[tt % 3], qf[qt], u == 0 ? cLo : (u == 16 ? cHi : zero), 0, 0, 0);
                SB0();
                const int tp = tt - 1, up = tp - qt;
                if (tp >= 0 && up >= 0 && up <= 16) {
                    const f32x4 p = s[tp & 1][qt];
                    if (EDGE && tp < 16) {
                        const float m = fmx(fmx(p[0], p[1]), fmx(p[2], p[3]));
                        mx[qt] = fmx(mx[qt], (tp < ts) ? NEG : m);
                    } else {
                        mx[qt] = fmx(fmx(mx[qt], p[0]), p[1]);
                        mx[qt] = fmx(fmx(mx[qt], p[2]), p[3]);
                    }
                }
                SB0();
            }
        }
    }
    CBAR();
    bf16x8 kf[2][2], vf[2][2];
    auto rdV = [&](int pp, bf16x8 (&v)[2]) {
#pragma unroll
        for (int dt = 0; dt < 2; ++dt) {
            s16x4 lo = tr_read(vb + (2 * pp) * 640 + dt * 32);
            s16x4 hi = tr_read(vb + (2 * pp + 1) * 640 + dt * 32);
            v[dt] = cat4(lo, hi);
        }
    };
    kf[0][0] = rdK(0); kf[0][1] = rdK(1);
    kf[1][0] = rdK(2); kf[1][1] = rdK(3);
    rdV(0, vf[0]);
    f32x4 cm[2], cl[2], ch[2];
#pragma unroll
    for (int qt = 0; qt < 2; ++qt) {
        float m = mx[qt];
        {
            const unsigned mi = __builtin_bit_cast(unsigned, m);
            auto r = __builtin_amdgcn_permlane32_swap(mi, mi, false, false);
            m = fmx(__builtin_bit_cast(float, (unsigned)r[0]), __builtin_bit_cast(float, (unsigned)r[1]));
        }
        {
            const unsigned mi = __builtin_bit_cast(unsigned, m);
            auto r = __builtin_amdgcn_permlane16_swap(mi, mi, false, false);
            m = fmx(__builtin_bit_cast(float, (unsigned)r[0]), __builtin_bit_cast(float, (unsigned)r[1]));
        }
        m = -m;
        cm[qt] = (f32x4){m, m, m, m};
#pragma unroll
        for (int r = 0; r < 4; ++r) {
            cl[qt][r] = (fr >= 4 * g + r) ? NEG : m;
            ch[qt][r] = (4 * g + r > fr) ? NEG : m;
        }
    }
    f32x4 oacc[2][3];
#pragma unroll
    for (int qt = 0; qt < 2; ++qt)
#pragma unroll
        for (int dt = 0; dt < 3; ++dt) oacc[qt][dt] = zero;
    f32x4 s[2][2][2];
    u32x4 pk[2];
    auto valid = [](int tt, int qt) { return tt - qt >= 0 && tt - qt <= 16; };
    auto QK = [&](int pp, int hh, int qt) {
        const int tt = 2 * pp + hh, u = tt - qt;
        if (pp < 9 && valid(tt, qt))
            s[pp & 1][hh][qt] = __builtin_amdgcn_mfma_f32_16x16x32_bf16(kf[pp & 1][hh], qf[qt], u == 0 ? cl[qt] : (u == 16 ? ch[qt] : cm[qt]), 0, 0, 0);
        SB0();
    };
    auto EX = [&](int pp, int hh, int qt, int half) {
        const int tt = 2 * pp + hh;
        if (pp < 9) {
            unsigned v = 0u;
            if (valid(tt, qt)) {
                const f32x4 x = s[pp & 1][hh][qt];
                v = pack_bf16(__builtin_amdgcn_exp2f(x[2 * half]), __builtin_amdgcn_exp2f(x[2 * half + 1]));
                if (EDGE && tt < 16) v = (tt < ts) ? 0u : v;
            }
            pk[qt][2 * hh + half] = v;
        }
        SB0();
    };
    auto PV = [&](int pp, int qt, int dt) {
        if (pp >= 0 && pp < 9)
            oacc[qt][dt] = __builtin_amdgcn_mfma_f32_16x16x32_bf16(dt < 2 ? vf[pp & 1][dt] : ones, __builtin_bit_cast(bf16x8, pk[qt]), oacc[qt][dt], 0, 0, 0);
        SB0();
    };
    SB0();
    QK(0, 0, 0); QK(0, 1, 0); QK(0, 0, 1); QK(0, 1, 1);
#pragma unroll
    for (int pp = 0; pp < 10; ++pp) {
        PV(pp - 1, 1, 0); EX(pp, 0, 0, 0);
        PV(pp - 1, 1, 1); EX(pp, 0, 0, 1);
        PV(pp - 1, 1, 2);
        if (pp + 1 < 9) rdV(pp + 1, vf[(pp + 1) & 1]);
        QK(pp + 1, 0, 0); EX(pp, 1, 0, 0);
        QK(pp + 1, 1, 0); EX(pp, 1, 0, 1);
        PV(pp, 0, 0); EX(pp, 0, 1, 0);
        PV(pp, 0, 1); EX(pp, 0, 1, 1);
        PV(pp, 0, 2);
        QK(pp + 1, 0, 1); EX(pp, 1, 1, 0);
        QK(pp + 1, 1, 1);
        if (pp + 2 < 9) { kf[pp & 1][0] = rdK(2 * pp + 4); kf[pp & 1][1] = rdK(2 * pp + 5); }
        EX(pp, 1, 1, 1);
    }
#pragma unroll
    for (int qt = 0; qt < 2; ++qt) {
        const float inv = gg[qt] / oacc[qt][2][0];
        const int m = qw + 16 * qt + fr;
        {
            const int k0 = h * DH_ + 4 * g;
            u32x2 a = {pack_bf16(oacc[qt][0][0] * inv, oacc[qt][0][1] * inv), pack_bf16(oacc[qt][0][2] * inv, oacc[qt][0][3] * inv)};
            st_agent(oF + ((size_t)((m >> 4) * KS_O + (k0 >> 5)) * 64 + (m & 15) + 16 * ((k0 & 31) >> 3)) * 16 + (k0 & 4) * 2, a);
        }
        if (g == 0) {
            const int k0 = h * DH_ + 16;
            u32x2 a = {pack_bf16(oacc[qt][1][0] * inv, oacc[qt][1][1] * inv), pack_bf16(oacc[qt][1][2] * inv, oacc[qt][1][3] * inv)};
            st_agent(oF + ((size_t)((m >> 4) * KS_O + (k0 >> 5)) * 64 + (m & 15) + 16 * ((k0 & 31) >> 3)) * 16 + (k0 & 4) * 2, a);
        }
    }
}

__device__ __forceinline__ void k2_attn(char* __restrict__ ws, char* smem, int bid) {
    const int h = (bid & 7) + 8 * ((bid >> 3) & 3);
    const int qb = bid >> 5;
    const int q0 = qb * 128;
    const int t = threadIdx.x, lane = t & 63;
    const int w = __builtin_amdgcn_readfirstlane(t >> 6);
    const char* Qb = ws + WS_Q + (size_t)h * S_ * 40;
    const char* Kb = ws + WS_K + (size_t)h * S_ * 40;
    const char* Vb = ws + WS_V + (size_t)h * S_ * 40;
    char* sK = smem;
    char* sV = smem + 15360;

    ST2(0); ST2ID();
    if (APRIO) __builtin_amdgcn_s_setprio(APRIO);
#pragma unroll
    for (int i = 0; i < 4; ++i) {
        const int p = i * 256 + t;
        if (i < 3 || w < 3) {
            int off = (q0 - 256) * 40 + p * 16;
            off = off < 0 ? 0 : off;
            glds16(Kb + off, sK + p * 16);
            glds16(Vb + off, sV + p * 16);
        }
    }
    if (t < 4) *(u32x4*)(smem + 2 * 15360 + t * 16) = (u32x4){0u, 0u, 0u, 0u};
    const int fr = lane & 15, g = lane >> 4;
    const int qw = q0 + 32 * w;
    bf16x8 qf[2];
    float gg[2];
#pragma unroll
    for (int qt = 0; qt < 2; ++qt) {
        const char* qp = Qb + (size_t)(qw + qt * 16 + fr) * 40 + (g < 3 ? g : 2) * 16;
        u32x2 lo = *(const u32x2*)qp;
        u32x2 hi = *(const u32x2*)(qp + (g < 2 ? 8 : 0));
        if (g >= 2) hi = (u32x2){0u, 0u};
        if (g == 3) lo = (u32x2){0u, 0u};
        u32x4 v = {lo[0], lo[1], hi[0], hi[1]};
        qf[qt] = __builtin_bit_cast(bf16x8, v);
        gg[qt] = ((const float*)(ws + WS_G))[qw + qt * 16 + fr];
    }
    __syncthreads();
    ST2(1);
    char* oF = ws + WS_O;
    if (qb >= 2) k2_attn_wave<false>(oF, sK, sV, qf, gg, h, qw, 32 * w, lane);
    else k2_attn_wave<true>(oF, sK, sV, qf, gg, h, qw, 32 * w, lane);
    ST2(2);
    asm volatile("s_waitcnt vmcnt(0)" ::: "memory");
    ST2(3);
    if (lane == 0)
        __hip_atomic_store((unsigned*)(ws + WS_FLAG + (qw >> 6) * FLAG_LD) + h * 2 + ((qw >> 5) & 1), 1u, __ATOMIC_RELAXED, __HIP_MEMORY_SCOPE_AGENT);
}

constexpr int RED_LD = 84;
__device__ __forceinline__ void k2_gemm(char* __restrict__ ws, u16* __restrict__ out, char* smem, int bid) {
    float* red = (float*)smem;
    const int xcd = bid & 7, idx = bid >> 3;
    const int mt = (xcd >> 1) * 8 + (idx & 7);
    const int nt = (xcd & 1) * 4 + (idx >> 3);
    const int t = threadIdx.x, lane = t & 63;
    const int w = __builtin_amdgcn_readfirstlane(t >> 6);
    const int fr = lane & 15, g = lane >> 4;
    const int nsteps = (w < 3) ? 14 : 12;
    const rsrc_t rs = __builtin_amdgcn_make_buffer_rsrc(ws, 0, (int)WS_FLAG, 0x00020000);
    const int vo = lane * 16;
    int cur = ROT ? (((idx & 7) + 2 * (idx >> 3)) & 7) * nsteps >> 3 : 0;
    const int ap = (int)WS_H + ((mt * 4) * KS_H + 14 * w) * 1024;
    const int bp = (int)WS_WD + ((nt * 5) * KS_H + 14 * w) * 1024;

    f32x4 acc[4][5];
#pragma unroll
    for (int i = 0; i < 4; ++i)
#pragma unroll
        for (int j = 0; j < 5; ++j) acc[i][j] = (f32x4){0.f, 0.f, 0.f, 0.f};
    bf16x8 a0[4], b0[5], a1[4], b1[5];
    auto ld = [&](bf16x8 (&a)[4], bf16x8 (&b)[5]) {
#pragma unroll
        for (int i = 0; i < 4; ++i) a[i] = bld<0>(rs, vo, ap + (i * KS_H + cur) * 1024);
#pragma unroll
        for (int j = 0; j < 5; ++j) b[j] = bld<0>(rs, vo, bp + (j * KS_H + cur) * 1024);
        cur = (cur + 1 == nsteps) ? 0 : cur + 1;
    };
    auto mm = [&](bf16x8 (&a)[4], bf16x8 (&b)[5]) {
#pragma unroll
        for (int i = 0; i < 4; ++i)
#pragma unroll
            for (int j = 0; j < 5; ++j)
                acc[i][j] = __builtin_amdgcn_mfma_f32_16x16x32_bf16(b[j], a[i], acc[i][j], 0, 0, 0);
    };
    ST2(0); ST2ID();
    if (GPRIO) __builtin_amdgcn_s_setprio(GPRIO);
#pragma unroll 1
    for (int i = 0; i < GDELAY; ++i) __builtin_amdgcn_s_sleep(8);
    ld(a0, b0);
    ld(a1, b1);
    const float gl = ((const float*)(ws + WS_G))[mt * 64 + lane];
    auto mmld = [&](bf16x8 (&a)[4], bf16x8 (&b)[5]) {
#pragma unroll
        for (int j = 0; j < 5; ++j) {
#pragma unroll
            for (int i = 0; i < 4; ++i) {
                acc[i][j] = __builtin_amdgcn_mfma_f32_16x16x32_bf16(b[j], a[i], acc[i][j], 0, 0, 0);
                if (j == 4) { __builtin_amdgcn_sched_barrier(0); a[i] = bld<0>(rs, vo, ap + (i * KS_H + cur) * 1024); __builtin_amdgcn_sched_barrier(0); }
            }
            __builtin_amdgcn_sched_barrier(0);
            b[j] = bld<0>(rs, vo, bp + (j * KS_H + cur) * 1024);
            __builtin_amdgcn_sched_barrier(0);
        }
        cur = (cur + 1 == nsteps) ? 0 : cur + 1;
    };
#pragma unroll 1
    for (int s = 0; s < nsteps - 2; s += 2) {
        if (ROLL) { mmld(a0, b0); mmld(a1, b1); }
        else {
            mm(a0, b0); ld(a0, b0);
            mm(a1, b1); ld(a1, b1);
        }
    }
    ST2(1);
    const int op = (int)WS_O + ((mt * 4) * KS_O + 5 * w) * 1024;
    const int wp = (int)WS_WO + ((nt * 5) * KS_O + 5 * w) * 1024;
    auto ldw = [&](bf16x8 (&b)[5], int s) {
#pragma unroll
        for (int j = 0; j < 5; ++j) b[j] = bld<0>(rs, vo, wp + (j * KS_O + s) * 1024);
    };
    auto ldo = [&](bf16x8 (&a)[4], int s) {
#pragma unroll
        for (int i = 0; i < 4; ++i) a[i] = bld<ACQ ? 0 : AUX_AGENT>(rs, vo, op + (i * KS_O + s) * 1024);
    };
#define SB() __builtin_amdgcn_sched_barrier(0)
    SB(); mm(a0, b0); SB(); ldw(b0, 0);
    SB(); mm(a1, b1); SB(); ldw(b1, 1);
    SB();
#pragma unroll
    for (int i = 0; i < 4; ++i) {
        const float sc = rbf(1.f - __builtin_bit_cast(float, __builtin_amdgcn_ds_bpermute((i * 16 + fr) * 4, __builtin_bit_cast(int, gl))));
#pragma unroll
        for (int j = 0; j < 5; ++j) acc[i][j] = acc[i][j] * sc;
        asm volatile("" : "+v"(acc[i][0]), "+v"(acc[i][1]), "+v"(acc[i][2]), "+v"(acc[i][3]), "+v"(acc[i][4]));
    }
    ST2(2);
    {
        if (w == 0) {
            const unsigned* fp = (const unsigned*)(ws + WS_FLAG + mt * FLAG_LD) + lane;
            while (true) {
                const unsigned f = __hip_atomic_load(fp, __ATOMIC_RELAXED, __HIP_MEMORY_SCOPE_AGENT);
                if (__builtin_amdgcn_ballot_w64(f != 1u) == 0ull) break;
                __builtin_amdgcn_s_sleep(POLLSLP);
            }
        }
        asm volatile("" ::: "memory");
        __builtin_amdgcn_s_barrier();
        asm volatile("" ::: "memory");
    }
    ST2(3);
    ldo(a0, 0);
    ldo(a1, 1);
    SB(); mm(a0, b0); SB(); ldo(a0, 2); ldw(b0, 2);
    SB(); mm(a1, b1); SB(); ldo(a1, 3); ldw(b1, 3);
    SB(); mm(a0, b0); SB(); ldo(a0, 4); ldw(b0, 4);
    SB(); mm(a1, b1);
    SB(); mm(a0, b0);
    SB();
    ST2(4);
    float* rw = red + (w & 1) * 64 * RED_LD + fr * RED_LD + g * 4;
    if (w >= 2) {
#pragma unroll
        for (int i = 0; i < 4; ++i)
#pragma unroll
            for (int j = 0; j < 5; ++j) *(f32x4*)(rw + i * 16 * RED_LD + j * 16) = acc[i][j];
    }
    __syncthreads();
    if (w < 2) {
#pragma unroll
        for (int i = 0; i < 4; ++i)
#pragma unroll
            for (int j = 0; j < 5; ++j) {
                float* q = rw + i * 16 * RED_LD + j * 16;
                *(f32x4*)q = acc[i][j] + *(const f32x4*)q;
            }
    }
    __syncthreads();
    {
        const int m = t >> 2, c0 = (t & 3) * 20;
        u16* dst = out + (size_t)(mt * 64 + m) * D_ + nt * 80 + c0;
#pragma unroll
        for (int c = 0; c < 20; c += 4) {
            const f32x4 v = *(const f32x4*)(red + m * RED_LD + c0 + c) + *(const f32x4*)(red + (64 + m) * RED_LD + c0 + c);
            u32x2 pk = {pack_bf16(v[0], v[1]), pack_bf16(v[2], v[3])};
            *(u32x2*)(dst + c) = pk;
        }
    }
    ST2(5);
}

__global__ __launch_bounds__(256, 3) void k2_mix(char* __restrict__ ws, u16* __restrict__ out) {
    __shared__ __attribute__((aligned(16))) char smem[2 * 64 * RED_LD * 4];
    if (blockIdx.x < 256) k2_gemm(ws, out, smem, blockIdx.x);
    else k2_attn(ws, smem, blockIdx.x - 256);
}

extern "C" void kernel_launch(void* const* d_in, const int* in_sizes, int n_in,
                              void* d_out, int out_size, void* d_ws, size_t ws_size,
                              hipStream_t stream) {
    const u16* X = (const u16*)d_in[0];
    const u16* Wqkv = (const u16*)d_in[1];
    const u16* Wo = (const u16*)d_in[2];
    const u16* Wg = (const u16*)d_in[4];
    const u16* Wu = (const u16*)d_in[5];
    const u16* Wd = (const u16*)d_in[6];
    const u16* gw = (const u16*)d_in[7];
    char* ws = (char*)d_ws;
    k1_gemm<<<256, 768, 0, stream>>>(X, Wqkv, Wg, Wu, Wo, Wd, gw, ws);
    k2_mix<<<768, 256, 0, stream>>>(ws, (u16*)d_out);
}
```

```cpp
#include <hip/hip_runtime.h>
#include <hip/hip_bf16.h>
#include <stdint.h>

typedef unsigned short u16;
typedef short bf16x8 __attribute__((ext_vector_type(8)));
typedef short s16x4 __attribute__((ext_vector_type(4)));
typedef float f32x4 __attribute__((ext_vector_type(4)));
typedef float f32x2 __attribute__((ext_vector_type(2)));
typedef unsigned u32x4 __attribute__((ext_vector_type(4)));
typedef unsigned u32x2 __attribute__((ext_vector_type(2)));
typedef __bf16 bf2_t __attribute__((ext_vector_type(2)));

#define LDS_AS __attribute__((address_space(3)))

constexpr int S_ = 2048, D_ = 640, F_ = 1728, H_ = 32, DH_ = 20, NQKV = 1920;
constexpr int KS_O = D_ / 32;
constexpr int KS_H = F_ / 32;

constexpr size_t WS_Q = 0;
constexpr size_t QKV_BYTES = (size_t)H_ * S_ * DH_ * 2;
constexpr size_t WS_K = WS_Q + QKV_BYTES;
constexpr size_t WS_V = WS_K + QKV_BYTES;
constexpr size_t WS_H = WS_V + QKV_BYTES + 4096;
constexpr size_t WS_O = WS_H + (size_t)S_ * F_ * 2;
constexpr size_t WS_WO = WS_O + (size_t)S_ * D_ * 2;
constexpr size_t WS_WD = WS_WO + (size_t)D_ * D_ * 2;
constexpr size_t WS_G = WS_WD + (size_t)F_ * D_ * 2;
constexpr size_t WS_FLAG = WS_G + (size_t)S_ * 4;
constexpr int FLAG_LD = 4352;

__device__ __forceinline__ unsigned pack_bf16(float a, float b) {
    bf2_t v = {(__bf16)a, (__bf16)b};
    return __builtin_bit_cast(unsigned, v);
}
__device__ __forceinline__ float rbf(float x) { return (float)(__bf16)x; }
__device__ __forceinline__ float bf2f(u16 v) { return __uint_as_float(((unsigned)v) << 16); }

template <int CTRL> __device__ __forceinline__ float dpp_add(float x) {
    return x + __builtin_bit_cast(float, __builtin_amdgcn_update_dpp(0, __builtin_bit_cast(int, x), CTRL, 0xf, 0xf, false));
}
__device__ __forceinline__ float wave_sum(float x) {
    x = dpp_add<0xB1>(x); x = dpp_add<0x4E>(x); x = dpp_add<0x141>(x); x = dpp_add<0x140>(x);
    const int xi = __builtin_bit_cast(int, x);
    return (__builtin_bit_cast(float, __builtin_amdgcn_readlane(xi, 0)) + __builtin_bit_cast(float, __builtin_amdgcn_readlane(xi, 16))) +
           (__builtin_bit_cast(float, __builtin_amdgcn_readlane(xi, 32)) + __builtin_bit_cast(float, __builtin_amdgcn_readlane(xi, 48)));
}
__device__ __forceinline__ s16x4 tr_read(const char* lds_ptr) {
    return __builtin_amdgcn_ds_read_tr16_b64_v4i16((LDS_AS s16x4*)(lds_ptr));
}
__device__ __forceinline__ bf16x8 cat4(s16x4 a, s16x4 b) {
    bf16x8 r;
    r[0] = a[0]; r[1] = a[1]; r[2] = a[2]; r[3] = a[3];
    r[4] = b[0]; r[5] = b[1]; r[6] = b[2]; r[7] = b[3];
    return r;
}
__device__ __forceinline__ void glds16(const void* g, void* l) {
    __builtin_amdgcn_global_load_lds((const unsigned*)g, (LDS_AS unsigned*)l, 16, 0, 0);
}
__device__ __forceinline__ void glds16_asm(const void* gsrc, unsigned lds_dst) {
    unsigned keep;
    asm volatile("s_mov_b32 %0, m0\n\ts_mov_b32 m0, %2\n\ts_nop 0\n\tglobal_load_lds_dwordx4 %1, off\n\ts_mov_b32 m0, %0"
                 : "=&s"(keep) : "v"(gsrc), "s"(lds_dst) : "memory");
}
__device__ __forceinline__ void glds4_asm(const void* gsrc, unsigned lds_dst) {
    unsigned keep;
    asm volatile("s_mov_b32 %0, m0\n\ts_mov_b32 m0, %2\n\ts_nop 0\n\tglobal_load_lds_dword %1, off\n\ts_mov_b32 m0, %0"
                 : "=&s"(keep) : "v"(gsrc), "s"(lds_dst) : "memory");
}
__device__ __forceinline__ unsigned lds_addr(const void* p) { return (unsigned)(size_t)(LDS_AS const char*)p; }
typedef __amdgpu_buffer_rsrc_t rsrc_t;
constexpr int AUX_AGENT = 16;
__device__ __forceinline__ rsrc_t mk_rsrc(const void* p, unsigned bytes) { return __builtin_amdgcn_make_buffer_rsrc((void*)p, 0, (int)bytes, 0x00020000); }
template <int AUX> __device__ __forceinline__ void bst4(rsrc_t r, int voff, u32x4 v) { __builtin_amdgcn_raw_buffer_store_b128(v, r, voff, 0, AUX); }
template <int AUX> __device__ __forceinline__ void bst2(rsrc_t r, int voff, u32x2 v) { __builtin_amdgcn_raw_buffer_store_b64(v, r, voff, 0, AUX); }
#define WAIT_VM(n) asm volatile("s_waitcnt vmcnt(%0)" :: "n"(n) : "memory")
#define CBAR() asm volatile("" ::: "memory")
#define STAMP(kid, i) do {} while (0)
#define STAMPK(kid, i) do { if ((i) == 3) STAMP(kid, i); } while (0)

#define ET(i) do {} while (0)
#define ETW() do {} while (0)
#define ENTARG
#define ENTPASS
#define WPG 64
#define OSC 0
#define ESC
#define WSC1
#define K1SB 0
#define K1P0 0
#define K1UNR 1
constexpr int K1_ABYTES = 256 * 64 * 2;
constexpr int K1_BSLOT = 32 * 192 * 2;
constexpr int K1_BOFF = 3 * K1_ABYTES;
constexpr int K1_AUX = K1_BOFF + 5 * K1_BSLOT;
constexpr int K1_FLG = K1_AUX + 2048;
constexpr int K1_LDS = K1_FLG + 96;
#define K1FJ 2
#define K1FS 6
#define PFL 4
#define MF1(c, b, a) c = __builtin_amdgcn_mfma_f32_16x16x32_bf16(b, a, c, 0, 0, 0)
typedef volatile LDS_AS unsigned* lflag_t;
template <int SLP> __device__ __forceinline__ void k1_wait_rel(unsigned sb0, int lane, unsigned n) {
    lflag_t p = (lflag_t)(size_t)(sb0 + K1_FLG + (lane & 7) * 4);
    while (__builtin_amdgcn_ballot_w64(*p < n) != 0ull) { if (SLP) __builtin_amdgcn_s_sleep(SLP); }
    CBAR();
}
__device__ __forceinline__ void glds_x4(unsigned lds, const void* sb, unsigned v0, unsigned v1, unsigned v2, unsigned v3) {
    unsigned keep;
    asm volatile("s_mov_b32 %0, m0\n\ts_mov_b32 m0, %1\n\ts_nop 0\n\t"
                 "global_load_lds_dwordx4 %3, %2\n\t"
                 "global_load_lds_dwordx4 %4, %2 offset:1024\n\t"
                 "global_load_lds_dwordx4 %5, %2 offset:2048\n\t"
                 "global_load_lds_dwordx4 %6, %2 offset:3072\n\t"
                 "s_mov_b32 m0, %0"
                 : "=&s"(keep) : "s"(lds), "s"(sb), "v"(v0), "v"(v1), "v"(v2), "v"(v3) : "memory");
}
__device__ __forceinline__ void glds_x2(unsigned lds, const void* sb, unsigned v0, unsigned v1) {
    unsigned keep;
    asm volatile("s_mov_b32 %0, m0\n\ts_mov_b32 m0, %1\n\ts_nop 0\n\t"
                 "global_load_lds_dwordx4 %3, %2\n\t"
                 "global_load_lds_dwordx4 %4, %2 offset:1024\n\t"
                 "s_mov_b32 m0, %0"
                 : "=&s"(keep) : "s"(lds), "s"(sb), "v"(v0), "v"(v1) : "memory");
}
__device__ __forceinline__ void glds_x1(unsigned lds, const void* sb, unsigned v0) {
    unsigned keep;
    asm volatile("s_mov_b32 %0, m0\n\ts_mov_b32 m0, %1\n\ts_nop 0\n\t"
                 "global_load_lds_dwordx4 %3, %2\n\t"
                 "s_mov_b32 m0, %0"
                 : "=&s"(keep) : "s"(lds), "s"(sb), "v"(v0) : "memory");
}
__device__ __forceinline__ int lane_id() {
    int l;
    asm volatile("v_mbcnt_lo_u32_b32 %0, -1, 0\n\tv_mbcnt_hi_u32_b32 %0, -1, %0" : "=v"(l));
    return l;
}
__device__ __forceinline__ s16x4 tr_read_l(unsigned a) { return __builtin_amdgcn_ds_read_tr16_b64_v4i16((LDS_AS s16x4*)(size_t)a); }

template <int NJ>
__device__ __forceinline__ void k1_epilogue(f32x4 (&acc)[4][NJ], char* __restrict__ ws, char* smem, int m0, int c0, int nt, int wid) {
    const int lane = lane_id();
    const int t = wid * 64 + lane;
    const int wr = wid >> 1, wc = wid & 1;
    const int fr = lane & 15, g = lane >> 4;
    const rsrc_t rw = mk_rsrc(ws, (unsigned)WS_FLAG);
    if (NJ == 5) {
        const unsigned sb0 = lds_addr(smem);
        CBAR(); *(lflag_t)(size_t)(sb0 + K1_FLG + wid * 4) = 21u; CBAR();
        k1_wait_rel<0>(sb0, lane, 21u);
        const float qs = (nt < 4) ? 0.22360679774997896f * 1.4426950408889634f : 1.f;
#pragma unroll
        for (int j = 0; j < NJ; ++j) {
            const int n = wc * 80 + j * 16 + g * 4;
            const int hh = n / DH_, d = n % DH_;
            char* dst = smem + hh * 10240 + (wr * 64 + fr) * 40 + d * 2;
#pragma unroll
            for (int i = 0; i < 4; ++i) {
                u32x2 a = {pack_bf16(acc[i][j][0] * qs, acc[i][j][1] * qs), pack_bf16(acc[i][j][2] * qs, acc[i][j][3] * qs)};
                *(u32x2*)(dst + i * 640) = a;
            }
        }
        CBAR(); *(lflag_t)(size_t)(sb0 + K1_FLG + wid * 4) = 22u; CBAR();
        k1_wait_rel<0>(sb0, lane, 22u);
        const int which = nt >> 2;
        const int h0 = (nt & 3) * 8;
        char* dbase = ws + WS_Q + (size_t)which * QKV_BYTES + ((size_t)h0 * S_ + m0) * 40;
        const int dofs = (int)(WS_Q + (size_t)which * QKV_BYTES + ((size_t)h0 * S_ + m0) * 40);
#pragma unroll
        for (int it = 0; it < 10; ++it) {
            const int e = it * 512 + t;
            const int hh = e / 640, r = e % 640;
            const u32x4 v = *(const u32x4*)(smem + e * 16);
            bst4<AUX_AGENT>(rw, dofs + hh * (S_ * 40) + r * 16, v);
        }
    } else {
        u16* hF = (u16*)(ws + WS_H);
#pragma unroll
        for (int j = 0; j < 3; ++j) {
            const int f = c0 + wc * 48 + j * 16 + g * 4;
            const int ks = f >> 5;
            const int lq = (f & 31) >> 3;
            const int hf = f & 4;
#pragma unroll
            for (int i = 0; i < 4; ++i) {
                const int m = m0 + wr * 64 + i * 16 + fr;
                unsigned pk[2];
#pragma unroll
                for (int r = 0; r < 2; ++r) {
                    const f32x2 gt = {acc[i][j][2 * r], acc[i][j][2 * r + 1]};
                    const f32x2 up = {acc[i][(j + 3) % NJ][2 * r], acc[i][(j + 3) % NJ][2 * r + 1]};
                    f32x2 e = gt * -1.4426950408889634f;
                    e = (f32x2){__builtin_amdgcn_exp2f(e[0]), __builtin_amdgcn_exp2f(e[1])};
                    e = e + 1.f;
                    const f32x2 rc = {__builtin_amdgcn_rcpf(e[0]), __builtin_amdgcn_rcpf(e[1])};
                    const f32x2 hv = (gt * up) * rc;
                    pk[r] = pack_bf16(hv[0], hv[1]);
                }
                u32x2 a = {pk[0], pk[1]};
                u16* dst = hF + ((size_t)((m >> 4) * KS_H + ks) * 64 + (m & 15) + 16 * lq) * 8 + hf;
                bst2<AUX_AGENT>(rw, (int)WS_H + ((((m >> 4) * KS_H + ks) * 64 + (m & 15) + 16 * lq) * 8 + hf) * 2, a);
            }
        }
    }
}

template <int NJ>
__device__ __forceinline__ void k1_tile(const u16* __restrict__ X, const u16* __restrict__ W0, const u16* __restrict__ W1,
                                        char* __restrict__ ws, char* smem, int mt, int nt, int xs, int xcnt, int uw ENTARG) {
    constexpr int BN = 32 * NJ;
    constexpr int ROWB = (NJ == 5) ? 320 : 192;
    constexpr int UPR = ROWB / 16;
    constexpr int NBH = UPR / 2;
    constexpr int NB = 2 * NJ;
    constexpr int LDB = (NJ == 5) ? NQKV : F_;
    const int m0 = mt * 256;
    const int c0 = (NJ == 5) ? nt * 160 : nt * 96;
    const unsigned sb0 = lds_addr(smem);
    auto boffs = [&](int i, int lane) {
        const int p = i * 64 + lane;
        const int k = p / UPR, u = p % UPR;
        return (unsigned)((k * LDB + (((u >> 1) ^ ((k >> 3) & 1)) << 4) + (u & 1) * 8) * 2);
    };
    auto aoffs_f = [&](int lane) { return (unsigned)((lane >> 3) * (D_ * 2) + (((lane & 7) ^ (lane >> 3)) << 4)); };
    const char* xa = (const char*)(X + (size_t)m0 * D_);
    const char* wb0 = (const char*)(W0 + c0);
    const char* wb1 = (const char*)(W1 + c0);

#define KRA 0
#define KRB 0
    const int rot = (xs * KRA + ((mt & 3) * KRB >> 1)) % 10;
    if (uw >= 8) {
        const int dw = uw - 8;
        const int lane = lane_id();
        const unsigned aoffs = aoffs_f(lane);
        if (dw == 0 && lane < 24) *(lflag_t)(size_t)(sb0 + K1_FLG + lane * 4) = 0u;
        constexpr int NBP = NJ;
        unsigned vb[NBP], va[4];
#pragma unroll
        for (int i = 0; i < NBP; ++i) vb[i] = boffs((NJ == 5) ? 5 * dw + i : i, lane) - (i & 3) * 1024;
#pragma unroll
        for (int r = 0; r < 4; ++r) va[r] = aoffs + r * (8 * D_ * 2 - 1024);
        const char* xs0 = xa + dw * (128 * D_ * 2);
        const char* ws0 = (NJ == 6 && dw) ? wb1 : wb0;
        auto loadA = [&](int kt, int q0) {
            const unsigned base = sb0 + (kt % 3) * K1_ABYTES + dw * 16384;
            const int kr = kt + rot;
            const char* src = xs0 + (kr >= 10 ? kr - 10 : kr) * 128;
#pragma unroll
            for (int q = q0; q < q0 + 2; ++q) glds_x4(base + q * 4096, src + q * (32 * D_ * 2), va[0], va[1], va[2], va[3]);
        };
        auto loadB = [&](int h) {
            const unsigned base = sb0 + K1_BOFF + (h % 5) * K1_BSLOT + dw * (NBP * 1024);
            const int hr = h + 2 * rot;
            const char* s0 = ws0 + (size_t)(hr >= 20 ? hr - 20 : hr) * (32 * LDB * 2);
            glds_x4(base, s0, vb[0], vb[1], vb[2], vb[3]);
            if (NJ == 5) glds_x1(base + 4096, s0, vb[4]);
            else glds_x2(base + 4096, s0, vb[4], vb[5 % NBP]);
        };
        asm volatile("s_waitcnt lgkmcnt(0)" ::: "memory");
        __builtin_amdgcn_s_barrier();
        CBAR();
        ET(1);
        volatile LDS_AS u16* rdy = (volatile LDS_AS u16*)(size_t)(sb0 + K1_FLG + 32 + (lane & 7) * 4 + dw * 2);
        loadB(1);
#pragma unroll 1
        for (int kt = 1; kt < 10; ++kt) {
            if (kt >= 3) k1_wait_rel<0>(sb0, lane, 2 * kt - 4);
            loadB(2 * kt); loadA(kt, 0);
            WAIT_VM(2 * NBP + 8);
            *rdy = (u16)(2 * kt - 1);
            loadA(kt, 2);
            WAIT_VM(NBP + 16);
            *rdy = (u16)(2 * kt);
            if (kt >= 2) k1_wait_rel<0>(sb0, lane, 2 * kt - 3);
            loadB(2 * kt + 1);
        }
        WAIT_VM(NBP);
        *rdy = (u16)19;
        WAIT_VM(0);
        *rdy = (u16)30;
        ET(4); ETW();
        return;
    }

    ET(1);
    const int lane = lane_id();
    {
        const unsigned aoffs = aoffs_f(lane);
        wb0 += (size_t)rot * (64 * LDB * 2); wb1 += (size_t)rot * (64 * LDB * 2);
        glds_x4(sb0 + uw * 4096, xa + uw * (32 * D_ * 2) + rot * 128, aoffs, aoffs + (8 * D_ * 2 - 1024), aoffs + 2 * (8 * D_ * 2 - 1024), aoffs + 3 * (8 * D_ * 2 - 1024));
        if (NJ == 5) {
            if (uw < 5) glds_x2(sb0 + K1_BOFF + uw * 2048, wb0, boffs(2 * uw, lane), boffs(2 * uw + 1, lane) - 1024);
        } else {
            if (uw < 6) {
                const unsigned v = boffs(uw, lane);
                glds_x1(sb0 + K1_BOFF + uw * 1024, wb0, v);
                glds_x1(sb0 + K1_BOFF + 6144 + uw * 1024, wb1, v);
            }
        }
    }
    ET(2);
    __builtin_amdgcn_s_barrier();
    CBAR();
    const int wr = uw >> 1, wc = uw & 1;
    const int fr = lane & 15, g = lane >> 4;
    f32x4 acc[4][NJ];
#pragma unroll
    for (int i = 0; i < 4; ++i)
#pragma unroll
        for (int j = 0; j < NJ; ++j) acc[i][j] = (f32x4){0.f, 0.f, 0.f, 0.f};

    unsigned aa[2][2], be, bo;
    {
        const int row = wr * 64 + fr;
        aa[0][0] = sb0 + row * 128 + ((g ^ (row & 7)) << 4);
        aa[1][0] = sb0 + row * 128 + (((4 + g) ^ (row & 7)) << 4);
        aa[0][1] = aa[0][0] + 2 * K1_ABYTES;
        aa[1][1] = aa[1][0] + 2 * K1_ABYTES;
        const int q4 = (lane & 15) >> 2, p4 = lane & 3;
        const unsigned bb = sb0 + K1_BOFF + (8 * g + q4) * ROWB + p4 * 8 + wc * (NJ == 5 ? 5 : 3) * 32;
        const int sx = ((g & 1) << 5) * (wc ? -1 : 1);
        be = bb + sx; bo = bb - sx;
    }
    unsigned frel = sb0 + K1_FLG + uw * 4;
    unsigned frdy = sb0 + K1_FLG + 32 + uw * 4 + (lane & 1) * 2;
    asm volatile("" : "+v"(aa[0][0]), "+v"(aa[1][0]), "+v"(aa[0][1]), "+v"(aa[1][1]), "+v"(be), "+v"(bo), "+v"(frel), "+v"(frdy));
    WAIT_VM(0);
    ET(3);
    *(lflag_t)(size_t)(frel + 64) = 1u;
    {
        lflag_t p = (lflag_t)(size_t)(sb0 + K1_FLG + 64 + (lane & 7) * 4);
        while (__builtin_amdgcn_ballot_w64(*p == 0u) != 0ull) {}
    }
    CBAR();
    ET(4);
    bf16x8 af[2][4], bq[NJ];
    auto rdA = [&](int h, int i) {
        const int st = (h >> 1) % 3;
        return *(const LDS_AS bf16x8*)(size_t)(aa[h & 1][st == 2] + (st == 1 ? K1_ABYTES : 0) + i * 2048);
    };
    auto rdB = [&](int h, int j) {
        const int jj = (NJ == 5) ? j : j % 3, sub = (NJ == 5) ? 0 : (j / 3) * 6144;
        const unsigned b = ((jj & 1) ? bo : be) + (h % 5) * K1_BSLOT + jj * 32 + sub;
        s16x4 lo = tr_read_l(b);
        s16x4 hi = tr_read_l(b + 4 * ROWB);
        return cat4(lo, hi);
    };
    auto rd_rdy = [&]() { return (unsigned)*(volatile LDS_AS u16*)(size_t)frdy; };
#define WAIT_RDY(fl, need) asm volatile("v_cmp_gt_u32 vcc, %2, %0\n\ts_cbranch_vccz L_ok_%=\n" \
        "L_spin_%=:\n\tds_read_u16 %0, %1\n\ts_waitcnt lgkmcnt(0)\n\tv_cmp_gt_u32 vcc, %2, %0\n\ts_cbranch_vccnz L_spin_%=\n" \
        "L_ok_%=:" : "+v"(fl) : "v"(frdy), "n"(need) : "vcc", "memory")
    auto release = [&](unsigned n) { CBAR(); *(lflag_t)(size_t)frel = n; CBAR(); };
#pragma unroll
    for (int i = 0; i < 4; ++i) af[0][i] = rdA(0, i);
    __builtin_amdgcn_sched_barrier(0);
#pragma unroll
    for (int j = 0; j < NJ; ++j) { bq[j] = rdB(0, j); __builtin_amdgcn_sched_barrier(0); }
    release(1u);
    unsigned fl = rd_rdy();
    __builtin_amdgcn_sched_barrier(0);
#pragma unroll
    for (int p = 0; p < 20; ++p) {
#pragma unroll
        for (int sl = 0; sl < 4 * NJ; ++sl) {
            const int j = sl >> 2, i = sl & 3;
            MF1(acc[i][j], bq[j], af[p & 1][i]);
            __builtin_amdgcn_sched_barrier(0);
            if (sl == 0 && p > 0) bq[NJ - 1] = rdB(p, NJ - 1);
            if (sl == 1 && p > 0 && p < 16) release(p + 1);
            if (p < 19) {
                if (sl == 2) WAIT_RDY(fl, p + 2);
                if (sl >= 4 && i == 0) bq[j - 1] = rdB(p + 1, j - 1);
                if (sl == 5) af[(p + 1) & 1][0] = rdA(p + 1, 0);
                if (sl == 6) af[(p + 1) & 1][1] = rdA(p + 1, 1);
                if (sl == 7) af[(p + 1) & 1][2] = rdA(p + 1, 2);
                if (sl == 9) af[(p + 1) & 1][3] = rdA(p + 1, 3);
                if (sl == 4 * NJ - K1FS && p < 18) fl = rd_rdy();
            }
            __builtin_amdgcn_sched_barrier(0);
        }
    }

    ET(1);
    k1_epilogue<NJ>(acc, ws, smem, m0, c0, nt, uw);
    ET(5); ETW();
}

#define AUXM 0
#define AUXS 12
#define K1NW 12
template <int NU, int NR, bool EARLY>
__device__ __forceinline__ void k1_aux(const u16* __restrict__ X, const u16* __restrict__ Wo, const u16* __restrict__ Wd,
                                       const u16* __restrict__ gate_w, char* __restrict__ ws, char* wl,
                                       int u0, int us, int ue, int rb, int r0, int rs, int re) {
    const int lane = lane_id();
    float* gout = (float*)(ws + WS_G);
    const int q4 = (lane & 15) >> 2, p4 = lane & 3, g = lane >> 4;
    const rsrc_t rw = mk_rsrc(ws, (unsigned)WS_FLAG);
    auto fetch = [&](int unit, u32x4 (&r)[4]) {
        const int ksg = unit / 10, cc = unit % 10;
        const u16* src = (ksg < KS_O) ? (Wo + (size_t)ksg * 32 * D_) : (Wd + (size_t)(ksg - KS_O) * 32 * D_);
#pragma unroll
        for (int i = 0; i < 4; ++i) r[i] = *(const u32x4*)(src + (size_t)(i * 8 + (lane >> 3)) * D_ + cc * 64 + (lane & 7) * 8);
    };
    auto emit = [&](int unit, const u32x4 (&r)[4]) {
        const int ksg = unit / 10, cc = unit % 10;
#pragma unroll
        for (int hf = 0; hf < 2; ++hf) {
            CBAR();
            if (((lane >> 2) & 1) == hf) {
#pragma unroll
                for (int i = 0; i < 4; ++i) *(u32x4*)(wl + (i * 8 + (lane >> 3)) * 64 + (lane & 3) * 16) = r[i];
            }
            CBAR();
#pragma unroll
            for (int j = 0; j < 2; ++j) {
                const char* a = wl + (8 * g + q4) * 64 + j * 32 + p4 * 8;
                s16x4 lo = tr_read(a);
                s16x4 hi = tr_read(a + 4 * 64);
                bf16x8 v = cat4(lo, hi);
                const int nt = cc * 4 + hf * 2 + j;
                const int dst = (ksg < KS_O) ? (int)WS_WO + ((nt * KS_O + ksg) * 64 + lane) * 16
                                             : (int)WS_WD + ((nt * KS_H + (ksg - KS_O)) * 64 + lane) * 16;
                bst4<AUX_AGENT>(rw, dst, __builtin_bit_cast(u32x4, v));
            }
        }
    };
    u32x4 r[NU ? NU : 1][4];
    u32x4 x0[NR ? NR : 1], x1[NR ? NR : 1], w0, w1;
    auto gfetch = [&]() {
        w0 = *(const u32x4*)(gate_w + lane * 8);
        w1 = *(const u32x4*)(gate_w + 512 + (lane & 15) * 8);
#pragma unroll
        for (int i = 0; i < NR; ++i) {
            int row = r0 + rs * i;
            row = rb + (row < re ? row : r0);
            x0[i] = *(const u32x4*)(X + (size_t)row * D_ + lane * 8);
            x1[i] = *(const u32x4*)(X + (size_t)row * D_ + 512 + (lane & 15) * 8);
        }
    };
    if (NU) {
        const bool extra = u0 + (NU - 1) * us < ue;
#pragma unroll
        for (int u = 0; u < NU - 1; ++u) fetch(u0 + u * us, r[u]);
        if (extra) fetch(u0 + (NU - 1) * us, r[NU - 1]);
        if (EARLY && NR) gfetch();
#pragma unroll
        for (int u = 0; u < NU - 1; ++u) emit(u0 + u * us, r[u]);
        if (extra) emit(u0 + (NU - 1) * us, r[NU - 1]);
    }
    CBAR();
    if (NR == 0) return;
    if (!EARLY || !NU) gfetch();
    if (lane >= 16) w1 = (u32x4){0u, 0u, 0u, 0u};
    float sacc[NR ? NR : 1];
#pragma unroll
    for (int i = 0; i < NR; ++i) {
        float a = 0.f;
#pragma unroll
        for (int e = 0; e < 4; ++e) {
            const unsigned xa = x0[i][e], wa = w0[e], xb = x1[i][e], wb = w1[e];
            a = __builtin_amdgcn_fdot2_f32_bf16(__builtin_bit_cast(bf2_t, xa), __builtin_bit_cast(bf2_t, wa), a, false);
            a = __builtin_amdgcn_fdot2_f32_bf16(__builtin_bit_cast(bf2_t, xb), __builtin_bit_cast(bf2_t, wb), a, false);
        }
        sacc[i] = a;
    }
    {
        float z = 0.f;
#pragma unroll
        for (int i = 0; i < NR; ++i) {
            const float tot = wave_sum(sacc[i]);
            z = (lane == i) ? tot : z;
        }
        const int row = r0 + rs * lane;
        z = rbf(z);
        if (lane < NR && row < re) gout[rb + row] = rbf(1.f / (1.f + __expf(-z)));
    }
}

__global__ __launch_bounds__(K1NW * 64) void k1_gemm(const u16* __restrict__ X, const u16* __restrict__ Wqkv,
                                               const u16* __restrict__ Wg, const u16* __restrict__ Wu,
                                               const u16* __restrict__ Wo, const u16* __restrict__ Wd,
                                               const u16* __restrict__ gate_w, char* __restrict__ ws) {
    __shared__ __attribute__((aligned(16))) char smem[K1_LDS];
    const int bid = blockIdx.x;
    const int xcd = bid & 7, idx = bid >> 3;
    const int ng = xcd >> 1, mg = xcd & 1;
    const int cnt = (ng < 2) ? 8 : 7;
    const int nstart = (ng < 2) ? ng * 8 : 16 + (ng - 2) * 7;
    const int uw = __builtin_amdgcn_readfirstlane(threadIdx.x >> 6);
    if (idx >= 4 * cnt) {
        const int sbk = (xcd - 4) * 4 + (idx - 28);
        if (threadIdx.x < 32)
            *(u32x4*)(ws + WS_FLAG + (size_t)(sbk * 2 + (threadIdx.x >> 4)) * FLAG_LD + (threadIdx.x & 15) * 16) = (u32x4){0u, 0u, 0u, 0u};
        constexpr int NWV = 16 * K1NW;
        const int gw = sbk * K1NW + uw, wq = (((xcd - 4) >> 1) * 4 + (idx - 28)) * K1NW + uw;
        constexpr int NRS = (1024 + NWV / 2 - 1) / (NWV / 2);
        if (AUXM == 0) k1_aux<(740 + NWV - 1) / NWV, NRS, false>(X, Wo, Wd, gate_w, ws, smem + uw * 2048, gw, NWV, 740, mg * 1024, wq, NWV / 2, 1024);
        if (AUXM == 1) k1_aux<1, 0, false>(X, Wo, Wd, gate_w, ws, smem + uw * 2048, 720 + uw * 16 + sbk, 0, 740, 0, 0, 0, 0);
        if (AUXM == 2) k1_aux<1, NRS, true>(X, Wo, Wd, gate_w, ws, smem + uw * 2048, 720 + uw * 16 + sbk, 0, 740, mg * 1024, wq, NWV / 2, 1024);
        if (AUXM == 3) k1_aux<(740 + NWV - 1) / NWV, 0, false>(X, Wo, Wd, gate_w, ws, smem + uw * 2048, gw, NWV, 740, mg * 1024, wq, NWV / 2, 1024);
        if (AUXM == 4) k1_aux<(260 + NWV - 1) / NWV, 0, false>(X, Wo, Wd, gate_w, ws, smem + uw * 2048, 480 + gw, NWV, 740, mg * 1024, wq, NWV / 2, 1024);
        return;
    }
    const int mt = mg * 4 + (idx & 3);
    const int nt = nstart + (idx >> 2);
    if (uw >= 10) {
        if (AUXM == 0 || uw > 10) return;
        __builtin_amdgcn_s_barrier();
        CBAR();
        k1_wait_rel<4>(lds_addr(smem), lane_id(), AUXS);
        const int lin = mt * 30 + nt;
        if (AUXM == 1) k1_aux<3, 9, true>(X, Wo, Wd, gate_w, ws, smem + K1_AUX, lin, 240, 720, mt * 256, nt, 30, 256);
        if (AUXM == 2) k1_aux<3, 0, true>(X, Wo, Wd, gate_w, ws, smem + K1_AUX, lin, 240, 720, mt * 256, nt, 30, 256);
        if (AUXM == 3) k1_aux<0, 9, true>(X, Wo, Wd, gate_w, ws, smem + K1_AUX, lin, 240, 720, mt * 256, nt, 30, 256);
        if (AUXM == 4) k1_aux<2, 9, true>(X, Wo, Wd, gate_w, ws, smem + K1_AUX, lin, 240, 480, mt * 256, nt, 30, 256);
        return;
    }
    if (nt < 12) k1_tile<5>(X, Wqkv, Wqkv, ws, smem, mt, nt, idx >> 2, cnt, uw ENTPASS);
    else k1_tile<6>(X, Wg, Wu, ws, smem, mt, nt - 12, idx >> 2, cnt, uw ENTPASS);
}

typedef unsigned long long u64;
#define GDELAY 4
#define PF2 0
#define ACQ 0
#define ROT 0
#define ROLL 0
#define GPRIO 0
#define POLLSLP 4
#define APRIO 2
#define ST2(i) do {} while (0)
#define ST2ID() do {} while (0)
__device__ __forceinline__ void st_agent(void* p, u32x2 v) {
    __hip_atomic_store((u64*)p, __builtin_bit_cast(u64, v), __ATOMIC_RELAXED, __HIP_MEMORY_SCOPE_AGENT);
}
template <int AUX> __device__ __forceinline__ bf16x8 bld(rsrc_t r, int voff, int soff) {
    return __builtin_bit_cast(bf16x8, __builtin_amdgcn_raw_buffer_load_b128(r, voff, soff, AUX));
}

__device__ __forceinline__ float fmx(float a, float b) { return __builtin_elementwise_maximum(a, b); }

template <bool EDGE>
__device__ __forceinline__ void k2_attn_wave(char* __restrict__ oF, const char* sK, const char* sV, const bf16x8 (&qf)[2],
                                             const float (&gg)[2], int h, int qw, int rbase, int lane) {
    const int fr = lane & 15, g = lane >> 4;
    const int q4 = (lane & 15) >> 2, p4 = lane & 3;
    const float NEG = -__builtin_inff();
    const bf16x8 ones = {0x3f80, 0x3f80, 0x3f80, 0x3f80, 0x3f80, 0x3f80, 0x3f80, 0x3f80};
    const int ts = 16 - (qw >> 4);
    const char* kb = sK + (rbase + fr) * 40 + (g < 3 ? g : 2) * 16;
    const char* vb = sV + (rbase + 4 * g + q4) * 40 + p4 * 8;
    f32x4 cLo, cHi;
#pragma unroll
    for (int r = 0; r < 4; ++r) {
        cLo[r] = (fr >= 4 * g + r) ? NEG : 0.f;
        cHi[r] = (4 * g + r > fr) ? NEG : 0.f;
    }
    auto rdK = [&](int tt) {
        const u32x2 klo = *(const u32x2*)(kb + tt * 640), khi = *(const u32x2*)(kb + tt * 640 + 8);
        const u32x4 kv = {klo[0], klo[1], khi[0], khi[1]};
        return __builtin_bit_cast(bf16x8, kv);
    };
    const f32x4 zero = {0.f, 0.f, 0.f, 0.f};
#define SB0() __builtin_amdgcn_sched_barrier(0)
    float mx[2] = {NEG, NEG};
    {
        bf16x8 kf[3];
        f32x4 s[2][2];
        kf[0] = rdK(0); kf[1] = rdK(1);
#pragma unroll
        for (int tt = 0; tt < 19; ++tt) {
            if (tt + 2 < 18) kf[(tt + 2) % 3] = rdK(tt + 2);
#pragma unroll
            for (int qt = 0; qt < 2; ++qt) {
                const int u = tt - qt;
                if (tt < 18 && u >= 0 && u <= 16)
                    s[tt & 1][qt] = __builtin_amdgcn_mfma_f32_16x16x32_bf16(kf[tt % 3], qf[qt], u == 0 ? cLo : (u == 16 ? cHi : zero), 0, 0, 0);
                SB0();
                const int tp = tt - 1, up = tp - qt;
                if (tp >= 0 && up >= 0 && up <= 16) {
                    const f32x4 p = s[tp & 1][qt];
                    if (EDGE && tp < 16) {
                        const float m = fmx(fmx(p[0], p[1]), fmx(p[2], p[3]));
                        mx[qt] = fmx(mx[qt], (tp < ts) ? NEG : m);
                    } else {
                        mx[qt] = fmx(fmx(mx[qt], p[0]), p[1]);
                        mx[qt] = fmx(fmx(mx[qt], p[2]), p[3]);
                    }
                }
                SB0();
            }
        }
    }
    CBAR();
    bf16x8 kf[2][2], vf[2][2];
    auto rdV = [&](int pp, bf16x8 (&v)[2]) {
#pragma unroll
        for (int dt = 0; dt < 2; ++dt) {
            s16x4 lo = tr_read(vb + (2 * pp) * 640 + dt * 32);
            s16x4 hi = tr_read(vb + (2 * pp + 1) * 640 + dt * 32);
            v[dt] = cat4(lo, hi);
        }
    };
    kf[0][0] = rdK(0); kf[0][1] = rdK(1);
    kf[1][0] = rdK(2); kf[1][1] = rdK(3);
    rdV(0, vf[0]);
    f32x4 cm[2], cl[2], ch[2];
#pragma unroll
    for (int qt = 0; qt < 2; ++qt) {
        float m = mx[qt];
        {
            const unsigned mi = __builtin_bit_cast(unsigned, m);
            auto r = __builtin_amdgcn_permlane32_swap(mi, mi, false, false);
            m = fmx(__builtin_bit_cast(float, (unsigned)r[0]), __builtin_bit_cast(float, (unsigned)r[1]));
        }
        {
            const unsigned mi = __builtin_bit_cast(unsigned, m);
            auto r = __builtin_amdgcn_permlane16_swap(mi, mi, false, false);
            m = fmx(__builtin_bit_cast(float, (unsigned)r[0]), __builtin_bit_cast(float, (unsigned)r[1]));
        }
        m = -m;
        cm[qt] = (f32x4){m, m, m, m};
#pragma unroll
        for (int r = 0; r < 4; ++r) {
            cl[qt][r] = (fr >= 4 * g + r) ? NEG : m;
            ch[qt][r] = (4 * g + r > fr) ? NEG : m;
        }
    }
    f32x4 oacc[2][3];
#pragma unroll
    for (int qt = 0; qt < 2; ++qt)
#pragma unroll
        for (int dt = 0; dt < 3; ++dt) oacc[qt][dt] = zero;
    f32x4 s[2][2][2];
    u32x4 pk[2];
    auto valid = [](int tt, int qt) { return tt - qt >= 0 && tt - qt <= 16; };
    auto QK = [&](int pp, int hh, int qt) {
        const int tt = 2 * pp + hh, u = tt - qt;
        if (pp < 9 && valid(tt, qt))
            s[pp & 1][hh][qt] = __builtin_amdgcn_mfma_f32_16x16x32_bf16(kf[pp & 1][hh], qf[qt], u == 0 ? cl[qt] : (u == 16 ? ch[qt] : cm[qt]), 0, 0, 0);
        SB0();
    };
    auto EX = [&](int pp, int hh, int qt, int half) {
        const int tt = 2 * pp + hh;
        if (pp < 9) {
            unsigned v = 0u;
            if (valid(tt, qt)) {
                const f32x4 x = s[pp & 1][hh][qt];
                v = pack_bf16(__builtin_amdgcn_exp2f(x[2 * half]), __builtin_amdgcn_exp2f(x[2 * half + 1]));
                if (EDGE && tt < 16) v = (tt < ts) ? 0u : v;
            }
            pk[qt][2 * hh + half] = v;
        }
        SB0();
    };
    auto PV = [&](int pp, int qt, int dt) {
        if (pp >= 0 && pp < 9)
            oacc[qt][dt] = __builtin_amdgcn_mfma_f32_16x16x32_bf16(dt < 2 ? vf[pp & 1][dt] : ones, __builtin_bit_cast(bf16x8, pk[qt]), oacc[qt][dt], 0, 0, 0);
        SB0();
    };
    SB0();
    QK(0, 0, 0); QK(0, 1, 0); QK(0, 0, 1); QK(0, 1, 1);
#pragma unroll
    for (int pp = 0; pp < 10; ++pp) {
        PV(pp - 1, 1, 0); EX(pp, 0, 0, 0);
        PV(pp - 1, 1, 1); EX(pp, 0, 0, 1);
        PV(pp - 1, 1, 2);
        if (pp + 1 < 9) rdV(pp + 1, vf[(pp + 1) & 1]);
        QK(pp + 1, 0, 0); EX(pp, 1, 0, 0);
        QK(pp + 1, 1, 0); EX(pp, 1, 0, 1);
        PV(pp, 0, 0); EX(pp, 0, 1, 0);
        PV(pp, 0, 1); EX(pp, 0, 1, 1);
        PV(pp, 0, 2);
        QK(pp + 1, 0, 1); EX(pp, 1, 1, 0);
        QK(pp + 1, 1, 1);
        if (pp + 2 < 9) { kf[pp & 1][0] = rdK(2 * pp + 4); kf[pp & 1][1] = rdK(2 * pp + 5); }
        EX(pp, 1, 1, 1);
    }
#pragma unroll
    for (int qt = 0; qt < 2; ++qt) {
        const float inv = gg[qt] / oacc[qt][2][0];
        const int m = qw + 16 * qt + fr;
        {
            const int k0 = h * DH_ + 4 * g;
            u32x2 a = {pack_bf16(oacc[qt][0][0] * inv, oacc[qt][0][1] * inv), pack_bf16(oacc[qt][0][2] * inv, oacc[qt][0][3] * inv)};
            st_agent(oF + ((size_t)((m >> 4) * KS_O + (k0 >> 5)) * 64 + (m & 15) + 16 * ((k0 & 31) >> 3)) * 16 + (k0 & 4) * 2, a);
        }
        if (g == 0) {
            const int k0 = h * DH_ + 16;
            u32x2 a = {pack_bf16(oacc[qt][1][0] * inv, oacc[qt][1][1] * inv), pack_bf16(oacc[qt][1][2] * inv, oacc[qt][1][3] * inv)};
            st_agent(oF + ((size_t)((m >> 4) * KS_O + (k0 >> 5)) * 64 + (m & 15) + 16 * ((k0 & 31) >> 3)) * 16 + (k0 & 4) * 2, a);
        }
    }
}

__device__ __forceinline__ void k2_attn(char* __restrict__ ws, char* smem, int bid) {
    const int h = (bid & 7) + 8 * ((bid >> 3) & 3);
    const int qb = bid >> 5;
    const int q0 = qb * 128;
    const int t = threadIdx.x, lane = t & 63;
    const int w = __builtin_amdgcn_readfirstlane(t >> 6);
    const char* Qb = ws + WS_Q + (size_t)h * S_ * 40;
    const char* Kb = ws + WS_K + (size_t)h * S_ * 40;
    const char* Vb = ws + WS_V + (size_t)h * S_ * 40;
    char* sK = smem;
    char* sV = smem + 15360;

    ST2(0); ST2ID();
    if (APRIO) __builtin_amdgcn_s_setprio(APRIO);
#pragma unroll
    for (int i = 0; i < 4; ++i) {
        const int p = i * 256 + t;
        if (i < 3 || w < 3) {
            int off = (q0 - 256) * 40 + p * 16;
            off = off < 0 ? 0 : off;
            glds16(Kb + off, sK + p * 16);
            glds16(Vb + off, sV + p * 16);
        }
    }
    if (t < 4) *(u32x4*)(smem + 2 * 15360 + t * 16) = (u32x4){0u, 0u, 0u, 0u};
    const int fr = lane & 15, g = lane >> 4;
    const int qw = q0 + 32 * w;
    bf16x8 qf[2];
    float gg[2];
#pragma unroll
    for (int qt = 0; qt < 2; ++qt) {
        const char* qp = Qb + (size_t)(qw + qt * 16 + fr) * 40 + (g < 3 ? g : 2) * 16;
        u32x2 lo = *(const u32x2*)qp;
        u32x2 hi = *(const u32x2*)(qp + (g < 2 ? 8 : 0));
        if (g >= 2) hi = (u32x2){0u, 0u};
        if (g == 3) lo = (u32x2){0u, 0u};
        u32x4 v = {lo[0], lo[1], hi[0], hi[1]};
        qf[qt] = __builtin_bit_cast(bf16x8, v);
        gg[qt] = ((const float*)(ws + WS_G))[qw + qt * 16 + fr];
    }
    __syncthreads();
    ST2(1);
    char* oF = ws + WS_O;
    if (qb >= 2) k2_attn_wave<false>(oF, sK, sV, qf, gg, h, qw, 32 * w, lane);
    else k2_attn_wave<true>(oF, sK, sV, qf, gg, h, qw, 32 * w, lane);
    ST2(2);
    asm volatile("s_waitcnt vmcnt(0)" ::: "memory");
    ST2(3);
    if (lane == 0)
        __hip_atomic_store((unsigned*)(ws + WS_FLAG + (qw >> 6) * FLAG_LD) + h * 2 + ((qw >> 5) & 1), 1u, __ATOMIC_RELAXED, __HIP_MEMORY_SCOPE_AGENT);
}

constexpr int RED_LD = 84;
__device__ __forceinline__ void k2_gemm(char* __restrict__ ws, u16* __restrict__ out, char* smem, int bid) {
    float* red = (float*)smem;
    const int xcd = bid & 7, idx = bid >> 3;
    const int mt = (xcd >> 1) * 8 + (idx & 7);
    const int nt = (xcd & 1) * 4 + (idx >> 3);
    const int t = threadIdx.x, lane = t & 63;
    const int w = __builtin_amdgcn_readfirstlane(t >> 6);
    const int fr = lane & 15, g = lane >> 4;
    const int nsteps = (w < 3) ? 14 : 12;
    const rsrc_t rs = __builtin_amdgcn_make_buffer_rsrc(ws, 0, (int)WS_FLAG, 0x00020000);
    const int vo = lane * 16;
    int cur = ROT ? (((idx & 7) + 2 * (idx >> 3)) & 7) * nsteps >> 3 : 0;
    const int ap = (int)WS_H + ((mt * 4) * KS_H + 14 * w) * 1024;
    const int bp = (int)WS_WD + ((nt * 5) * KS_H + 14 * w) * 1024;

    f32x4 acc[4][5];
#pragma unroll
    for (int i = 0; i < 4; ++i)
#pragma unroll
        for (int j = 0; j < 5; ++j) acc[i][j] = (f32x4){0.f, 0.f, 0.f, 0.f};
    bf16x8 a0[4], b0[5], a1[4], b1[5];
    auto ld = [&](bf16x8 (&a)[4], bf16x8 (&b)[5]) {
#pragma unroll
        for (int i = 0; i < 4; ++i) a[i] = bld<0>(rs, vo, ap + (i * KS_H + cur) * 1024);
#pragma unroll
        for (int j = 0; j < 5; ++j) b[j] = bld<0>(rs, vo, bp + (j * KS_H + cur) * 1024);
        cur = (cur + 1 == nsteps) ? 0 : cur + 1;
    };
    auto mm = [&](bf16x8 (&a)[4], bf16x8 (&b)[5]) {
#pragma unroll
        for (int i = 0; i < 4; ++i)
#pragma unroll
            for (int j = 0; j < 5; ++j)
                acc[i][j] = __builtin_amdgcn_mfma_f32_16x16x32_bf16(b[j], a[i], acc[i][j], 0, 0, 0);
    };
    ST2(0); ST2ID();
    if (GPRIO) __builtin_amdgcn_s_setprio(GPRIO);
#pragma unroll 1
    for (int i = 0; i < GDELAY; ++i) __builtin_amdgcn_s_sleep(8);
    ld(a0, b0);
    ld(a1, b1);
    const float gl = ((const float*)(ws + WS_G))[mt * 64 + lane];
    auto mmld = [&](bf16x8 (&a)[4], bf16x8 (&b)[5]) {
#pragma unroll
        for (int j = 0; j < 5; ++j) {
#pragma unroll
            for (int i = 0; i < 4; ++i) {
                acc[i][j] = __builtin_amdgcn_mfma_f32_16x16x32_bf16(b[j], a[i], acc[i][j], 0, 0, 0);
                if (j == 4) { __builtin_amdgcn_sched_barrier(0); a[i] = bld<0>(rs, vo, ap + (i * KS_H + cur) * 1024); __builtin_amdgcn_sched_barrier(0); }
            }
            __builtin_amdgcn_sched_barrier(0);
            b[j] = bld<0>(rs, vo, bp + (j * KS_H + cur) * 1024);
            __builtin_amdgcn_sched_barrier(0);
        }
        cur = (cur + 1 == nsteps) ? 0 : cur + 1;
    };
#pragma unroll 1
    for (int s = 0; s < nsteps - 2; s += 2) {
        if (ROLL) { mmld(a0, b0); mmld(a1, b1); }
        else {
            mm(a0, b0); ld(a0, b0);
            mm(a1, b1); ld(a1, b1);
        }
    }
    ST2(1);
    const int op = (int)WS_O + ((mt * 4) * KS_O + 5 * w) * 1024;
    const int wp = (int)WS_WO + ((nt * 5) * KS_O + 5 * w) * 1024;
    auto ldw = [&](bf16x8 (&b)[5], int s) {
#pragma unroll
        for (int j = 0; j < 5; ++j) b[j] = bld<0>(rs, vo, wp + (j * KS_O + s) * 1024);
    };
    auto ldo = [&](bf16x8 (&a)[4], int s) {
#pragma unroll
        for (int i = 0; i < 4; ++i) a[i] = bld<ACQ ? 0 : AUX_AGENT>(rs, vo, op + (i * KS_O + s) * 1024);
    };
#define SB() __builtin_amdgcn_sched_barrier(0)
    SB(); mm(a0, b0); SB(); ldw(b0, 0);
    SB(); mm(a1, b1); SB(); ldw(b1, 1);
    SB();
#pragma unroll
    for (int i = 0; i < 4; ++i) {
        const float sc = rbf(1.f - __builtin_bit_cast(float, __builtin_amdgcn_ds_bpermute((i * 16 + fr) * 4, __builtin_bit_cast(int, gl))));
#pragma unroll
        for (int j = 0; j < 5; ++j) acc[i][j] = acc[i][j] * sc;
        asm volatile("" : "+v"(acc[i][0]), "+v"(acc[i][1]), "+v"(acc[i][2]), "+v"(acc[i][3]), "+v"(acc[i][4]));
    }
    ST2(2);
    {
        if (w == 0) {
            const unsigned* fp = (const unsigned*)(ws + WS_FLAG + mt * FLAG_LD) + lane;
            while (true) {
                const unsigned f = __hip_atomic_load(fp, __ATOMIC_RELAXED, __HIP_MEMORY_SCOPE_AGENT);
                if (__builtin_amdgcn_ballot_w64(f != 1u) == 0ull) break;
                __builtin_amdgcn_s_sleep(POLLSLP);
            }
        }
        asm volatile("" ::: "memory");
        __builtin_amdgcn_s_barrier();
        asm volatile("" ::: "memory");
    }
    ST2(3);
    ldo(a0, 0);
    ldo(a1, 1);
    SB(); mm(a0, b0); SB(); ldo(a0, 2); ldw(b0, 2);
    SB(); mm(a1, b1); SB(); ldo(a1, 3); ldw(b1, 3);
    SB(); mm(a0, b0); SB(); ldo(a0, 4); ldw(b0, 4);
    SB(); mm(a1, b1);
    SB(); mm(a0, b0);
    SB();
    ST2(4);
    float* rw = red + (w & 1) * 64 * RED_LD + fr * RED_LD + g * 4;
    if (w >= 2) {
#pragma unroll
        for (int i = 0; i < 4; ++i)
#pragma unroll
            for (int j = 0; j < 5; ++j) *(f32x4*)(rw + i * 16 * RED_LD + j * 16) = acc[i][j];
    }
    __syncthreads();
    if (w < 2) {
#pragma unroll
        for (int i = 0; i < 4; ++i)
#pragma unroll
            for (int j = 0; j < 5; ++j) {
                float* q = rw + i * 16 * RED_LD + j * 16;
                *(f32x4*)q = acc[i][j] + *(const f32x4*)q;
            }
    }
    __syncthreads();
    {
        const int m = t >> 2, c0 = (t & 3) * 20;
        u16* dst = out + (size_t)(mt * 64 + m) * D_ + nt * 80 + c0;
#pragma unroll
        for (int c = 0; c < 20; c += 4) {
            const f32x4 v = *(const f32x4*)(red + m * RED_LD + c0 + c) + *(const f32x4*)(red + (64 + m) * RED_LD + c0 + c);
            u32x2 pk = {pack_bf16(v[0], v[1]), pack_bf16(v[2], v[3])};
            *(u32x2*)(dst + c) = pk;
        }
    }
    ST2(5);
}

__global__ __launch_bounds__(256, 3) void k2_mix(char* __restrict__ ws, u16* __restrict__ out) {
    __shared__ __attribute__((aligned(16))) char smem[2 * 64 * RED_LD * 4];
    if (blockIdx.x < 256) k2_gemm(ws, out, smem, blockIdx.x);
    else k2_attn(ws, smem, blockIdx.x - 256);
}

extern "C" void kernel_launch(void* const* d_in, const int* in_sizes, int n_in,
                              void* d_out, int out_size, void* d_ws, size_t ws_size,
                              hipStream_t stream) {
    const u16* X = (const u16*)d_in[0];
    const u16* Wqkv = (const u16*)d_in[1];
    const u16* Wo = (const u16*)d_in[2];
    const u16* Wg = (const u16*)d_in[4];
    const u16* Wu = (const u16*)d_in[5];
    const u16* Wd = (const u16*)d_in[6];
    const u16* gw = (const u16*)d_in[7];
    char* ws = (char*)d_ws;
    k1_gemm<<<256, K1NW * 64, 0, stream>>>(X, Wqkv, Wg, Wu, Wo, Wd, gw, ws);
    k2_mix<<<768, 256, 0, stream>>>(ws, (u16*)d_out);
}
```

```cpp
#include <hip/hip_runtime.h>
#include <hip/hip_bf16.h>
#include <stdint.h>
#include <type_traits>

typedef unsigned short u16;
typedef short bf16x8 __attribute__((ext_vector_type(8)));
typedef short s16x4 __attribute__((ext_vector_type(4)));
typedef float f32x4 __attribute__((ext_vector_type(4)));
typedef float f32x2 __attribute__((ext_vector_type(2)));
typedef unsigned u32x4 __attribute__((ext_vector_type(4)));
typedef unsigned u32x2 __attribute__((ext_vector_type(2)));
typedef __bf16 bf2_t __attribute__((ext_vector_type(2)));

#define LDS_AS __attribute__((address_space(3)))

constexpr int S_ = 2048, D_ = 640, F_ = 1728, H_ = 32, DH_ = 20, NQKV = 1920;
constexpr int KS_O = D_ / 32;
constexpr int KS_H = F_ / 32;

constexpr size_t WS_Q = 0;
constexpr size_t QKV_BYTES = (size_t)H_ * S_ * DH_ * 2;
constexpr size_t WS_K = WS_Q + QKV_BYTES;
constexpr size_t WS_V = WS_K + QKV_BYTES;
constexpr size_t WS_H = WS_V + QKV_BYTES + 4096;
constexpr size_t WS_O = WS_H + (size_t)S_ * F_ * 2;
constexpr size_t WS_WO = WS_O + (size_t)S_ * D_ * 2;
constexpr size_t WS_WD = WS_WO + (size_t)D_ * D_ * 2;
constexpr size_t WS_G = WS_WD + (size_t)F_ * D_ * 2;
constexpr size_t WS_FLAG = WS_G + (size_t)S_ * 4;
constexpr int FLAG_LD = 4352;

__device__ __forceinline__ unsigned pack_bf16(float a, float b) {
    bf2_t v = {(__bf16)a, (__bf16)b};
    return __builtin_bit_cast(unsigned, v);
}
__device__ __forceinline__ float rbf(float x) { return (float)(__bf16)x; }
__device__ __forceinline__ float bf2f(u16 v) { return __uint_as_float(((unsigned)v) << 16); }

template <int CTRL> __device__ __forceinline__ float dpp_add(float x) {
    return x + __builtin_bit_cast(float, __builtin_amdgcn_update_dpp(0, __builtin_bit_cast(int, x), CTRL, 0xf, 0xf, false));
}
__device__ __forceinline__ float wave_sum(float x) {
    x = dpp_add<0xB1>(x); x = dpp_add<0x4E>(x); x = dpp_add<0x141>(x); x = dpp_add<0x140>(x);
    const int xi = __builtin_bit_cast(int, x);
    return (__builtin_bit_cast(float, __builtin_amdgcn_readlane(xi, 0)) + __builtin_bit_cast(float, __builtin_amdgcn_readlane(xi, 16))) +
           (__builtin_bit_cast(float, __builtin_amdgcn_readlane(xi, 32)) + __builtin_bit_cast(float, __builtin_amdgcn_readlane(xi, 48)));
}
__device__ __forceinline__ s16x4 tr_read(const char* lds_ptr) {
    return __builtin_amdgcn_ds_read_tr16_b64_v4i16((LDS_AS s16x4*)(lds_ptr));
}
__device__ __forceinline__ bf16x8 cat4(s16x4 a, s16x4 b) {
    bf16x8 r;
    r[0] = a[0]; r[1] = a[1]; r[2] = a[2]; r[3] = a[3];
    r[4] = b[0]; r[5] = b[1]; r[6] = b[2]; r[7] = b[3];
    return r;
}
__device__ __forceinline__ void glds16(const void* g, void* l) {
    __builtin_amdgcn_global_load_lds((const unsigned*)g, (LDS_AS unsigned*)l, 16, 0, 0);
}
__device__ __forceinline__ void glds16_asm(const void* gsrc, unsigned lds_dst) {
    unsigned keep;
    asm volatile("s_mov_b32 %0, m0\n\ts_mov_b32 m0, %2\n\ts_nop 0\n\tglobal_load_lds_dwordx4 %1, off\n\ts_mov_b32 m0, %0"
                 : "=&s"(keep) : "v"(gsrc), "s"(lds_dst) : "memory");
}
__device__ __forceinline__ void glds4_asm(const void* gsrc, unsigned lds_dst) {
    unsigned keep;
    asm volatile("s_mov_b32 %0, m0\n\ts_mov_b32 m0, %2\n\ts_nop 0\n\tglobal_load_lds_dword %1, off\n\ts_mov_b32 m0, %0"
                 : "=&s"(keep) : "v"(gsrc), "s"(lds_dst) : "memory");
}
__device__ __forceinline__ unsigned lds_addr(const void* p) { return (unsigned)(size_t)(LDS_AS const char*)p; }
typedef __amdgpu_buffer_rsrc_t rsrc_t;
constexpr int AUX_AGENT = 16;
__device__ __forceinline__ rsrc_t mk_rsrc(const void* p, unsigned bytes) { return __builtin_amdgcn_make_buffer_rsrc((void*)p, 0, (int)bytes, 0x00020000); }
template <int AUX> __device__ __forceinline__ void bst4(rsrc_t r, int voff, u32x4 v) { __builtin_amdgcn_raw_buffer_store_b128(v, r, voff, 0, AUX); }
template <int AUX> __device__ __forceinline__ void bst2(rsrc_t r, int voff, u32x2 v) { __builtin_amdgcn_raw_buffer_store_b64(v, r, voff, 0, AUX); }
#define WAIT_VM(n) asm volatile("s_waitcnt vmcnt(%0)" :: "n"(n) : "memory")
#define CBAR() asm volatile("" ::: "memory")
#define STAMP(kid, i) do {} while (0)
#define STAMPK(kid, i) do { if ((i) == 3) STAMP(kid, i); } while (0)

#define ET(i) do {} while (0)
#define ETW() do {} while (0)
#define ENTARG
#define ENTPASS
#define WPG 64
#define OSC 0
#define ESC
#define HAUX 16
#define QAUX 16
#define WSC1
#define K1SB 0
#define K1P0 0
#define K1UNR 1
constexpr int K1_ABYTES = 256 * 64 * 2;
constexpr int K1_BSLOT = 32 * 192 * 2;
constexpr int K1_BOFF = 3 * K1_ABYTES;
constexpr int K1_AUX = K1_BOFF + 5 * K1_BSLOT;
constexpr int K1_FLG = K1_AUX + 2048;
constexpr int K1_LDS = K1_FLG + 96;
#define K1FJ 2
#define K1FS 6
#define PFL 4
#define MF1(c, b, a) c = __builtin_amdgcn_mfma_f32_16x16x32_bf16(b, a, c, 0, 0, 0)
typedef volatile LDS_AS unsigned* lflag_t;
template <int SLP> __device__ __forceinline__ void k1_wait_rel(unsigned sb0, int lane, unsigned n) {
    lflag_t p = (lflag_t)(size_t)(sb0 + K1_FLG + (lane & 7) * 4);
    while (__builtin_amdgcn_ballot_w64(*p < n) != 0ull) { if (SLP) __builtin_amdgcn_s_sleep(SLP); }
    CBAR();
}
__device__ __forceinline__ void glds_x4(unsigned lds, const void* sb, unsigned v0, unsigned v1, unsigned v2, unsigned v3) {
    unsigned keep;
    asm volatile("s_mov_b32 %0, m0\n\ts_mov_b32 m0, %1\n\ts_nop 0\n\t"
                 "global_load_lds_dwordx4 %3, %2\n\t"
                 "global_load_lds_dwordx4 %4, %2 offset:1024\n\t"
                 "global_load_lds_dwordx4 %5, %2 offset:2048\n\t"
                 "global_load_lds_dwordx4 %6, %2 offset:3072\n\t"
                 "s_mov_b32 m0, %0"
                 : "=&s"(keep) : "s"(lds), "s"(sb), "v"(v0), "v"(v1), "v"(v2), "v"(v3) : "memory");
}
__device__ __forceinline__ void glds_x2(unsigned lds, const void* sb, unsigned v0, unsigned v1) {
    unsigned keep;
    asm volatile("s_mov_b32 %0, m0\n\ts_mov_b32 m0, %1\n\ts_nop 0\n\t"
                 "global_load_lds_dwordx4 %3, %2\n\t"
                 "global_load_lds_dwordx4 %4, %2 offset:1024\n\t"
                 "s_mov_b32 m0, %0"
                 : "=&s"(keep) : "s"(lds), "s"(sb), "v"(v0), "v"(v1) : "memory");
}
__device__ __forceinline__ void glds_x1(unsigned lds, const void* sb, unsigned v0) {
    unsigned keep;
    asm volatile("s_mov_b32 %0, m0\n\ts_mov_b32 m0, %1\n\ts_nop 0\n\t"
                 "global_load_lds_dwordx4 %3, %2\n\t"
                 "s_mov_b32 m0, %0"
                 : "=&s"(keep) : "s"(lds), "s"(sb), "v"(v0) : "memory");
}
__device__ __forceinline__ int lane_id() {
    int l;
    asm volatile("v_mbcnt_lo_u32_b32 %0, -1, 0\n\tv_mbcnt_hi_u32_b32 %0, -1, %0" : "=v"(l));
    return l;
}
__device__ __forceinline__ s16x4 tr_read_l(unsigned a) { return __builtin_amdgcn_ds_read_tr16_b64_v4i16((LDS_AS s16x4*)(size_t)a); }

template <int NJ>
__device__ __forceinline__ void k1_epilogue(f32x4 (&acc)[4][NJ], char* __restrict__ ws, char* smem, int m0, int c0, int nt, int wid) {
    const int lane = lane_id();
    const int t = wid * 64 + lane;
    const int wr = wid >> 1, wc = wid & 1;
    const int fr = lane & 15, g = lane >> 4;
    const rsrc_t rw = mk_rsrc(ws, (unsigned)WS_FLAG);
    if (NJ == 5) {
        const unsigned sb0 = lds_addr(smem);
        CBAR(); *(lflag_t)(size_t)(sb0 + K1_FLG + wid * 4) = 21u; CBAR();
        k1_wait_rel<0>(sb0, lane, 21u);
        const float qs = (nt < 4) ? 0.22360679774997896f * 1.4426950408889634f : 1.f;
#pragma unroll
        for (int j = 0; j < NJ; ++j) {
            const int n = wc * 80 + j * 16 + g * 4;
            const int hh = n / DH_, d = n % DH_;
            char* dst = smem + hh * 10240 + (wr * 64 + fr) * 40 + d * 2;
#pragma unroll
            for (int i = 0; i < 4; ++i) {
                u32x2 a = {pack_bf16(acc[i][j][0] * qs, acc[i][j][1] * qs), pack_bf16(acc[i][j][2] * qs, acc[i][j][3] * qs)};
                *(u32x2*)(dst + i * 640) = a;
            }
        }
        CBAR(); *(lflag_t)(size_t)(sb0 + K1_FLG + wid * 4) = 22u; CBAR();
        k1_wait_rel<0>(sb0, lane, 22u);
        const int which = nt >> 2;
        const int h0 = (nt & 3) * 8;
        char* dbase = ws + WS_Q + (size_t)which * QKV_BYTES + ((size_t)h0 * S_ + m0) * 40;
        const int dofs = (int)(WS_Q + (size_t)which * QKV_BYTES + ((size_t)h0 * S_ + m0) * 40);
#pragma unroll
        for (int it = 0; it < 10; ++it) {
            const int e = it * 512 + t;
            const int hh = e / 640, r = e % 640;
            const u32x4 v = *(const u32x4*)(smem + e * 16);
            bst4<QAUX>(rw, dofs + hh * (S_ * 40) + r * 16, v);
        }
    } else {
        u16* hF = (u16*)(ws + WS_H);
#pragma unroll
        for (int j = 0; j < 3; ++j) {
            const int f = c0 + wc * 48 + j * 16 + g * 4;
            const int ks = f >> 5;
            const int lq = (f & 31) >> 3;
            const int hf = f & 4;
#pragma unroll
            for (int i = 0; i < 4; ++i) {
                const int m = m0 + wr * 64 + i * 16 + fr;
                unsigned pk[2];
#pragma unroll
                for (int r = 0; r < 2; ++r) {
                    const f32x2 gt = {acc[i][j][2 * r], acc[i][j][2 * r + 1]};
                    const f32x2 up = {acc[i][(j + 3) % NJ][2 * r], acc[i][(j + 3) % NJ][2 * r + 1]};
                    f32x2 e = gt * -1.4426950408889634f;
                    e = (f32x2){__builtin_amdgcn_exp2f(e[0]), __builtin_amdgcn_exp2f(e[1])};
                    e = e + 1.f;
                    const f32x2 rc = {__builtin_amdgcn_rcpf(e[0]), __builtin_amdgcn_rcpf(e[1])};
                    const f32x2 hv = (gt * up) * rc;
                    pk[r] = pack_bf16(hv[0], hv[1]);
                }
                u32x2 a = {pk[0], pk[1]};
                u16* dst = hF + ((size_t)((m >> 4) * KS_H + ks) * 64 + (m & 15) + 16 * lq) * 8 + hf;
                bst2<HAUX>(rw, (int)WS_H + ((((m >> 4) * KS_H + ks) * 64 + (m & 15) + 16 * lq) * 8 + hf) * 2, a);
            }
        }
    }
}

template <int NJ>
__device__ __forceinline__ void k1_tile(const u16* __restrict__ X, const u16* __restrict__ W0, const u16* __restrict__ W1,
                                        char* __restrict__ ws, char* smem, int mt, int nt, int xs, int xcnt, int uw ENTARG) {
    constexpr int BN = 32 * NJ;
    constexpr int ROWB = (NJ == 5) ? 320 : 192;
    constexpr int UPR = ROWB / 16;
    constexpr int NBH = UPR / 2;
    constexpr int NB = 2 * NJ;
    constexpr int LDB = (NJ == 5) ? NQKV : F_;
    const int m0 = mt * 256;
    const int c0 = (NJ == 5) ? nt * 160 : nt * 96;
    const unsigned sb0 = lds_addr(smem);
    auto boffs = [&](int i, int lane) {
        const int p = i * 64 + lane;
        const int k = p / UPR, u = p % UPR;
        return (unsigned)((k * LDB + (((u >> 1) ^ ((k >> 3) & 1)) << 4) + (u & 1) * 8) * 2);
    };
    auto aoffs_f = [&](int lane) { return (unsigned)((lane >> 3) * (D_ * 2) + (((lane & 7) ^ (lane >> 3)) << 4)); };
    const char* xa = (const char*)(X + (size_t)m0 * D_);
    const char* wb0 = (const char*)(W0 + c0);
    const char* wb1 = (const char*)(W1 + c0);

#define KRA 0
#define KRB 0
    const int rot = (xs * KRA + ((mt & 3) * KRB >> 1)) % 10;
    if (uw >= 8) {
        const int dw = uw - 8;
        const int lane = lane_id();
        const unsigned aoffs = aoffs_f(lane);
        if (dw == 0 && lane < 24) *(lflag_t)(size_t)(sb0 + K1_FLG + lane * 4) = 0u;
        constexpr int NBP = NJ;
        unsigned vb[NBP], va[4];
#pragma unroll
        for (int i = 0; i < NBP; ++i) vb[i] = boffs((NJ == 5) ? 5 * dw + i : i, lane) - (i & 3) * 1024;
#pragma unroll
        for (int r = 0; r < 4; ++r) va[r] = aoffs + r * (8 * D_ * 2 - 1024);
        const char* xs0 = xa + dw * (128 * D_ * 2);
        const char* ws0 = (NJ == 6 && dw) ? wb1 : wb0;
        auto loadA = [&](int kt, int q0) {
            const unsigned base = sb0 + (kt % 3) * K1_ABYTES + dw * 16384;
            const int kr = kt + rot;
            const char* src = xs0 + (kr >= 10 ? kr - 10 : kr) * 128;
#pragma unroll
            for (int q = q0; q < q0 + 2; ++q) glds_x4(base + q * 4096, src + q * (32 * D_ * 2), va[0], va[1], va[2], va[3]);
        };
        auto loadB = [&](int h) {
            const unsigned base = sb0 + K1_BOFF + (h % 5) * K1_BSLOT + dw * (NBP * 1024);
            const int hr = h + 2 * rot;
            const char* s0 = ws0 + (size_t)(hr >= 20 ? hr - 20 : hr) * (32 * LDB * 2);
            glds_x4(base, s0, vb[0], vb[1], vb[2], vb[3]);
            if (NJ == 5) glds_x1(base + 4096, s0, vb[4]);
            else glds_x2(base + 4096, s0, vb[4], vb[5 % NBP]);
        };
        asm volatile("s_waitcnt lgkmcnt(0)" ::: "memory");
        __builtin_amdgcn_s_barrier();
        CBAR();
        ET(1);
        volatile LDS_AS u16* rdy = (volatile LDS_AS u16*)(size_t)(sb0 + K1_FLG + 32 + (lane & 7) * 4 + dw * 2);
        loadB(1);
#pragma unroll 1
        for (int kt = 1; kt < 10; ++kt) {
            if (kt >= 3) k1_wait_rel<0>(sb0, lane, 2 * kt - 4);
            loadB(2 * kt); loadA(kt, 0);
            WAIT_VM(2 * NBP + 8);
            *rdy = (u16)(2 * kt - 1);
            loadA(kt, 2);
            WAIT_VM(NBP + 16);
            *rdy = (u16)(2 * kt);
            if (kt >= 2) k1_wait_rel<0>(sb0, lane, 2 * kt - 3);
            loadB(2 * kt + 1);
        }
        WAIT_VM(NBP);
        *rdy = (u16)19;
        WAIT_VM(0);
        *rdy = (u16)30;
        ET(4); ETW();
        return;
    }

    ET(1);
    const int lane = lane_id();
    {
        const unsigned aoffs = aoffs_f(lane);
        wb0 += (size_t)rot * (64 * LDB * 2); wb1 += (size_t)rot * (64 * LDB * 2);
        glds_x4(sb0 + uw * 4096, xa + uw * (32 * D_ * 2) + rot * 128, aoffs, aoffs + (8 * D_ * 2 - 1024), aoffs + 2 * (8 * D_ * 2 - 1024), aoffs + 3 * (8 * D_ * 2 - 1024));
        if (NJ == 5) {
            if (uw < 5) glds_x2(sb0 + K1_BOFF + uw * 2048, wb0, boffs(2 * uw, lane), boffs(2 * uw + 1, lane) - 1024);
        } else {
            if (uw < 6) {
                const unsigned v = boffs(uw, lane);
                glds_x1(sb0 + K1_BOFF + uw * 1024, wb0, v);
                glds_x1(sb0 + K1_BOFF + 6144 + uw * 1024, wb1, v);
            }
        }
    }
    ET(2);
    __builtin_amdgcn_s_barrier();
    CBAR();
    const int wr = uw >> 1, wc = uw & 1;
    const int fr = lane & 15, g = lane >> 4;
    f32x4 acc[4][NJ];
#pragma unroll
    for (int i = 0; i < 4; ++i)
#pragma unroll
        for (int j = 0; j < NJ; ++j) acc[i][j] = (f32x4){0.f, 0.f, 0.f, 0.f};

    unsigned aa[2][2], be, bo;
    {
        const int row = wr * 64 + fr;
        aa[0][0] = sb0 + row * 128 + ((g ^ (row & 7)) << 4);
        aa[1][0] = sb0 + row * 128 + (((4 + g) ^ (row & 7)) << 4);
        aa[0][1] = aa[0][0] + 2 * K1_ABYTES;
        aa[1][1] = aa[1][0] + 2 * K1_ABYTES;
        const int q4 = (lane & 15) >> 2, p4 = lane & 3;
        const unsigned bb = sb0 + K1_BOFF + (8 * g + q4) * ROWB + p4 * 8 + wc * (NJ == 5 ? 5 : 3) * 32;
        const int sx = ((g & 1) << 5) * (wc ? -1 : 1);
        be = bb + sx; bo = bb - sx;
    }
    unsigned frel = sb0 + K1_FLG + uw * 4;
    unsigned frdy = sb0 + K1_FLG + 32 + uw * 4 + (lane & 1) * 2;
    asm volatile("" : "+v"(aa[0][0]), "+v"(aa[1][0]), "+v"(aa[0][1]), "+v"(aa[1][1]), "+v"(be), "+v"(bo), "+v"(frel), "+v"(frdy));
    WAIT_VM(0);
    ET(3);
    *(lflag_t)(size_t)(frel + 64) = 1u;
    {
        lflag_t p = (lflag_t)(size_t)(sb0 + K1_FLG + 64 + (lane & 7) * 4);
        while (__builtin_amdgcn_ballot_w64(*p == 0u) != 0ull) {}
    }
    CBAR();
    ET(4);
    bf16x8 af[2][4], bq[NJ];
    auto rdA = [&](int h, int i) {
        const int st = (h >> 1) % 3;
        return *(const LDS_AS bf16x8*)(size_t)(aa[h & 1][st == 2] + (st == 1 ? K1_ABYTES : 0) + i * 2048);
    };
    auto rdB = [&](int h, int j) {
        const int jj = (NJ == 5) ? j : j % 3, sub = (NJ == 5) ? 0 : (j / 3) * 6144;
        const unsigned b = ((jj & 1) ? bo : be) + (h % 5) * K1_BSLOT + jj * 32 + sub;
        s16x4 lo = tr_read_l(b);
        s16x4 hi = tr_read_l(b + 4 * ROWB);
        return cat4(lo, hi);
    };
    auto rd_rdy = [&]() { return (unsigned)*(volatile LDS_AS u16*)(size_t)frdy; };
#define WAIT_RDY(fl, need) asm volatile("v_cmp_gt_u32 vcc, %2, %0\n\ts_cbranch_vccz L_ok_%=\n" \
        "L_spin_%=:\n\tds_read_u16 %0, %1\n\ts_waitcnt lgkmcnt(0)\n\tv_cmp_gt_u32 vcc, %2, %0\n\ts_cbranch_vccnz L_spin_%=\n" \
        "L_ok_%=:" : "+v"(fl) : "v"(frdy), "n"(need) : "vcc", "memory")
    auto release = [&](unsigned n) { CBAR(); *(lflag_t)(size_t)frel = n; CBAR(); };
#pragma unroll
    for (int i = 0; i < 4; ++i) af[0][i] = rdA(0, i);
    __builtin_amdgcn_sched_barrier(0);
#pragma unroll
    for (int j = 0; j < NJ; ++j) { bq[j] = rdB(0, j); __builtin_amdgcn_sched_barrier(0); }
    release(1u);
    unsigned fl = rd_rdy();
    __builtin_amdgcn_sched_barrier(0);
#pragma unroll
    for (int p = 0; p < 20; ++p) {
#pragma unroll
        for (int sl = 0; sl < 4 * NJ; ++sl) {
            const int j = sl >> 2, i = sl & 3;
            MF1(acc[i][j], bq[j], af[p & 1][i]);
            __builtin_amdgcn_sched_barrier(0);
            if (sl == 0 && p > 0) bq[NJ - 1] = rdB(p, NJ - 1);
            if (sl == 1 && p > 0 && p < 16) release(p + 1);
            if (p < 19) {
                if (sl == 2) WAIT_RDY(fl, p + 2);
                if (sl >= 4 && i == 0) bq[j - 1] = rdB(p + 1, j - 1);
                if (sl == 5) af[(p + 1) & 1][0] = rdA(p + 1, 0);
                if (sl == 6) af[(p + 1) & 1][1] = rdA(p + 1, 1);
                if (sl == 7) af[(p + 1) & 1][2] = rdA(p + 1, 2);
                if (sl == 9) af[(p + 1) & 1][3] = rdA(p + 1, 3);
                if (sl == 4 * NJ - K1FS && p < 18) fl = rd_rdy();
            }
            __builtin_amdgcn_sched_barrier(0);
        }
    }

    ET(1);
    k1_epilogue<NJ>(acc, ws, smem, m0, c0, nt, uw);
    ET(5); ETW();
}

#define AUXM 0
#define AUXS 12
#define K1NW 12
template <int NU, int NR, bool EARLY>
__device__ __forceinline__ void k1_aux(const u16* __restrict__ X, const u16* __restrict__ Wo, const u16* __restrict__ Wd,
                                       const u16* __restrict__ gate_w, char* __restrict__ ws, char* wl,
                                       int u0, int us, int ue, int rb, int r0, int rs, int re) {
    const int lane = lane_id();
    float* gout = (float*)(ws + WS_G);
    const int q4 = (lane & 15) >> 2, p4 = lane & 3, g = lane >> 4;
    const rsrc_t rw = mk_rsrc(ws, (unsigned)WS_FLAG);
    auto fetch = [&](int unit, u32x4 (&r)[4]) {
        const int ksg = unit / 10, cc = unit % 10;
        const u16* src = (ksg < KS_O) ? (Wo + (size_t)ksg * 32 * D_) : (Wd + (size_t)(ksg - KS_O) * 32 * D_);
#pragma unroll
        for (int i = 0; i < 4; ++i) r[i] = *(const u32x4*)(src + (size_t)(i * 8 + (lane >> 3)) * D_ + cc * 64 + (lane & 7) * 8);
    };
    auto emit = [&](int unit, const u32x4 (&r)[4]) {
        const int ksg = unit / 10, cc = unit % 10;
#pragma unroll
        for (int hf = 0; hf < 2; ++hf) {
            CBAR();
            if (((lane >> 2) & 1) == hf) {
#pragma unroll
                for (int i = 0; i < 4; ++i) *(u32x4*)(wl + (i * 8 + (lane >> 3)) * 64 + (lane & 3) * 16) = r[i];
            }
            CBAR();
#pragma unroll
            for (int j = 0; j < 2; ++j) {
                const char* a = wl + (8 * g + q4) * 64 + j * 32 + p4 * 8;
                s16x4 lo = tr_read(a);
                s16x4 hi = tr_read(a + 4 * 64);
                bf16x8 v = cat4(lo, hi);
                const int nt = cc * 4 + hf * 2 + j;
                const int dst = (ksg < KS_O) ? (int)WS_WO + ((nt * KS_O + ksg) * 64 + lane) * 16
                                             : (int)WS_WD + ((nt * KS_H + (ksg - KS_O)) * 64 + lane) * 16;
                bst4<AUX_AGENT>(rw, dst, __builtin_bit_cast(u32x4, v));
            }
        }
    };
    u32x4 r[NU ? NU : 1][4];
    u32x4 x0[NR ? NR : 1], x1[NR ? NR : 1], w0, w1;
    auto gfetch = [&]() {
        w0 = *(const u32x4*)(gate_w + lane * 8);
        w1 = *(const u32x4*)(gate_w + 512 + (lane & 15) * 8);
#pragma unroll
        for (int i = 0; i < NR; ++i) {
            int row = r0 + rs * i;
            row = rb + (row < re ? row : r0);
            x0[i] = *(const u32x4*)(X + (size_t)row * D_ + lane * 8);
            x1[i] = *(const u32x4*)(X + (size_t)row * D_ + 512 + (lane & 15) * 8);
        }
    };
    if (NU) {
        const bool extra = u0 + (NU - 1) * us < ue;
#pragma unroll
        for (int u = 0; u < NU - 1; ++u) fetch(u0 + u * us, r[u]);
        if (extra) fetch(u0 + (NU - 1) * us, r[NU - 1]);
        if (EARLY && NR) gfetch();
#pragma unroll
        for (int u = 0; u < NU - 1; ++u) emit(u0 + u * us, r[u]);
        if (extra) emit(u0 + (NU - 1) * us, r[NU - 1]);
    }
    CBAR();
    if (NR == 0) return;
    if (!EARLY || !NU) gfetch();
    if (lane >= 16) w1 = (u32x4){0u, 0u, 0u, 0u};
    float sacc[NR ? NR : 1];
#pragma unroll
    for (int i = 0; i < NR; ++i) {
        float a = 0.f;
#pragma unroll
        for (int e = 0; e < 4; ++e) {
            const unsigned xa = x0[i][e], wa = w0[e], xb = x1[i][e], wb = w1[e];
            a = __builtin_amdgcn_fdot2_f32_bf16(__builtin_bit_cast(bf2_t, xa), __builtin_bit_cast(bf2_t, wa), a, false);
            a = __builtin_amdgcn_fdot2_f32_bf16(__builtin_bit_cast(bf2_t, xb), __builtin_bit_cast(bf2_t, wb), a, false);
        }
        sacc[i] = a;
    }
    {
        float z = 0.f;
#pragma unroll
        for (int i = 0; i < NR; ++i) {
            const float tot = wave_sum(sacc[i]);
            z = (lane == i) ? tot : z;
        }
        const int row = r0 + rs * lane;
        z = rbf(z);
        if (lane < NR && row < re) gout[rb + row] = rbf(1.f / (1.f + __expf(-z)));
    }
}

__global__ __launch_bounds__(K1NW * 64) void k1_gemm(const u16* __restrict__ X, const u16* __restrict__ Wqkv,
                                               const u16* __restrict__ Wg, const u16* __restrict__ Wu,
                                               const u16* __restrict__ Wo, const u16* __restrict__ Wd,
                                               const u16* __restrict__ gate_w, char* __restrict__ ws) {
    __shared__ __attribute__((aligned(16))) char smem[K1_LDS];
    const int bid = blockIdx.x;
    const int xcd = bid & 7, idx = bid >> 3;
    const int ng = xcd >> 1, mg = xcd & 1;
    const int cnt = (ng < 2) ? 8 : 7;
    const int nstart = (ng < 2) ? ng * 8 : 16 + (ng - 2) * 7;
    const int uw = __builtin_amdgcn_readfirstlane(threadIdx.x >> 6);
    if (idx >= 4 * cnt) {
        const int sbk = (xcd - 4) * 4 + (idx - 28);
        if (threadIdx.x < 32)
            *(u32x4*)(ws + WS_FLAG + (size_t)(sbk * 2 + (threadIdx.x >> 4)) * FLAG_LD + (threadIdx.x & 15) * 16) = (u32x4){0u, 0u, 0u, 0u};
        constexpr int NWV = 16 * K1NW;
        const int gw = sbk * K1NW + uw, wq = (((xcd - 4) >> 1) * 4 + (idx - 28)) * K1NW + uw;
        constexpr int NRS = (1024 + NWV / 2 - 1) / (NWV / 2);
        if (AUXM == 0) k1_aux<(740 + NWV - 1) / NWV, NRS, false>(X, Wo, Wd, gate_w, ws, smem + uw * 2048, gw, NWV, 740, mg * 1024, wq, NWV / 2, 1024);
        if (AUXM == 1) k1_aux<1, 0, false>(X, Wo, Wd, gate_w, ws, smem + uw * 2048, 720 + uw * 16 + sbk, 0, 740, 0, 0, 0, 0);
        if (AUXM == 2) k1_aux<1, NRS, true>(X, Wo, Wd, gate_w, ws, smem + uw * 2048, 720 + uw * 16 + sbk, 0, 740, mg * 1024, wq, NWV / 2, 1024);
        if (AUXM == 3) k1_aux<(740 + NWV - 1) / NWV, 0, false>(X, Wo, Wd, gate_w, ws, smem + uw * 2048, gw, NWV, 740, mg * 1024, wq, NWV / 2, 1024);
        if (AUXM == 4) k1_aux<(260 + NWV - 1) / NWV, 0, false>(X, Wo, Wd, gate_w, ws, smem + uw * 2048, 480 + gw, NWV, 740, mg * 1024, wq, NWV / 2, 1024);
        return;
    }
    const int mt = mg * 4 + (idx & 3);
    const int nt = nstart + (idx >> 2);
    if (uw >= 10) {
        if (AUXM == 0 || uw > 10) return;
        __builtin_amdgcn_s_barrier();
        CBAR();
        k1_wait_rel<4>(lds_addr(smem), lane_id(), AUXS);
        const int lin = mt * 30 + nt;
        if (AUXM == 1) k1_aux<3, 9, true>(X, Wo, Wd, gate_w, ws, smem + K1_AUX, lin, 240, 720, mt * 256, nt, 30, 256);
        if (AUXM == 2) k1_aux<3, 0, true>(X, Wo, Wd, gate_w, ws, smem + K1_AUX, lin, 240, 720, mt * 256, nt, 30, 256);
        if (AUXM == 3) k1_aux<0, 9, true>(X, Wo, Wd, gate_w, ws, smem + K1_AUX, lin, 240, 720, mt * 256, nt, 30, 256);
        if (AUXM == 4) k1_aux<2, 9, true>(X, Wo, Wd, gate_w, ws, smem + K1_AUX, lin, 240, 480, mt * 256, nt, 30, 256);
        return;
    }
    if (nt < 12) k1_tile<5>(X, Wqkv, Wqkv, ws, smem, mt, nt, idx >> 2, cnt, uw ENTPASS);
    else k1_tile<6>(X, Wg, Wu, ws, smem, mt, nt - 12, idx >> 2, cnt, uw ENTPASS);
}

typedef unsigned long long u64;
#define GDELAY 8
#define PF2 0
#define ACQ 0
#define ROT 0
#define ROLL 0
#define GPRIO 0
#define POLLSLP 4
#define APRIO 2
#define ST2(i) do {} while (0)
#define ST2ID() do {} while (0)
__device__ __forceinline__ void st_agent(void* p, u32x2 v) {
    __hip_atomic_store((u64*)p, __builtin_bit_cast(u64, v), __ATOMIC_RELAXED, __HIP_MEMORY_SCOPE_AGENT);
}
template <int AUX> __device__ __forceinline__ bf16x8 bld(rsrc_t r, int voff, int soff) {
    return __builtin_bit_cast(bf16x8, __builtin_amdgcn_raw_buffer_load_b128(r, voff, soff, AUX));
}

__device__ __forceinline__ float fmx(float a, float b) { return __builtin_elementwise_maximum(a, b); }

typedef float f32x16 __attribute__((ext_vector_type(16)));
#define MF32(a, b, c) __builtin_amdgcn_mfma_f32_32x32x16_bf16(a, b, c, 0, 0, 0)
#define MF8(a, b, c) __builtin_amdgcn_mfma_f32_32x32x8bf16_1k(a, b, c, 0, 0, 0)
constexpr int AT_V = 15360, AT_PAD = 30720, AT_ONE = AT_PAD + 64;
constexpr int AT_LDS = AT_ONE + 35 * 320 + 16;
template <bool EDGE>
__device__ __forceinline__ void k2_attn_wave(char* __restrict__ oF, const char* smem, const bf16x8 q1, const s16x4 q2,
                                             const float gg, int h, int qw, int rbase, int lane) {
    const int hi = lane >> 5, ql = lane & 31;
    const float NEG = -__builtin_inff();
    const int ts = 8 - (qw >> 5);
    const char* kb = smem + (rbase + ql) * 40;
    const char* ka = kb + hi * 16;
    const char* vb;
    {
        const int q4 = (lane & 15) >> 2, p4 = lane & 3, db = (lane >> 4) & 1;
        vb = smem + AT_V + (rbase + 4 * hi + q4) * 40 + db * 32 + p4 * 8;
        if (db == 1 && p4 >= 1) vb = smem + AT_ONE + (p4 >> 1) * 8;
    }
    auto rd16 = [&](const char* p) {
        const u32x2 lo = *(const u32x2*)p, up = *(const u32x2*)(p + 8);
        const u32x4 v = {lo[0], lo[1], up[0], up[1]};
        return __builtin_bit_cast(bf16x8, v);
    };
    auto rdK1 = [&](int t) { return rd16(ka + t * 1280); };
    auto rdK2 = [&](int t) { return *(const s16x4*)(kb + t * 1280 + 32); };
    auto rdV = [&](int t, int a) {
        s16x4 lo = tr_read(vb + t * 1280 + a * 640);
        s16x4 up = tr_read(vb + t * 1280 + a * 640 + 320);
        return cat4(lo, up);
    };
    f32x16 zero;
#pragma unroll
    for (int i = 0; i < 16; ++i) zero[i] = 0.f;
#define SB0() __builtin_amdgcn_sched_barrier(0)
    auto TL = [](int u) { return u < 7 ? u + 1 : (u == 7 ? 0 : 8); };
    float mx = NEG, my = NEG;
    f32x16 s[3];
    bf16x8 k1[2], vf[2][2];
    s16x4 k2[2];
    k1[0] = rdK1(TL(0)); k2[0] = rdK2(TL(0)); k1[1] = rdK1(TL(1)); k2[1] = rdK2(TL(1));
    SB0();
#pragma unroll
    for (int u = -1; u < 9; ++u) {
        if (u + 1 <= 8) {
            s[(u + 1) % 3] = MF32(k1[(u + 1) & 1], q1, zero);
            SB0();
            if (u + 3 <= 8) k1[(u + 1) & 1] = rdK1(TL(u + 3));
            else if (u + 3 == 9) k1[(u + 1) & 1] = rdK1(1);
            else k1[(u + 1) & 1] = rdK1(2);
        }
        SB0();
        const int p = u - 1;
        if (p >= 0 && p < 7 && (!EDGE || p + 1 >= ts)) {
            const f32x16 x = s[p % 3];
            mx = fmx(fmx(mx, x[0]), x[1]); my = fmx(fmx(my, x[2]), x[3]);
            mx = fmx(fmx(mx, x[4]), x[5]); my = fmx(fmx(my, x[6]), x[7]);
        }
        SB0();
        if (u >= 0) {
            s[u % 3] = MF8(k2[u & 1], q2, s[u % 3]);
            SB0();
            if (u + 2 <= 8) k2[u & 1] = rdK2(TL(u + 2));
            else if (u + 2 == 9) k2[u & 1] = rdK2(1);
            else k2[u & 1] = rdK2(2);
        }
        SB0();
        if (p >= 0 && p < 7 && (!EDGE || p + 1 >= ts)) {
            const f32x16 x = s[p % 3];
            mx = fmx(fmx(mx, x[8]), x[9]); my = fmx(fmx(my, x[10]), x[11]);
            mx = fmx(fmx(mx, x[12]), x[13]); my = fmx(fmx(my, x[14]), x[15]);
        }
        SB0();
    }
    vf[0][0] = rdV(8, 0); vf[0][1] = rdV(8, 1);
    if (!EDGE) { vf[1][0] = rdV(0, 0); vf[1][1] = rdV(0, 1); }
    const int z = ql - 4 * hi;
    f32x16 mg;
#pragma unroll
    for (int i = 0; i < 16; ++i) {
        const bool c = 8 * (i >> 2) + (i & 3) > z;
        mg[i] = c ? (EDGE ? NEG : s[1][i]) : s[2][i];
    }
#pragma unroll
    for (int i = 0; i < 16; i += 4) { mx = fmx(fmx(mx, mg[i]), mg[i + 1]); my = fmx(fmx(my, mg[i + 2]), mg[i + 3]); }
    mx = fmx(mx, my);
    {
        const unsigned mi = __builtin_bit_cast(unsigned, mx);
        auto r = __builtin_amdgcn_permlane32_swap(mi, mi, false, false);
        mx = fmx(__builtin_bit_cast(float, (unsigned)r[0]), __builtin_bit_cast(float, (unsigned)r[1]));
    }
    const float nm = -mx;
    f32x16 cm;
#pragma unroll
    for (int i = 0; i < 16; ++i) cm[i] = nm;
    SB0();
    f32x16 o = zero;
    s[1] = MF32(k1[1], q1, cm);
    SB0();
    k1[1] = rdK1(3);
    {
        u32x4 pa, pb;
#pragma unroll
        for (int a = 0; a < 2; ++a) {
#pragma unroll
            for (int e = 0; e < 4; ++e) {
                const int i = 8 * a + 2 * e;
                const float x0 = __builtin_amdgcn_exp2f(mg[i] + nm), x1 = __builtin_amdgcn_exp2f(mg[i + 1] + nm);
                const unsigned pk = pack_bf16(x0, x1);
                if (EDGE) pa[e] = pk;
                else {
                    const bool c0 = 8 * (i >> 2) + (i & 3) > z, c1 = 8 * (i >> 2) + (i & 3) + 1 > z;
                    pb[e] = pack_bf16(c0 ? x0 : 0.f, c1 ? x1 : 0.f);
                    pa[e] = pk ^ pb[e];
                }
                if (a == 0 && e == 1) { SB0(); s[1] = MF8(k2[1], q2, s[1]); SB0(); k2[1] = rdK2(3); }
            }
            SB0();
            o = MF32(vf[0][a], __builtin_bit_cast(bf16x8, pa), o);
            SB0();
            if (!EDGE) {
                if (a == 1) { s[0] = MF32(k1[0], q1, cm); SB0(); k1[0] = rdK1(4); }
                o = MF32(vf[1][a], __builtin_bit_cast(bf16x8, pb), o);
                SB0();
            } else if (a == 1) { s[0] = MF32(k1[0], q1, cm); SB0(); k1[0] = rdK1(4); }
            vf[1][a] = rdV(1, a);
            vf[0][a] = rdV(2, a);
            SB0();
        }
    }
    u32x4 pk;
    float xa, xb;
#pragma unroll
    for (int t = 1; t < 8; ++t) {
        const bool on = !EDGE || t >= ts;
        auto EX = [&](int i) { xa = __builtin_amdgcn_exp2f(s[t & 1][i]); xb = __builtin_amdgcn_exp2f(s[t & 1][i + 1]); };
        float ya, yb;
        if (t < 7) { s[(t + 1) & 1] = MF8(k2[(t + 1) & 1], q2, s[(t + 1) & 1]); SB0(); if (t + 3 < 8) k2[(t + 1) & 1] = rdK2(t + 3); }
        SB0();
        if (on) {
            EX(0); SB0();
            ya = xa; yb = xb; EX(2); pk[0] = pack_bf16(ya, yb); SB0();
            ya = xa; yb = xb; EX(4); pk[1] = pack_bf16(ya, yb); SB0();
            ya = xa; yb = xb; EX(6); pk[2] = pack_bf16(ya, yb); SB0();
            ya = xa; yb = xb; EX(8); pk[3] = pack_bf16(ya, yb); SB0();
            ya = xa; yb = xb; EX(10);
            const unsigned n0 = pack_bf16(ya, yb); SB0();
            o = MF32(vf[t & 1][0], __builtin_bit_cast(bf16x8, pk), o);
            SB0();
            if (t + 2 < 8) vf[t & 1][0] = rdV(t + 2, 0);
            u32x4 pn;
            pn[0] = n0;
            ya = xa; yb = xb; EX(12); pn[1] = pack_bf16(ya, yb); SB0();
            ya = xa; yb = xb; EX(14); pn[2] = pack_bf16(ya, yb); SB0();
            if (!EDGE && t + 2 < 8) { s[t & 1] = MF32(k1[t & 1], q1, cm); SB0(); if (t + 4 < 8) k1[t & 1] = rdK1(t + 4); }
            pn[3] = pack_bf16(xa, xb); SB0();
            o = MF32(vf[t & 1][1], __builtin_bit_cast(bf16x8, pn), o);
            SB0();
            if (t + 2 < 8) vf[t & 1][1] = rdV(t + 2, 1);
        } else if (t + 2 < 8) { vf[t & 1][0] = rdV(t + 2, 0); vf[t & 1][1] = rdV(t + 2, 1); }
        SB0();
        if (EDGE && t + 2 < 8) { s[t & 1] = MF32(k1[t & 1], q1, cm); SB0(); if (t + 4 < 8) k1[t & 1] = rdK1(t + 4); }
        SB0();
    }
    float sum;
    {
        const float o8 = o[8];
        const unsigned si = __float_as_uint(o8);
        auto r = __builtin_amdgcn_permlane32_swap(si, si, false, false);
        sum = __builtin_bit_cast(float, (unsigned)r[1]);
    }
    const float inv = gg * __builtin_amdgcn_rcpf(sum);
    const int m = qw + ql;
    char* ob = oF + ((size_t)(m >> 4) * KS_O * 64 + (m & 15)) * 16;
#pragma unroll
    for (int j = 0; j < 3; ++j) {
        const int k0 = h * DH_ + 8 * j + 4 * hi;
        u32x2 a = {pack_bf16(o[4 * j] * inv, o[4 * j + 1] * inv), pack_bf16(o[4 * j + 2] * inv, o[4 * j + 3] * inv)};
        if (j < 2 || hi == 0) st_agent(ob + ((k0 >> 5) * 64 + 16 * ((k0 & 31) >> 3)) * 16 + (k0 & 4) * 2, a);
    }
}

__device__ __forceinline__ void k2_attn(char* __restrict__ ws, char* smem, int bid) {
    const int h = (bid & 7) + 8 * ((bid >> 3) & 3);
    const int qb = bid >> 5;
    const int q0 = qb * 128;
    const int t = threadIdx.x, lane = t & 63;
    const int w = __builtin_amdgcn_readfirstlane(t >> 6);
    const char* Qb = ws + WS_Q + (size_t)h * S_ * 40;
    const char* Kb = ws + WS_K + (size_t)h * S_ * 40;
    const char* Vb = ws + WS_V + (size_t)h * S_ * 40;
    char* sK = smem;
    char* sV = smem + AT_V;

    ST2(0); ST2ID();
    if (APRIO) __builtin_amdgcn_s_setprio(APRIO);
#pragma unroll
    for (int i = 0; i < 4; ++i) {
        const int p = i * 256 + t;
        if (i < 3 || w < 3) {
            int off = (q0 - 256) * 40 + p * 16;
            off = off < 0 ? 0 : off;
            glds16(Kb + off, sK + p * 16);
            glds16(Vb + off, sV + p * 16);
        }
    }
    if (t < 4) *(u32x4*)(smem + AT_PAD + t * 16) = (u32x4){0u, 0u, 0u, 0u};
    if (t >= 64 && t < 100) *(u32x4*)(smem + AT_ONE + (t - 64) * 320) = (u32x4){0x3f80u, 0u, 0u, 0u};
    const int hi = lane >> 5;
    const int qw = q0 + 32 * w;
    bf16x8 q1;
    s16x4 q2;
    {
        const char* qp = Qb + (size_t)(qw + (lane & 31)) * 40;
        const u32x2 a = *(const u32x2*)(qp + hi * 16), b = *(const u32x2*)(qp + hi * 16 + 8);
        u32x2 c = *(const u32x2*)(qp + 32);
        if (hi) c = (u32x2){0u, 0u};
        const u32x4 v1 = {a[0], a[1], b[0], b[1]};
        q1 = __builtin_bit_cast(bf16x8, v1); q2 = __builtin_bit_cast(s16x4, c);
    }
    const float gg = ((const float*)(ws + WS_G))[qw + (lane & 31)];
    __syncthreads();
    ST2(1);
    char* oF = ws + WS_O;
    if (qb >= 2) k2_attn_wave<false>(oF, smem, q1, q2, gg, h, qw, 32 * w, lane);
    else k2_attn_wave<true>(oF, smem, q1, q2, gg, h, qw, 32 * w, lane);
    ST2(2);
    asm volatile("s_waitcnt vmcnt(0)" ::: "memory");
    ST2(3);
    if (lane == 0)
        __hip_atomic_store((unsigned*)(ws + WS_FLAG + (qw >> 6) * FLAG_LD) + h * 2 + ((qw >> 5) & 1), 1u, __ATOMIC_RELAXED, __HIP_MEMORY_SCOPE_AGENT);
}

__device__ __forceinline__ void k2_gemm(char* __restrict__ ws, u16* __restrict__ out, char* smem, int bid) {
    const int xcd = bid & 7, idx = bid >> 3;
    const int mt = (xcd >> 1) * 8 + (idx & 7);
    const int nt = (xcd & 1) * 4 + (idx >> 3);
    const int t = threadIdx.x, lane = t & 63;
    const int w = __builtin_amdgcn_readfirstlane(t >> 6);
    const int fr = lane & 15, g = lane >> 4;
    const int nsteps = (w < 3) ? 14 : 12;
    const rsrc_t rs = __builtin_amdgcn_make_buffer_rsrc(ws, 0, (int)WS_FLAG, 0x00020000);
    const int vo = lane * 16;
    int ap[4];
#pragma unroll
#define K2SAME 0
    for (int i = 0; i < 4; ++i) ap[i] = (int)WS_H + ((((K2SAME & 1) ? 0 : (K2SAME & 4) ? (mt & 24) : mt) * 4 + (i ^ w)) * KS_H + 14 * w) * 1024;
    int bp = (int)WS_WD + ((((K2SAME & 2) ? 0 : nt) * 5) * KS_H + 14 * w) * 1024;
#define RTA 0
#define RTB 0
    int cur = ((RTA * (nt & 3) + (RTB * (mt & 7) >> 2)) % nsteps) * 1024;
    auto adv = [&]() { cur += 1024; cur = (cur == nsteps * 1024) ? 0 : cur; };

    f32x4 acc[4][5];
#pragma unroll
    for (int i = 0; i < 4; ++i)
#pragma unroll
        for (int j = 0; j < 5; ++j) acc[i][j] = (f32x4){0.f, 0.f, 0.f, 0.f};
    bf16x8 a0[4], b0[5], a1[4], b1[5];
#define SB() __builtin_amdgcn_sched_barrier(0)
#define MFA(c, b, a) asm volatile("v_mfma_f32_16x16x32_bf16 %0, %1, %2, %0" : "+v"(c) : "v"(b), "v"(a))
    const u32x4 rq = {(unsigned)(size_t)ws, (unsigned)((size_t)ws >> 32) & 0xffffu, (unsigned)WS_FLAG, 0x00020000u};
#define LDN(r, so) asm volatile("buffer_load_dwordx4 %0, %1, %2, %3 offen" : "=v"(r) : "v"(vo), "s"(rq), "s"(so) : "memory")
#define LDR(r, so) asm volatile("buffer_load_dwordx4 %0, %1, %2, %3 offen" : "+v"(r) : "v"(vo), "s"(rq), "s"(so) : "memory")
#define LDC(r, so) asm volatile("buffer_load_dwordx4 %0, %1, %2, %3 offen sc1" : "+v"(r) : "v"(vo), "s"(rq), "s"(so) : "memory")
    auto ld = [&](bf16x8 (&a)[4], bf16x8 (&b)[5]) {
#pragma unroll
        for (int j = 0; j < 4; ++j) LDN(b[j], bp + j * KS_H * 1024 + cur);
#pragma unroll
        for (int i = 0; i < 4; ++i) LDN(a[i], ap[i] + cur);
        LDN(b[4], bp + 4 * KS_H * 1024 + cur);
        adv();
    };
    auto mmx = [&](auto w0, auto w4, bf16x8 (&a)[4], bf16x8 (&b)[5], auto fa, auto fb) {
        constexpr int W0 = decltype(w0)::value, W4 = decltype(w4)::value;
#pragma unroll
        for (int j = 0; j < 5; ++j) {
#pragma unroll
            for (int i = 0; i < 4; ++i) {
                if (j == 0) { if (i == 0) WAIT_VM(W0); if (i == 1) WAIT_VM(W0 - 1); if (i == 2) WAIT_VM(W0 - 2); if (i == 3) WAIT_VM(W0 - 3); }
                if (j == 4 && i == 0 && W4 >= 0) WAIT_VM(W4 >= 0 ? W4 : 0);
                MFA(acc[i][j], b[j], a[i]);
                if (j == 4) fa(a[i], i);
            }
            fb(b[j], j);
        }
    };
#define IC(n) std::integral_constant<int, n>{}
    auto nof = [&](bf16x8&, int) {};
    auto fha = [&](bf16x8& r, int i) { LDR(r, ap[i] + cur); };
    auto fhb = [&](bf16x8& r, int j) { LDR(r, bp + j * KS_H * 1024 + cur); };
    ST2(0); ST2ID();
    if (GPRIO) __builtin_amdgcn_s_setprio(GPRIO);
#pragma unroll 1
    for (int i = 0; i < GDELAY; ++i) __builtin_amdgcn_s_sleep(8);
    float gl;
    asm volatile("global_load_dword %0, %1, %2" : "=v"(gl) : "v"(vo >> 2), "s"(ws + WS_G + mt * 256) : "memory");
    ld(a0, b0);
    ld(a1, b1);
#pragma unroll
    for (int i = 0; i < 4; ++i) asm volatile("s_nop 1" : "+v"(acc[i][0]), "+v"(acc[i][1]), "+v"(acc[i][2]), "+v"(acc[i][3]), "+v"(acc[i][4]));
#pragma unroll 1
    for (int s = 0; s < nsteps - 4; s += 2) {
        mmx(IC(13), IC(13), a0, b0, fha, fhb); adv();
        mmx(IC(13), IC(13), a1, b1, fha, fhb); adv();
    }
    const char* fb_ = ws + WS_FLAG + mt * FLAG_LD + 64 * w;
    const int fv = (vo >> 2) & 60;
    mmx(IC(13), IC(13), a0, b0, fha, fhb); adv();
    unsigned fl;
    asm volatile("global_load_dword %0, %1, %2 sc1" : "=v"(fl) : "v"(fv), "s"(fb_) : "memory");
    mmx(IC(14), IC(14), a1, b1, fha, fhb);
    ST2(1);
    int op[4];
#pragma unroll
    for (int i = 0; i < 4; ++i) op[i] = (int)WS_O + ((mt * 4 + (i ^ w)) * KS_O + 5 * w) * 1024;
    const int wp = (int)WS_WO + ((nt * 5) * KS_O + 5 * w) * 1024;
    int so = 0;
    auto foa = [&](bf16x8& r, int i) { LDC(r, op[i] + so); };
    auto fob = [&](bf16x8& r, int j) { LDR(r, wp + j * KS_O * 1024 + so); };
    mmx(IC(14), IC(14), a0, b0, nof, fob);
    ST2(2);
    asm volatile("s_waitcnt vmcnt(14)\n\tv_cmp_ne_u32 vcc, 1, %0\n\ts_cbranch_vccz L_pok_%=\n"
                 "L_pspin_%=:\n\ts_sleep %3\n\tglobal_load_dword %0, %1, %2 sc1\n\ts_waitcnt vmcnt(0)\n\t"
                 "v_cmp_ne_u32 vcc, 1, %0\n\ts_cbranch_vccnz L_pspin_%=\n"
                 "L_pok_%=:" : "+v"(fl) : "v"(fv), "s"(fb_), "n"(POLLSLP) : "vcc", "memory");
    ST2(3);
#pragma unroll
    for (int i = 0; i < 4; ++i) foa(a0[i], i);
    so = 1024;
    mmx(IC(13), IC(13), a1, b1, foa, fob);
    asm volatile("s_nop 7\n\ts_nop 7" : "+v"(gl) :: "memory");
#pragma unroll
    for (int i = 0; i < 4; ++i) {
        const float sc = rbf(1.f - __builtin_bit_cast(float, __builtin_amdgcn_ds_bpermute((i ^ w) * 64 + fv, __builtin_bit_cast(int, gl))));
        asm volatile("" : "+v"(acc[i][0]), "+v"(acc[i][1]), "+v"(acc[i][2]), "+v"(acc[i][3]), "+v"(acc[i][4]));
#pragma unroll
        for (int j = 0; j < 5; ++j) acc[i][j] = acc[i][j] * sc;
        asm volatile("" : "+v"(acc[i][0]), "+v"(acc[i][1]), "+v"(acc[i][2]), "+v"(acc[i][3]), "+v"(acc[i][4]));
    }
    asm volatile("s_nop 3" ::: "memory");
    so = 2048; mmx(IC(12), IC(-1), a0, b0, foa, fob);
    so = 3072; mmx(IC(13), IC(13), a1, b1, foa, fob);
    so = 4096; mmx(IC(13), IC(13), a0, b0, foa, fob);
    mmx(IC(13), IC(9), a1, b1, nof, nof);
    mmx(IC(4), IC(0), a0, b0, nof, nof);
    asm volatile("s_nop 7\n\ts_nop 7" ::: "memory");
#pragma unroll
    for (int i = 0; i < 4; ++i) asm volatile("" : "+v"(acc[i][0]), "+v"(acc[i][1]), "+v"(acc[i][2]), "+v"(acc[i][3]), "+v"(acc[i][4]));
    ST2(4);
    const int ln = lane_id();
    char* mine = smem + w * 10240 + ln * 16;
    char* peer = smem + (w ^ 2) * 10240 + ln * 16;
#pragma unroll
    for (int i = 0; i < 2; ++i)
#pragma unroll
        for (int j = 0; j < 5; ++j) *(f32x4*)(mine + (i * 5 + j) * 1024) = acc[2 + i][j];
    __syncthreads();
#pragma unroll
    for (int j = 0; j < 5; ++j) acc[1][j] += *(const f32x4*)(peer + (5 + j) * 1024);
#pragma unroll
    for (int j = 0; j < 5; ++j) *(f32x4*)(peer + (5 + j) * 1024) = acc[1][j];
#pragma unroll
    for (int j = 0; j < 5; ++j) acc[0][j] += *(const f32x4*)(peer + j * 1024);
    __syncthreads();
    {
        const char* q = smem + (w ^ 3) * 10240 + ln * 16 + 5 * 1024;
        u16* dst = out + (size_t)(mt * 64 + w * 16 + (ln & 15)) * D_ + nt * 80 + (ln >> 4) * 4;
#pragma unroll
        for (int j = 0; j < 5; ++j) {
            const f32x4 v = acc[0][j] + *(const f32x4*)(q + j * 1024);
            u32x2 pk = {pack_bf16(v[0], v[1]), pack_bf16(v[2], v[3])};
            *(u32x2*)(dst + j * 16) = pk;
        }
    }
    ST2(5);
}

__global__ __launch_bounds__(256, 3) void k2_mix(char* __restrict__ ws, u16* __restrict__ out) {
    __shared__ __attribute__((aligned(16))) char smem[AT_LDS > 40960 ? AT_LDS : 40960];
    if (blockIdx.x < 256) k2_gemm(ws, out, smem, blockIdx.x);
    else k2_attn(ws, smem, blockIdx.x - 256);
}

extern "C" void kernel_launch(void* const* d_in, const int* in_sizes, int n_in,
                              void* d_out, int out_size, void* d_ws, size_t ws_size,
                              hipStream_t stream) {
    const u16* X = (const u16*)d_in[0];
    const u16* Wqkv = (const u16*)d_in[1];
    const u16* Wo = (const u16*)d_in[2];
    const u16* Wg = (const u16*)d_in[4];
    const u16* Wu = (const u16*)d_in[5];
    const u16* Wd = (const u16*)d_in[6];
    const u16* gw = (const u16*)d_in[7];
    char* ws = (char*)d_ws;
    k1_gemm<<<256, K1NW * 64, 0, stream>>>(X, Wqkv, Wg, Wu, Wo, Wd, gw, ws);
    k2_mix<<<768, 256, 0, stream>>>(ws, (u16*)d_out);
}
```

```cpp
#include <hip/hip_runtime.h>
#include <hip/hip_bf16.h>
#include <stdint.h>
#include <type_traits>

typedef unsigned short u16;
typedef short bf16x8 __attribute__((ext_vector_type(8)));
typedef short s16x4 __attribute__((ext_vector_type(4)));
typedef float f32x4 __attribute__((ext_vector_type(4)));
typedef float f32x2 __attribute__((ext_vector_type(2)));
typedef unsigned u32x4 __attribute__((ext_vector_type(4)));
typedef unsigned u32x2 __attribute__((ext_vector_type(2)));
typedef __bf16 bf2_t __attribute__((ext_vector_type(2)));

#define LDS_AS __attribute__((address_space(3)))

constexpr int S_ = 2048, D_ = 640, F_ = 1728, H_ = 32, DH_ = 20, NQKV = 1920;
constexpr int KS_O = D_ / 32;
constexpr int KS_H = F_ / 32;

constexpr size_t WS_Q = 0;
constexpr size_t QKV_BYTES = (size_t)H_ * S_ * DH_ * 2;
constexpr size_t WS_K = WS_Q + QKV_BYTES;
constexpr size_t WS_V = WS_K + QKV_BYTES;
constexpr size_t WS_H = WS_V + QKV_BYTES + 4096;
constexpr size_t WS_O = WS_H + (size_t)S_ * F_ * 2;
constexpr size_t WS_WO = WS_O + (size_t)S_ * D_ * 2;
constexpr size_t WS_WD = WS_WO + (size_t)D_ * D_ * 2;
constexpr size_t WS_G = WS_WD + (size_t)F_ * D_ * 2;
constexpr size_t WS_FLAG = WS_G + (size_t)S_ * 4;
constexpr int FLAG_LD = 4352;

__device__ __forceinline__ unsigned pack_bf16(float a, float b) {
    bf2_t v = {(__bf16)a, (__bf16)b};
    return __builtin_bit_cast(unsigned, v);
}
__device__ __forceinline__ float rbf(float x) { return (float)(__bf16)x; }
__device__ __forceinline__ float bf2f(u16 v) { return __uint_as_float(((unsigned)v) << 16); }

template <int CTRL> __device__ __forceinline__ float dpp_add(float x) {
    return x + __builtin_bit_cast(float, __builtin_amdgcn_update_dpp(0, __builtin_bit_cast(int, x), CTRL, 0xf, 0xf, false));
}
__device__ __forceinline__ float wave_sum(float x) {
    x = dpp_add<0xB1>(x); x = dpp_add<0x4E>(x); x = dpp_add<0x141>(x); x = dpp_add<0x140>(x);
    const int xi = __builtin_bit_cast(int, x);
    return (__builtin_bit_cast(float, __builtin_amdgcn_readlane(xi, 0)) + __builtin_bit_cast(float, __builtin_amdgcn_readlane(xi, 16))) +
           (__builtin_bit_cast(float, __builtin_amdgcn_readlane(xi, 32)) + __builtin_bit_cast(float, __builtin_amdgcn_readlane(xi, 48)));
}
__device__ __forceinline__ s16x4 tr_read(const char* lds_ptr) {
    return __builtin_amdgcn_ds_read_tr16_b64_v4i16((LDS_AS s16x4*)(lds_ptr));
}
__device__ __forceinline__ bf16x8 cat4(s16x4 a, s16x4 b) {
    bf16x8 r;
    r[0] = a[0]; r[1] = a[1]; r[2] = a[2]; r[3] = a[3];
    r[4] = b[0]; r[5] = b[1]; r[6] = b[2]; r[7] = b[3];
    return r;
}
__device__ __forceinline__ void glds16(const void* g, void* l) {
    __builtin_amdgcn_global_load_lds((const unsigned*)g, (LDS_AS unsigned*)l, 16, 0, 0);
}
__device__ __forceinline__ void glds16_asm(const void* gsrc, unsigned lds_dst) {
    unsigned keep;
    asm volatile("s_mov_b32 %0, m0\n\ts_mov_b32 m0, %2\n\ts_nop 0\n\tglobal_load_lds_dwordx4 %1, off\n\ts_mov_b32 m0, %0"
                 : "=&s"(keep) : "v"(gsrc), "s"(lds_dst) : "memory");
}
__device__ __forceinline__ void glds4_asm(const void* gsrc, unsigned lds_dst) {
    unsigned keep;
    asm volatile("s_mov_b32 %0, m0\n\ts_mov_b32 m0, %2\n\ts_nop 0\n\tglobal_load_lds_dword %1, off\n\ts_mov_b32 m0, %0"
                 : "=&s"(keep) : "v"(gsrc), "s"(lds_dst) : "memory");
}
__device__ __forceinline__ unsigned lds_addr(const void* p) { return (unsigned)(size_t)(LDS_AS const char*)p; }
typedef __amdgpu_buffer_rsrc_t rsrc_t;
constexpr int AUX_AGENT = 16;
__device__ __forceinline__ rsrc_t mk_rsrc(const void* p, unsigned bytes) { return __builtin_amdgcn_make_buffer_rsrc((void*)p, 0, (int)bytes, 0x00020000); }
template <int AUX> __device__ __forceinline__ void bst4(rsrc_t r, int voff, u32x4 v) { __builtin_amdgcn_raw_buffer_store_b128(v, r, voff, 0, AUX); }
template <int AUX> __device__ __forceinline__ void bst2(rsrc_t r, int voff, u32x2 v) { __builtin_amdgcn_raw_buffer_store_b64(v, r, voff, 0, AUX); }
#define WAIT_VM(n) asm volatile("s_waitcnt vmcnt(%0)" :: "n"(n) : "memory")
#define CBAR() asm volatile("" ::: "memory")
#define STAMP(kid, i) do {} while (0)
#define STAMPK(kid, i) do { if ((i) == 3) STAMP(kid, i); } while (0)

#define ET(i) do {} while (0)
#define ETW() do {} while (0)
#define ENTARG
#define ENTPASS
#define WPG 64
#define OSC 0
#define ESC
#define HAUX 16
#define QAUX 16
#define WSC1
#define K1SB 0
#define K1P0 0
#define K1UNR 1
constexpr int K1_ABYTES = 256 * 64 * 2;
constexpr int K1_BSLOT = 32 * 192 * 2;
constexpr int K1_BOFF = 3 * K1_ABYTES;
constexpr int K1_AUX = K1_BOFF + 5 * K1_BSLOT;
constexpr int K1_FLG = K1_AUX + 2048;
#define PSLP 0
#define PSCH 0
#define BDEP 2
constexpr int NPRD = (PSCH == 6) ? 4 : 2;
constexpr int K1_T0 = 32 + 16 * NPRD;
constexpr int K1_LDS = K1_FLG + K1_T0 + 32;
#define K1FJ 2
#define K1FS 6
#define PFL 4
#define MF1(c, b, a) c = __builtin_amdgcn_mfma_f32_16x16x32_bf16(b, a, c, 0, 0, 0)
typedef volatile LDS_AS unsigned* lflag_t;
template <int SLP> __device__ __forceinline__ void k1_wait_rel(unsigned sb0, int lane, unsigned n) {
    lflag_t p = (lflag_t)(size_t)(sb0 + K1_FLG + (lane & 7) * 4);
    while (__builtin_amdgcn_ballot_w64(*p < n) != 0ull) { if (SLP) __builtin_amdgcn_s_sleep(SLP); }
    CBAR();
}
__device__ __forceinline__ void glds_x4(unsigned lds, const void* sb, unsigned v0, unsigned v1, unsigned v2, unsigned v3) {
    unsigned keep;
    asm volatile("s_mov_b32 %0, m0\n\ts_mov_b32 m0, %1\n\ts_nop 0\n\t"
                 "global_load_lds_dwordx4 %3, %2\n\t"
                 "global_load_lds_dwordx4 %4, %2 offset:1024\n\t"
                 "global_load_lds_dwordx4 %5, %2 offset:2048\n\t"
                 "global_load_lds_dwordx4 %6, %2 offset:3072\n\t"
                 "s_mov_b32 m0, %0"
                 : "=&s"(keep) : "s"(lds), "s"(sb), "v"(v0), "v"(v1), "v"(v2), "v"(v3) : "memory");
}
__device__ __forceinline__ void glds_x2(unsigned lds, const void* sb, unsigned v0, unsigned v1) {
    unsigned keep;
    asm volatile("s_mov_b32 %0, m0\n\ts_mov_b32 m0, %1\n\ts_nop 0\n\t"
                 "global_load_lds_dwordx4 %3, %2\n\t"
                 "global_load_lds_dwordx4 %4, %2 offset:1024\n\t"
                 "s_mov_b32 m0, %0"
                 : "=&s"(keep) : "s"(lds), "s"(sb), "v"(v0), "v"(v1) : "memory");
}
__device__ __forceinline__ void glds_x1(unsigned lds, const void* sb, unsigned v0) {
    unsigned keep;
    asm volatile("s_mov_b32 %0, m0\n\ts_mov_b32 m0, %1\n\ts_nop 0\n\t"
                 "global_load_lds_dwordx4 %3, %2\n\t"
                 "s_mov_b32 m0, %0"
                 : "=&s"(keep) : "s"(lds), "s"(sb), "v"(v0) : "memory");
}
__device__ __forceinline__ int lane_id() {
    int l;
    asm volatile("v_mbcnt_lo_u32_b32 %0, -1, 0\n\tv_mbcnt_hi_u32_b32 %0, -1, %0" : "=v"(l));
    return l;
}
__device__ __forceinline__ s16x4 tr_read_l(unsigned a) { return __builtin_amdgcn_ds_read_tr16_b64_v4i16((LDS_AS s16x4*)(size_t)a); }

template <int NJ>
__device__ __forceinline__ void k1_epilogue(f32x4 (&acc)[4][NJ], char* __restrict__ ws, char* smem, int m0, int c0, int nt, int wid) {
    const int lane = lane_id();
    const int t = wid * 64 + lane;
    const int wr = wid >> 1, wc = wid & 1;
    const int fr = lane & 15, g = lane >> 4;
    const rsrc_t rw = mk_rsrc(ws, (unsigned)WS_FLAG);
    if (NJ == 5) {
        const unsigned sb0 = lds_addr(smem);
        CBAR(); *(lflag_t)(size_t)(sb0 + K1_FLG + wid * 4) = 21u; CBAR();
        k1_wait_rel<0>(sb0, lane, 21u);
        const float qs = (nt < 4) ? 0.22360679774997896f * 1.4426950408889634f : 1.f;
#pragma unroll
        for (int j = 0; j < NJ; ++j) {
            const int n = wc * 80 + j * 16 + g * 4;
            const int hh = n / DH_, d = n % DH_;
            char* dst = smem + hh * 10240 + (wr * 64 + fr) * 40 + d * 2;
#pragma unroll
            for (int i = 0; i < 4; ++i) {
                u32x2 a = {pack_bf16(acc[i][j][0] * qs, acc[i][j][1] * qs), pack_bf16(acc[i][j][2] * qs, acc[i][j][3] * qs)};
                *(u32x2*)(dst + i * 640) = a;
            }
        }
        CBAR(); *(lflag_t)(size_t)(sb0 + K1_FLG + wid * 4) = 22u; CBAR();
        k1_wait_rel<0>(sb0, lane, 22u);
        const int which = nt >> 2;
        const int h0 = (nt & 3) * 8;
        char* dbase = ws + WS_Q + (size_t)which * QKV_BYTES + ((size_t)h0 * S_ + m0) * 40;
        const int dofs = (int)(WS_Q + (size_t)which * QKV_BYTES + ((size_t)h0 * S_ + m0) * 40);
#pragma unroll
        for (int it = 0; it < 10; ++it) {
            const int e = it * 512 + t;
            const int hh = e / 640, r = e % 640;
            const u32x4 v = *(const u32x4*)(smem + e * 16);
            bst4<QAUX>(rw, dofs + hh * (S_ * 40) + r * 16, v);
        }
    } else {
        u16* hF = (u16*)(ws + WS_H);
#pragma unroll
        for (int j = 0; j < 3; ++j) {
            const int f = c0 + wc * 48 + j * 16 + g * 4;
            const int ks = f >> 5;
            const int lq = (f & 31) >> 3;
            const int hf = f & 4;
#pragma unroll
            for (int i = 0; i < 4; ++i) {
                const int m = m0 + wr * 64 + i * 16 + fr;
                unsigned pk[2];
#pragma unroll
                for (int r = 0; r < 2; ++r) {
                    const f32x2 gt = {acc[i][j][2 * r], acc[i][j][2 * r + 1]};
                    const f32x2 up = {acc[i][(j + 3) % NJ][2 * r], acc[i][(j + 3) % NJ][2 * r + 1]};
                    f32x2 e = gt * -1.4426950408889634f;
                    e = (f32x2){__builtin_amdgcn_exp2f(e[0]), __builtin_amdgcn_exp2f(e[1])};
                    e = e + 1.f;
                    const f32x2 rc = {__builtin_amdgcn_rcpf(e[0]), __builtin_amdgcn_rcpf(e[1])};
                    const f32x2 hv = (gt * up) * rc;
                    pk[r] = pack_bf16(hv[0], hv[1]);
                }
                u32x2 a = {pk[0], pk[1]};
                u16* dst = hF + ((size_t)((m >> 4) * KS_H + ks) * 64 + (m & 15) + 16 * lq) * 8 + hf;
                bst2<HAUX>(rw, (int)WS_H + ((((m >> 4) * KS_H + ks) * 64 + (m & 15) + 16 * lq) * 8 + hf) * 2, a);
            }
        }
    }
}

template <int NJ>
__device__ __forceinline__ void k1_tile(const u16* __restrict__ X, const u16* __restrict__ W0, const u16* __restrict__ W1,
                                        char* __restrict__ ws, char* smem, int mt, int nt, int xs, int xcnt, int uw ENTARG) {
    constexpr int BN = 32 * NJ;
    constexpr int ROWB = (NJ == 5) ? 320 : 192;
    constexpr int UPR = ROWB / 16;
    constexpr int NBH = UPR / 2;
    constexpr int NB = 2 * NJ;
    constexpr int LDB = (NJ == 5) ? NQKV : F_;
    const int m0 = mt * 256;
    const int c0 = (NJ == 5) ? nt * 160 : nt * 96;
    const unsigned sb0 = lds_addr(smem);
    auto boffs = [&](int i, int lane) {
        const int p = i * 64 + lane;
        const int k = p / UPR, u = p % UPR;
        return (unsigned)((k * LDB + (((u >> 1) ^ ((k >> 3) & 1)) << 4) + (u & 1) * 8) * 2);
    };
    auto aoffs_f = [&](int lane) { return (unsigned)((lane >> 3) * (D_ * 2) + (((lane & 7) ^ (lane >> 3)) << 4)); };
    const char* xa = (const char*)(X + (size_t)m0 * D_);
    const char* wb0 = (const char*)(W0 + c0);
    const char* wb1 = (const char*)(W1 + c0);

#define KRA 0
#define KRB 0
    const int rot = (xs * KRA + ((mt & 3) * KRB >> 1)) % 10;
    if (uw >= 8) {
        const int dw = uw & 1;
        const int lane = lane_id();
        const unsigned aoffs = aoffs_f(lane);
        if (uw == 8 && lane < (K1_T0 + 32) / 4) *(lflag_t)(size_t)(sb0 + K1_FLG + lane * 4) = 0u;
        constexpr int NBP = NJ;
        unsigned vb[NBP], va[4];
#pragma unroll
        for (int i = 0; i < NBP; ++i) vb[i] = boffs((NJ == 5) ? 5 * dw + i : i, lane) - (i & 3) * 1024;
#pragma unroll
        for (int r = 0; r < 4; ++r) va[r] = aoffs + r * (8 * D_ * 2 - 1024);
        const char* xs0 = xa + dw * (128 * D_ * 2);
        const char* ws0 = (NJ == 6 && dw) ? wb1 : wb0;
        auto loadA = [&](int kt, int q0) {
            const unsigned base = sb0 + (kt % 3) * K1_ABYTES + dw * 16384;
            const int kr = kt + rot;
            const char* src = xs0 + (kr >= 10 ? kr - 10 : kr) * 128;
#pragma unroll
            for (int q = q0; q < q0 + 2; ++q) glds_x4(base + q * 4096, src + q * (32 * D_ * 2), va[0], va[1], va[2], va[3]);
        };
        auto loadB = [&](int h) {
            const unsigned base = sb0 + K1_BOFF + (h % 5) * K1_BSLOT + dw * (NBP * 1024);
            const int hr = h + 2 * rot;
            const char* s0 = ws0 + (size_t)(hr >= 20 ? hr - 20 : hr) * (32 * LDB * 2);
            glds_x4(base, s0, vb[0], vb[1], vb[2], vb[3]);
            if (NJ == 5) glds_x1(base + 4096, s0, vb[4]);
            else glds_x2(base + 4096, s0, vb[4], vb[5 % NBP]);
        };
        asm volatile("s_waitcnt lgkmcnt(0)" ::: "memory");
        __builtin_amdgcn_s_barrier();
        CBAR();
        ET(1);
        volatile LDS_AS u16* rdy = (volatile LDS_AS u16*)(size_t)(sb0 + K1_FLG + 32 + (lane & 7) * (2 * NPRD) + (uw - 8) * 2);
        loadB(1);
#pragma unroll 1
        for (int kt = 1; kt < 10; ++kt) {
            if (kt >= 3) k1_wait_rel<PSLP>(sb0, lane, 2 * kt - 4);
            loadB(2 * kt); loadA(kt, 0);
            WAIT_VM(2 * NBP + 8);
            *rdy = (u16)(2 * kt - 1);
            loadA(kt, 2);
            WAIT_VM(NBP + 16);
            *rdy = (u16)(2 * kt);
            if (kt >= 2) k1_wait_rel<PSLP>(sb0, lane, 2 * kt - 3);
            loadB(2 * kt + 1);
        }
        WAIT_VM(NBP);
        *rdy = (u16)19;
        WAIT_VM(0);
        *rdy = (u16)30;
        ET(4); ETW();
        return;
    }

    ET(1);
    const int lane = lane_id();
    {
        const unsigned aoffs = aoffs_f(lane);
        wb0 += (size_t)rot * (64 * LDB * 2); wb1 += (size_t)rot * (64 * LDB * 2);
        glds_x4(sb0 + uw * 4096, xa + uw * (32 * D_ * 2) + rot * 128, aoffs, aoffs + (8 * D_ * 2 - 1024), aoffs + 2 * (8 * D_ * 2 - 1024), aoffs + 3 * (8 * D_ * 2 - 1024));
        if (NJ == 5) {
            if (uw < 5) glds_x2(sb0 + K1_BOFF + uw * 2048, wb0, boffs(2 * uw, lane), boffs(2 * uw + 1, lane) - 1024);
        } else {
            if (uw < 6) {
                const unsigned v = boffs(uw, lane);
                glds_x1(sb0 + K1_BOFF + uw * 1024, wb0, v);
                glds_x1(sb0 + K1_BOFF + 6144 + uw * 1024, wb1, v);
            }
        }
    }
    ET(2);
    __builtin_amdgcn_s_barrier();
    CBAR();
    const int wr = uw >> 1, wc = uw & 1;
    const int fr = lane & 15, g = lane >> 4;
    f32x4 acc[4][NJ];
#pragma unroll
    for (int i = 0; i < 4; ++i)
#pragma unroll
        for (int j = 0; j < NJ; ++j) acc[i][j] = (f32x4){0.f, 0.f, 0.f, 0.f};

    unsigned aa[2][2], be, bo;
    {
        const int row = wr * 64 + fr;
        aa[0][0] = sb0 + row * 128 + ((g ^ (row & 7)) << 4);
        aa[1][0] = sb0 + row * 128 + (((4 + g) ^ (row & 7)) << 4);
        aa[0][1] = aa[0][0] + 2 * K1_ABYTES;
        aa[1][1] = aa[1][0] + 2 * K1_ABYTES;
        const int q4 = (lane & 15) >> 2, p4 = lane & 3;
        const unsigned bb = sb0 + K1_BOFF + (8 * g + q4) * ROWB + p4 * 8 + wc * (NJ == 5 ? 5 : 3) * 32;
        const int sx = ((g & 1) << 5) * (wc ? -1 : 1);
        be = bb + sx; bo = bb - sx;
    }
    unsigned frel = sb0 + K1_FLG + uw * 4;
    unsigned frdy = sb0 + K1_FLG + 32 + uw * (2 * NPRD) + (lane & (NPRD - 1)) * 2;
    asm volatile("" : "+v"(aa[0][0]), "+v"(aa[1][0]), "+v"(aa[0][1]), "+v"(aa[1][1]), "+v"(be), "+v"(bo), "+v"(frel), "+v"(frdy));
    WAIT_VM(0);
    ET(3);
    *(lflag_t)(size_t)(frel + K1_T0) = 1u;
    {
        lflag_t p = (lflag_t)(size_t)(sb0 + K1_FLG + K1_T0 + (lane & 7) * 4);
        while (__builtin_amdgcn_ballot_w64(*p == 0u) != 0ull) {}
    }
    CBAR();
    ET(4);
    bf16x8 af[2][4], bq[NJ];
    auto rdA = [&](int h, int i) {
        const int st = (h >> 1) % 3;
        return *(const LDS_AS bf16x8*)(size_t)(aa[h & 1][st == 2] + (st == 1 ? K1_ABYTES : 0) + i * 2048);
    };
    auto rdB = [&](int h, int j) {
        const int jj = (NJ == 5) ? j : j % 3, sub = (NJ == 5) ? 0 : (j / 3) * 6144;
        const unsigned b = ((jj & 1) ? bo : be) + (h % 5) * K1_BSLOT + jj * 32 + sub;
        s16x4 lo = tr_read_l(b);
        s16x4 hi = tr_read_l(b + 4 * ROWB);
        return cat4(lo, hi);
    };
    auto rd_rdy = [&]() { return (unsigned)*(volatile LDS_AS u16*)(size_t)frdy; };
#define WAIT_RDY(fl, need) asm volatile("v_cmp_gt_u32 vcc, %2, %0\n\ts_cbranch_vccz L_ok_%=\n" \
        "L_spin_%=:\n\tds_read_u16 %0, %1\n\ts_waitcnt lgkmcnt(0)\n\tv_cmp_gt_u32 vcc, %2, %0\n\ts_cbranch_vccnz L_spin_%=\n" \
        "L_ok_%=:" : "+v"(fl) : "v"(frdy), "n"(need) : "vcc", "memory")
    auto release = [&](unsigned n) { CBAR(); *(lflag_t)(size_t)frel = n; CBAR(); };
#pragma unroll
    for (int i = 0; i < 4; ++i) af[0][i] = rdA(0, i);
    __builtin_amdgcn_sched_barrier(0);
#pragma unroll
    for (int j = 0; j < NJ; ++j) { bq[j] = rdB(0, j); __builtin_amdgcn_sched_barrier(0); }
    release(1u);
    unsigned fl = rd_rdy();
    __builtin_amdgcn_sched_barrier(0);
#pragma unroll
    for (int p = 0; p < 20; ++p) {
#pragma unroll
        for (int sl = 0; sl < 4 * NJ; ++sl) {
            const int j = sl >> 2, i = sl & 3;
            MF1(acc[i][j], bq[j], af[p & 1][i]);
            __builtin_amdgcn_sched_barrier(0);
            if (sl == 0 && p > 0) bq[NJ - 1] = rdB(p, NJ - 1);
            if (sl == 1 && p > 0 && p < 16) release(p + 1);
            if (p < 19) {
                if (sl == 2) WAIT_RDY(fl, p + 2);
                if (sl >= 4 && i == 0) bq[j - 1] = rdB(p + 1, j - 1);
                if (sl == 5) af[(p + 1) & 1][0] = rdA(p + 1, 0);
                if (sl == 6) af[(p + 1) & 1][1] = rdA(p + 1, 1);
                if (sl == 7) af[(p + 1) & 1][2] = rdA(p + 1, 2);
                if (sl == 9) af[(p + 1) & 1][3] = rdA(p + 1, 3);
                if (sl == 4 * NJ - K1FS && p < 18) fl = rd_rdy();
            }
            __builtin_amdgcn_sched_barrier(0);
        }
    }

    k1_epilogue<NJ>(acc, ws, smem, m0, c0, nt, uw);
    ET(5); ETW();
}

#define AUXM 0
#define AUXS 12
#define K1NW 12
template <int NU, int NR, bool EARLY>
__device__ __forceinline__ void k1_aux(const u16* __restrict__ X, const u16* __restrict__ Wo, const u16* __restrict__ Wd,
                                       const u16* __restrict__ gate_w, char* __restrict__ ws, char* wl,
                                       int u0, int us, int ue, int rb, int r0, int rs, int re) {
    const int lane = lane_id();
    float* gout = (float*)(ws + WS_G);
    const int q4 = (lane & 15) >> 2, p4 = lane & 3, g = lane >> 4;
    const rsrc_t rw = mk_rsrc(ws, (unsigned)WS_FLAG);
    auto fetch = [&](int unit, u32x4 (&r)[4]) {
        const int ksg = unit / 10, cc = unit % 10;
#define OPERM 1
        const int e = lane >> 3;
#pragma unroll
        for (int i = 0; i < 4; ++i) {
            const int G = ksg * 4 + i, P = G / 5, gi = G % 5;
            const int ro = (gi < 4) ? (2 * P + (gi >> 1)) * DH_ + (e >> 2) * 8 + 4 * (gi & 1) + (e & 3) : (2 * P + (e >> 2)) * DH_ + 16 + (e & 3);
            const u16* src = (ksg < KS_O) ? Wo + (size_t)ro * D_ : Wd + (size_t)((ksg - KS_O) * 32 + i * 8 + e) * D_;
            r[i] = *(const u32x4*)(src + cc * 64 + (lane & 7) * 8);
        }
    };
    auto emit = [&](int unit, const u32x4 (&r)[4]) {
        const int ksg = unit / 10, cc = unit % 10;
#pragma unroll
        for (int hf = 0; hf < 2; ++hf) {
            CBAR();
            if (((lane >> 2) & 1) == hf) {
#pragma unroll
                for (int i = 0; i < 4; ++i) *(u32x4*)(wl + (i * 8 + (lane >> 3)) * 64 + (lane & 3) * 16) = r[i];
            }
            CBAR();
#pragma unroll
            for (int j = 0; j < 2; ++j) {
                const char* a = wl + (8 * g + q4) * 64 + j * 32 + p4 * 8;
                s16x4 lo = tr_read(a);
                s16x4 hi = tr_read(a + 4 * 64);
                bf16x8 v = cat4(lo, hi);
                const int nt = cc * 4 + hf * 2 + j;
                const int dst = (ksg < KS_O) ? (int)WS_WO + ((nt * KS_O + ksg) * 64 + lane) * 16
                                             : (int)WS_WD + ((nt * KS_H + (ksg - KS_O)) * 64 + lane) * 16;
#define WAUX 0
                bst4<WAUX>(rw, dst, __builtin_bit_cast(u32x4, v));
            }
        }
    };
    u32x4 r[NU ? NU : 1][4];
    u32x4 x0[NR ? NR : 1], x1[NR ? NR : 1], w0, w1;
    auto gfetch = [&]() {
        w0 = *(const u32x4*)(gate_w + lane * 8);
        w1 = *(const u32x4*)(gate_w + 512 + (lane & 15) * 8);
#pragma unroll
        for (int i = 0; i < NR; ++i) {
            int row = r0 + rs * i;
            row = rb + (row < re ? row : r0);
            x0[i] = *(const u32x4*)(X + (size_t)row * D_ + lane * 8);
            x1[i] = *(const u32x4*)(X + (size_t)row * D_ + 512 + (lane & 15) * 8);
        }
    };
    if (NU) {
        const bool extra = u0 + (NU - 1) * us < ue;
#pragma unroll
        for (int u = 0; u < NU - 1; ++u) fetch(u0 + u * us, r[u]);
        if (extra) fetch(u0 + (NU - 1) * us, r[NU - 1]);
        if (EARLY && NR) gfetch();
#pragma unroll
        for (int u = 0; u < NU - 1; ++u) emit(u0 + u * us, r[u]);
        if (extra) emit(u0 + (NU - 1) * us, r[NU - 1]);
    }
    CBAR();
    if (NR == 0) return;
    if (!EARLY || !NU) gfetch();
    if (lane >= 16) w1 = (u32x4){0u, 0u, 0u, 0u};
    float sacc[NR ? NR : 1];
#pragma unroll
    for (int i = 0; i < NR; ++i) {
        float a = 0.f;
#pragma unroll
        for (int e = 0; e < 4; ++e) {
            const unsigned xa = x0[i][e], wa = w0[e], xb = x1[i][e], wb = w1[e];
            a = __builtin_amdgcn_fdot2_f32_bf16(__builtin_bit_cast(bf2_t, xa), __builtin_bit_cast(bf2_t, wa), a, false);
            a = __builtin_amdgcn_fdot2_f32_bf16(__builtin_bit_cast(bf2_t, xb), __builtin_bit_cast(bf2_t, wb), a, false);
        }
        sacc[i] = a;
    }
    {
        float z = 0.f;
#pragma unroll
        for (int i = 0; i < NR; ++i) {
            const float tot = wave_sum(sacc[i]);
            z = (lane == i) ? tot : z;
        }
        const int row = r0 + rs * lane;
        z = rbf(z);
        if (lane < NR && row < re) gout[rb + row] = rbf(1.f / (1.f + __expf(-z)));
    }
}

__global__ __launch_bounds__(K1NW * 64) void k1_gemm(const u16* __restrict__ X, const u16* __restrict__ Wqkv,
                                               const u16* __restrict__ Wg, const u16* __restrict__ Wu,
                                               const u16* __restrict__ Wo, const u16* __restrict__ Wd,
                                               const u16* __restrict__ gate_w, char* __restrict__ ws) {
    __shared__ __attribute__((aligned(16))) char smem[K1_LDS];
    const int bid = blockIdx.x;
    const int xcd = bid & 7, idx = bid >> 3;
    const int ng = xcd >> 1, mg = xcd & 1;
    const int cnt = (ng < 2) ? 8 : 7;
    const int nstart = (ng < 2) ? ng * 8 : 16 + (ng - 2) * 7;
    const int uw = __builtin_amdgcn_readfirstlane(threadIdx.x >> 6);
    if (idx >= 4 * cnt) {
        const int sbk = (xcd - 4) * 4 + (idx - 28);
        if (threadIdx.x < 32)
            *(u32x4*)(ws + WS_FLAG + (size_t)(sbk * 2 + (threadIdx.x >> 4)) * FLAG_LD + (threadIdx.x & 15) * 16) = (u32x4){0u, 0u, 0u, 0u};
        constexpr int NWV = 16 * K1NW;
        const int gw = sbk * K1NW + uw, wq = (((xcd - 4) >> 1) * 4 + (idx - 28)) * K1NW + uw;
        constexpr int NRS = (1024 + NWV / 2 - 1) / (NWV / 2);
        if (AUXM == 0) k1_aux<(740 + NWV - 1) / NWV, NRS, false>(X, Wo, Wd, gate_w, ws, smem + uw * 2048, gw, NWV, 740, mg * 1024, wq, NWV / 2, 1024);
        if (AUXM == 1) k1_aux<1, 0, false>(X, Wo, Wd, gate_w, ws, smem + uw * 2048, 720 + uw * 16 + sbk, 0, 740, 0, 0, 0, 0);
        if (AUXM == 2) k1_aux<1, NRS, true>(X, Wo, Wd, gate_w, ws, smem + uw * 2048, 720 + uw * 16 + sbk, 0, 740, mg * 1024, wq, NWV / 2, 1024);
        if (AUXM == 3) k1_aux<(740 + NWV - 1) / NWV, 0, false>(X, Wo, Wd, gate_w, ws, smem + uw * 2048, gw, NWV, 740, mg * 1024, wq, NWV / 2, 1024);
        if (AUXM == 4) k1_aux<(260 + NWV - 1) / NWV, 0, false>(X, Wo, Wd, gate_w, ws, smem + uw * 2048, 480 + gw, NWV, 740, mg * 1024, wq, NWV / 2, 1024);
        return;
    }
    const int mt = mg * 4 + (idx & 3);
    const int nt = nstart + (idx >> 2);
#define L2PF 0
#define PFD 0
#define PFK 2
#define PFS 0
    if (uw >= 8 + NPRD) {
        if (AUXM == 0 || uw > 10) return;
        __builtin_amdgcn_s_barrier();
        CBAR();
        k1_wait_rel<4>(lds_addr(smem), lane_id(), AUXS);
        const int lin = mt * 30 + nt;
        if (AUXM == 1) k1_aux<3, 9, true>(X, Wo, Wd, gate_w, ws, smem + K1_AUX, lin, 240, 720, mt * 256, nt, 30, 256);
        if (AUXM == 2) k1_aux<3, 0, true>(X, Wo, Wd, gate_w, ws, smem + K1_AUX, lin, 240, 720, mt * 256, nt, 30, 256);
        if (AUXM == 3) k1_aux<0, 9, true>(X, Wo, Wd, gate_w, ws, smem + K1_AUX, lin, 240, 720, mt * 256, nt, 30, 256);
        if (AUXM == 4) k1_aux<2, 9, true>(X, Wo, Wd, gate_w, ws, smem + K1_AUX, lin, 240, 480, mt * 256, nt, 30, 256);
        return;
    }
    if (nt < 12) k1_tile<5>(X, Wqkv, Wqkv, ws, smem, mt, nt, idx >> 2, cnt, uw ENTPASS);
    else k1_tile<6>(X, Wg, Wu, ws, smem, mt, nt - 12, idx >> 2, cnt, uw ENTPASS);
}

typedef unsigned long long u64;
#define GDELAY 8
#define PF2 0
#define ACQ 0
#define ROT 0
#define ROLL 0
#define GPRIO 0
#define POLLSLP 4
#define APRIO 2
#define ST2(i) do {} while (0)
#define ST2ID() do {} while (0)
__device__ __forceinline__ void st_agent(void* p, u32x2 v) {
    __hip_atomic_store((u64*)p, __builtin_bit_cast(u64, v), __ATOMIC_RELAXED, __HIP_MEMORY_SCOPE_AGENT);
}
template <int AUX> __device__ __forceinline__ bf16x8 bld(rsrc_t r, int voff, int soff) {
    return __builtin_bit_cast(bf16x8, __builtin_amdgcn_raw_buffer_load_b128(r, voff, soff, AUX));
}

__device__ __forceinline__ float fmx(float a, float b) { return __builtin_elementwise_maximum(a, b); }

typedef float f32x16 __attribute__((ext_vector_type(16)));
#define MF32(a, b, c) __builtin_amdgcn_mfma_f32_32x32x16_bf16(a, b, c, 0, 0, 0)
#define MF8(a, b, c) __builtin_amdgcn_mfma_f32_32x32x8bf16_1k(a, b, c, 0, 0, 0)
constexpr int AT_V = 15360, AT_PAD = 30720, AT_ONE = AT_PAD + 64;
constexpr int AT_LDS = AT_ONE + 35 * 320 + 16;
template <bool EDGE>
__device__ __forceinline__ void k2_attn_wave(char* __restrict__ oF, const char* smem, const bf16x8 q1, const s16x4 q2,
                                             const float gg, int h, int qw, int rbase, int lane) {
    const int hi = lane >> 5, ql = lane & 31;
    const float NEG = -__builtin_inff();
    const int ts = 8 - (qw >> 5);
    const char* kb = smem + (rbase + ql) * 40;
    const char* ka = kb + hi * 16;
    const char* vb;
    {
        const int q4 = (lane & 15) >> 2, p4 = lane & 3, db = (lane >> 4) & 1;
        vb = smem + AT_V + (rbase + 4 * hi + q4) * 40 + db * 32 + p4 * 8;
        if (db == 1 && p4 >= 1) vb = smem + AT_ONE + (p4 >> 1) * 8;
    }
    auto rd16 = [&](const char* p) {
        const u32x2 lo = *(const u32x2*)p, up = *(const u32x2*)(p + 8);
        const u32x4 v = {lo[0], lo[1], up[0], up[1]};
        return __builtin_bit_cast(bf16x8, v);
    };
    auto rdK1 = [&](int t) { return rd16(ka + t * 1280); };
    auto rdK2 = [&](int t) { return *(const s16x4*)(kb + t * 1280 + 32); };
    auto rdV = [&](int t, int a) {
        s16x4 lo = tr_read(vb + t * 1280 + a * 640);
        s16x4 up = tr_read(vb + t * 1280 + a * 640 + 320);
        return cat4(lo, up);
    };
    f32x16 zero;
#pragma unroll
    for (int i = 0; i < 16; ++i) zero[i] = 0.f;
#define SB0() __builtin_amdgcn_sched_barrier(0)
    auto TL = [](int u) { return u < 7 ? u + 1 : (u == 7 ? 0 : 8); };
    float mx = NEG, my = NEG;
    f32x16 s[3];
    bf16x8 k1[2], vf[2][2];
    s16x4 k2[2];
    k1[0] = rdK1(TL(0)); k2[0] = rdK2(TL(0)); k1[1] = rdK1(TL(1)); k2[1] = rdK2(TL(1));
    SB0();
#pragma unroll
    for (int u = -1; u < 9; ++u) {
        if (u + 1 <= 8) {
            s[(u + 1) % 3] = MF32(k1[(u + 1) & 1], q1, zero);
            SB0();
            if (u + 3 <= 8) k1[(u + 1) & 1] = rdK1(TL(u + 3));
            else if (u + 3 == 9) k1[(u + 1) & 1] = rdK1(1);
            else k1[(u + 1) & 1] = rdK1(2);
        }
        SB0();
        const int p = u - 1;
        if (p >= 0 && p < 7 && (!EDGE || p + 1 >= ts)) {
            const f32x16 x = s[p % 3];
            mx = fmx(fmx(mx, x[0]), x[1]); my = fmx(fmx(my, x[2]), x[3]);
            mx = fmx(fmx(mx, x[4]), x[5]); my = fmx(fmx(my, x[6]), x[7]);
        }
        SB0();
        if (u >= 0) {
            s[u % 3] = MF8(k2[u & 1], q2, s[u % 3]);
            SB0();
            if (u + 2 <= 8) k2[u & 1] = rdK2(TL(u + 2));
            else if (u + 2 == 9) k2[u & 1] = rdK2(1);
            else k2[u & 1] = rdK2(2);
        }
        SB0();
        if (p >= 0 && p < 7 && (!EDGE || p + 1 >= ts)) {
            const f32x16 x = s[p % 3];
            mx = fmx(fmx(mx, x[8]), x[9]); my = fmx(fmx(my, x[10]), x[11]);
            mx = fmx(fmx(mx, x[12]), x[13]); my = fmx(fmx(my, x[14]), x[15]);
        }
        SB0();
    }
    vf[0][0] = rdV(8, 0); vf[0][1] = rdV(8, 1);
    if (!EDGE) { vf[1][0] = rdV(0, 0); vf[1][1] = rdV(0, 1); }
    const int z = ql - 4 * hi;
    f32x16 mg;
#pragma unroll
    for (int i = 0; i < 16; ++i) {
        const bool c = 8 * (i >> 2) + (i & 3) > z;
        mg[i] = c ? (EDGE ? NEG : s[1][i]) : s[2][i];
    }
#pragma unroll
    for (int i = 0; i < 16; i += 4) { mx = fmx(fmx(mx, mg[i]), mg[i + 1]); my = fmx(fmx(my, mg[i + 2]), mg[i + 3]); }
    mx = fmx(mx, my);
    {
        const unsigned mi = __builtin_bit_cast(unsigned, mx);
        auto r = __builtin_amdgcn_permlane32_swap(mi, mi, false, false);
        mx = fmx(__builtin_bit_cast(float, (unsigned)r[0]), __builtin_bit_cast(float, (unsigned)r[1]));
    }
    const float nm = -mx;
    f32x16 cm;
#pragma unroll
    for (int i = 0; i < 16; ++i) cm[i] = nm;
    SB0();
    f32x16 o = zero;
    s[1] = MF32(k1[1], q1, cm);
    SB0();
    k1[1] = rdK1(3);
    {
        u32x4 pa, pb;
#pragma unroll
        for (int a = 0; a < 2; ++a) {
#pragma unroll
            for (int e = 0; e < 4; ++e) {
                const int i = 8 * a + 2 * e;
                const float x0 = __builtin_amdgcn_exp2f(mg[i] + nm), x1 = __builtin_amdgcn_exp2f(mg[i + 1] + nm);
                const unsigned pk = pack_bf16(x0, x1);
                if (EDGE) pa[e] = pk;
                else {
                    const bool c0 = 8 * (i >> 2) + (i & 3) > z, c1 = 8 * (i >> 2) + (i & 3) + 1 > z;
                    pb[e] = pack_bf16(c0 ? x0 : 0.f, c1 ? x1 : 0.f);
                    pa[e] = pk ^ pb[e];
                }
                if (a == 0 && e == 1) { SB0(); s[1] = MF8(k2[1], q2, s[1]); SB0(); k2[1] = rdK2(3); }
            }
            SB0();
            o = MF32(vf[0][a], __builtin_bit_cast(bf16x8, pa), o);
            SB0();
            if (!EDGE) {
                if (a == 1) { s[0] = MF32(k1[0], q1, cm); SB0(); k1[0] = rdK1(4); }
                o = MF32(vf[1][a], __builtin_bit_cast(bf16x8, pb), o);
                SB0();
            } else if (a == 1) { s[0] = MF32(k1[0], q1, cm); SB0(); k1[0] = rdK1(4); }
            vf[1][a] = rdV(1, a);
            vf[0][a] = rdV(2, a);
            SB0();
        }
    }
    u32x4 pk;
    float xa, xb;
#pragma unroll
    for (int t = 1; t < 8; ++t) {
        const bool on = !EDGE || t >= ts;
        auto EX = [&](int i) { xa = __builtin_amdgcn_exp2f(s[t & 1][i]); xb = __builtin_amdgcn_exp2f(s[t & 1][i + 1]); };
        float ya, yb;
        if (t < 7) { s[(t + 1) & 1] = MF8(k2[(t + 1) & 1], q2, s[(t + 1) & 1]); SB0(); if (t + 3 < 8) k2[(t + 1) & 1] = rdK2(t + 3); }
        SB0();
        if (on) {
            EX(0); SB0();
            ya = xa; yb = xb; EX(2); pk[0] = pack_bf16(ya, yb); SB0();
            ya = xa; yb = xb; EX(4); pk[1] = pack_bf16(ya, yb); SB0();
            ya = xa; yb = xb; EX(6); pk[2] = pack_bf16(ya, yb); SB0();
            ya = xa; yb = xb; EX(8); pk[3] = pack_bf16(ya, yb); SB0();
            ya = xa; yb = xb; EX(10);
            const unsigned n0 = pack_bf16(ya, yb); SB0();
            o = MF32(vf[t & 1][0], __builtin_bit_cast(bf16x8, pk), o);
            SB0();
            if (t + 2 < 8) vf[t & 1][0] = rdV(t + 2, 0);
            u32x4 pn;
            pn[0] = n0;
            ya = xa; yb = xb; EX(12); pn[1] = pack_bf16(ya, yb); SB0();
            ya = xa; yb = xb; EX(14); pn[2] = pack_bf16(ya, yb); SB0();
            if (!EDGE && t + 2 < 8) { s[t & 1] = MF32(k1[t & 1], q1, cm); SB0(); if (t + 4 < 8) k1[t & 1] = rdK1(t + 4); }
            pn[3] = pack_bf16(xa, xb); SB0();
            o = MF32(vf[t & 1][1], __builtin_bit_cast(bf16x8, pn), o);
            SB0();
            if (t + 2 < 8) vf[t & 1][1] = rdV(t + 2, 1);
        } else if (t + 2 < 8) { vf[t & 1][0] = rdV(t + 2, 0); vf[t & 1][1] = rdV(t + 2, 1); }
        SB0();
        if (EDGE && t + 2 < 8) { s[t & 1] = MF32(k1[t & 1], q1, cm); SB0(); if (t + 4 < 8) k1[t & 1] = rdK1(t + 4); }
        SB0();
    }
    float sum;
    {
        const float o8 = o[8];
        const unsigned si = __float_as_uint(o8);
        auto r = __builtin_amdgcn_permlane32_swap(si, si, false, false);
        sum = __builtin_bit_cast(float, (unsigned)r[1]);
    }
    const float inv = gg * __builtin_amdgcn_rcpf(sum);
    const int m = qw + ql;
    {
        const rsrc_t ro = mk_rsrc(oF, (unsigned)(S_ * D_ * 2));
        const int ob = ((m >> 4) * KS_O * 64 + (m & 15)) * 16 + (h >> 1) * 1280;
        const u32x4 a = {pack_bf16(o[0] * inv, o[1] * inv), pack_bf16(o[2] * inv, o[3] * inv), pack_bf16(o[4] * inv, o[5] * inv), pack_bf16(o[6] * inv, o[7] * inv)};
        bst4<AUX_AGENT>(ro, ob + (2 * (h & 1) + hi) * 256, a);
        const u32x2 b = {pack_bf16(o[8] * inv, o[9] * inv), pack_bf16(o[10] * inv, o[11] * inv)};
        if (hi == 0) bst2<AUX_AGENT>(ro, ob + 1024 + (h & 1) * 8, b);
    }
}

__device__ __forceinline__ void k2_attn(char* __restrict__ ws, char* smem, int bid) {
    const int h = (bid & 7) + 8 * ((bid >> 3) & 3);
    const int qb = bid >> 5;
    const int q0 = qb * 128;
    const int t = threadIdx.x, lane = t & 63;
    const int w = __builtin_amdgcn_readfirstlane(t >> 6);
    const char* Qb = ws + WS_Q + (size_t)h * S_ * 40;
    const char* Kb = ws + WS_K + (size_t)h * S_ * 40;
    const char* Vb = ws + WS_V + (size_t)h * S_ * 40;
    char* sK = smem;
    char* sV = smem + AT_V;

    ST2(0); ST2ID();
    if (APRIO) __builtin_amdgcn_s_setprio(APRIO);
#pragma unroll
    for (int i = 0; i < 4; ++i) {
        const int p = i * 256 + t;
        if (i < 3 || w < 3) {
            int off = (q0 - 256) * 40 + p * 16;
            off = off < 0 ? 0 : off;
            glds16(Kb + off, sK + p * 16);
            glds16(Vb + off, sV + p * 16);
        }
    }
    if (t < 4) *(u32x4*)(smem + AT_PAD + t * 16) = (u32x4){0u, 0u, 0u, 0u};
    if (t >= 64 && t < 100) *(u32x4*)(smem + AT_ONE + (t - 64) * 320) = (u32x4){0x3f80u, 0u, 0u, 0u};
    const int hi = lane >> 5;
    const int qw = q0 + 32 * w;
    bf16x8 q1;
    s16x4 q2;
    {
        const char* qp = Qb + (size_t)(qw + (lane & 31)) * 40;
        const u32x2 a = *(const u32x2*)(qp + hi * 16), b = *(const u32x2*)(qp + hi * 16 + 8);
        u32x2 c = *(const u32x2*)(qp + 32);
        if (hi) c = (u32x2){0u, 0u};
        const u32x4 v1 = {a[0], a[1], b[0], b[1]};
        q1 = __builtin_bit_cast(bf16x8, v1); q2 = __builtin_bit_cast(s16x4, c);
    }
    const float gg = ((const float*)(ws + WS_G))[qw + (lane & 31)];
    __syncthreads();
    ST2(1);
    char* oF = ws + WS_O;
    if (qb >= 2) k2_attn_wave<false>(oF, smem, q1, q2, gg, h, qw, 32 * w, lane);
    else k2_attn_wave<true>(oF, smem, q1, q2, gg, h, qw, 32 * w, lane);
    ST2(2);
    asm volatile("s_waitcnt vmcnt(0)" ::: "memory");
    ST2(3);
    if (lane == 0)
        __hip_atomic_store((unsigned*)(ws + WS_FLAG + (qw >> 6) * FLAG_LD) + h * 2 + ((qw >> 5) & 1), 1u, __ATOMIC_RELAXED, __HIP_MEMORY_SCOPE_AGENT);
}

__device__ __forceinline__ void k2_gemm(char* __restrict__ ws, u16* __restrict__ out, char* smem, int bid) {
    const int xcd = bid & 7, idx = bid >> 3;
    const int mt = (xcd >> 1) * 8 + (idx & 7);
    const int nt = (xcd & 1) * 4 + (idx >> 3);
    const int t = threadIdx.x, lane = t & 63;
    const int w = __builtin_amdgcn_readfirstlane(t >> 6);
    const int fr = lane & 15, g = lane >> 4;
    const int nsteps = (w < 3) ? 14 : 12;
    const rsrc_t rs = __builtin_amdgcn_make_buffer_rsrc(ws, 0, (int)WS_FLAG, 0x00020000);
    const int vo = lane * 16;
    int ap[4];
#pragma unroll
#define K2SAME 0
    for (int i = 0; i < 4; ++i) ap[i] = (int)WS_H + ((((K2SAME & 1) ? 0 : (K2SAME & 4) ? (mt & 24) : mt) * 4 + (i ^ w)) * KS_H + 14 * w) * 1024;
    int bp = (int)WS_WD + ((((K2SAME & 2) ? 0 : nt) * 5) * KS_H + 14 * w) * 1024;
#define RTA 0
#define RTB 0
    int cur = ((RTA * (nt & 3) + (RTB * (mt & 7) >> 2)) % nsteps) * 1024;
    auto adv = [&]() { cur += 1024; cur = (cur == nsteps * 1024) ? 0 : cur; };

    f32x4 acc[4][5];
#pragma unroll
    for (int i = 0; i < 4; ++i)
#pragma unroll
        for (int j = 0; j < 5; ++j) acc[i][j] = (f32x4){0.f, 0.f, 0.f, 0.f};
    bf16x8 a0[4], b0[5], a1[4], b1[5];
#define SB() __builtin_amdgcn_sched_barrier(0)
#define MFA(c, b, a) asm volatile("v_mfma_f32_16x16x32_bf16 %0, %1, %2, %0" : "+v"(c) : "v"(b), "v"(a))
    const u32x4 rq = {(unsigned)(size_t)ws, (unsigned)((size_t)ws >> 32) & 0xffffu, (unsigned)WS_FLAG, 0x00020000u};
#define LDN(r, so) asm volatile("buffer_load_dwordx4 %0, %1, %2, %3 offen" : "=v"(r) : "v"(vo), "s"(rq), "s"(so) : "memory")
#define LDR(r, so) asm volatile("buffer_load_dwordx4 %0, %1, %2, %3 offen" : "+v"(r) : "v"(vo), "s"(rq), "s"(so) : "memory")
#define LDC(r, so) asm volatile("buffer_load_dwordx4 %0, %1, %2, %3 offen sc1" : "+v"(r) : "v"(vo), "s"(rq), "s"(so) : "memory")
    auto ld = [&](bf16x8 (&a)[4], bf16x8 (&b)[5]) {
#pragma unroll
        for (int j = 0; j < 4; ++j) LDN(b[j], bp + j * KS_H * 1024 + cur);
#pragma unroll
        for (int i = 0; i < 4; ++i) LDN(a[i], ap[i] + cur);
        LDN(b[4], bp + 4 * KS_H * 1024 + cur);
        adv();
    };
    auto mmx = [&](auto w0, auto w4, bf16x8 (&a)[4], bf16x8 (&b)[5], auto fa, auto fb) {
        constexpr int W0 = decltype(w0)::value, W4 = decltype(w4)::value;
#pragma unroll
        for (int j = 0; j < 5; ++j) {
#pragma unroll
            for (int i = 0; i < 4; ++i) {
                if (j == 0) { if (i == 0) WAIT_VM(W0); if (i == 1) WAIT_VM(W0 - 1); if (i == 2) WAIT_VM(W0 - 2); if (i == 3) WAIT_VM(W0 - 3); }
                if (j == 4 && i == 0 && W4 >= 0) WAIT_VM(W4 >= 0 ? W4 : 0);
                MFA(acc[i][j], b[j], a[i]);
                if (j == 4) fa(a[i], i);
            }
            fb(b[j], j);
        }
    };
#define IC(n) std::integral_constant<int, n>{}
    auto nof = [&](bf16x8&, int) {};
    auto fha = [&](bf16x8& r, int i) { LDR(r, ap[i] + cur); };
    auto fhb = [&](bf16x8& r, int j) { LDR(r, bp + j * KS_H * 1024 + cur); };
    ST2(0); ST2ID();
    if (GPRIO) __builtin_amdgcn_s_setprio(GPRIO);
#pragma unroll 1
    for (int i = 0; i < GDELAY; ++i) __builtin_amdgcn_s_sleep(8);
    float gl;
    asm volatile("global_load_dword %0, %1, %2" : "=v"(gl) : "v"(vo >> 2), "s"(ws + WS_G + mt * 256) : "memory");
    ld(a0, b0);
    ld(a1, b1);
#pragma unroll
    for (int i = 0; i < 4; ++i) asm volatile("s_nop 1" : "+v"(acc[i][0]), "+v"(acc[i][1]), "+v"(acc[i][2]), "+v"(acc[i][3]), "+v"(acc[i][4]));
#pragma unroll 1
    for (int s = 0; s < nsteps - 4; s += 2) {
        mmx(IC(13), IC(13), a0, b0, fha, fhb); adv();
        mmx(IC(13), IC(13), a1, b1, fha, fhb); adv();
    }
    const char* fb_ = ws + WS_FLAG + mt * FLAG_LD + 64 * w;
    const int fv = (vo >> 2) & 60;
    mmx(IC(13), IC(13), a0, b0, fha, fhb); adv();
    unsigned fl;
    asm volatile("global_load_dword %0, %1, %2 sc1" : "=v"(fl) : "v"(fv), "s"(fb_) : "memory");
    mmx(IC(14), IC(14), a1, b1, fha, fhb);
    ST2(1);
    int op[4];
#pragma unroll
    for (int i = 0; i < 4; ++i) op[i] = (int)WS_O + ((mt * 4 + (i ^ w)) * KS_O + 5 * w) * 1024;
    const int wp = (int)WS_WO + ((nt * 5) * KS_O + 5 * w) * 1024;
    int so = 0;
    auto foa = [&](bf16x8& r, int i) { LDC(r, op[i] + so); };
    auto fob = [&](bf16x8& r, int j) { LDR(r, wp + j * KS_O * 1024 + so); };
    mmx(IC(14), IC(14), a0, b0, nof, fob);
    ST2(2);
    asm volatile("s_waitcnt vmcnt(14)\n\tv_cmp_ne_u32 vcc, 1, %0\n\ts_cbranch_vccz L_pok_%=\n"
                 "L_pspin_%=:\n\ts_sleep %3\n\tglobal_load_dword %0, %1, %2 sc1\n\ts_waitcnt vmcnt(0)\n\t"
                 "v_cmp_ne_u32 vcc, 1, %0\n\ts_cbranch_vccnz L_pspin_%=\n"
                 "L_pok_%=:" : "+v"(fl) : "v"(fv), "s"(fb_), "n"(POLLSLP) : "vcc", "memory");
    ST2(3);
#pragma unroll
    for (int i = 0; i < 4; ++i) foa(a0[i], i);
    so = 1024;
    mmx(IC(13), IC(13), a1, b1, foa, fob);
    asm volatile("s_nop 7\n\ts_nop 7" : "+v"(gl) :: "memory");
#pragma unroll
    for (int i = 0; i < 4; ++i) {
        const float sc = rbf(1.f - __builtin_bit_cast(float, __builtin_amdgcn_ds_bpermute((i ^ w) * 64 + fv, __builtin_bit_cast(int, gl))));
        asm volatile("" : "+v"(acc[i][0]), "+v"(acc[i][1]), "+v"(acc[i][2]), "+v"(acc[i][3]), "+v"(acc[i][4]));
#pragma unroll
        for (int j = 0; j < 5; ++j) acc[i][j] = acc[i][j] * sc;
        asm volatile("" : "+v"(acc[i][0]), "+v"(acc[i][1]), "+v"(acc[i][2]), "+v"(acc[i][3]), "+v"(acc[i][4]));
    }
    asm volatile("s_nop 3" ::: "memory");
    so = 2048; mmx(IC(12), IC(-1), a0, b0, foa, fob);
    so = 3072; mmx(IC(13), IC(13), a1, b1, foa, fob);
    so = 4096; mmx(IC(13), IC(13), a0, b0, foa, fob);
    mmx(IC(13), IC(9), a1, b1, nof, nof);
    mmx(IC(4), IC(0), a0, b0, nof, nof);
    asm volatile("s_nop 7\n\ts_nop 7" ::: "memory");
#pragma unroll
    for (int i = 0; i < 4; ++i) asm volatile("" : "+v"(acc[i][0]), "+v"(acc[i][1]), "+v"(acc[i][2]), "+v"(acc[i][3]), "+v"(acc[i][4]));
    ST2(4);
    const int ln = lane_id();
    char* mine = smem + w * 10240 + ln * 16;
    char* peer = smem + (w ^ 2) * 10240 + ln * 16;
#pragma unroll
    for (int i = 0; i < 2; ++i)
#pragma unroll
        for (int j = 0; j < 5; ++j) *(f32x4*)(mine + (i * 5 + j) * 1024) = acc[2 + i][j];
    __syncthreads();
#pragma unroll
    for (int j = 0; j < 5; ++j) acc[1][j] += *(const f32x4*)(peer + (5 + j) * 1024);
#pragma unroll
    for (int j = 0; j < 5; ++j) *(f32x4*)(peer + (5 + j) * 1024) = acc[1][j];
#pragma unroll
    for (int j = 0; j < 5; ++j) acc[0][j] += *(const f32x4*)(peer + j * 1024);
    __syncthreads();
    {
        const char* q = smem + (w ^ 3) * 10240 + ln * 16 + 5 * 1024;
        u16* dst = out + (size_t)(mt * 64 + w * 16 + (ln & 15)) * D_ + nt * 80 + (ln >> 4) * 4;
#pragma unroll
        for (int j = 0; j < 5; ++j) {
            const f32x4 v = acc[0][j] + *(const f32x4*)(q + j * 1024);
            u32x2 pk = {pack_bf16(v[0], v[1]), pack_bf16(v[2], v[3])};
            *(u32x2*)(dst + j * 16) = pk;
        }
    }
    ST2(5);
}

__global__ __launch_bounds__(256, 3) void k2_mix(char* __restrict__ ws, u16* __restrict__ out) {
    __shared__ __attribute__((aligned(16))) char smem[AT_LDS > 40960 ? AT_LDS : 40960];
    if (blockIdx.x < 256) k2_gemm(ws, out, smem, blockIdx.x);
    else k2_attn(ws, smem, blockIdx.x - 256);
}

extern "C" void kernel_launch(void* const* d_in, const int* in_sizes, int n_in,
                              void* d_out, int out_size, void* d_ws, size_t ws_size,
                              hipStream_t stream) {
    const u16* X = (const u16*)d_in[0];
    const u16* Wqkv = (const u16*)d_in[1];
    const u16* Wo = (const u16*)d_in[2];
    const u16* Wg = (const u16*)d_in[4];
    const u16* Wu = (const u16*)d_in[5];
    const u16* Wd = (const u16*)d_in[6];
    const u16* gw = (const u16*)d_in[7];
    char* ws = (char*)d_ws;
    k1_gemm<<<256, K1NW * 64, 0, stream>>>(X, Wqkv, Wg, Wu, Wo, Wd, gw, ws);
    k2_mix<<<768, 256, 0, stream>>>(ws, (u16*)d_out);
}
```

```cpp
#include <hip/hip_runtime.h>
#include <hip/hip_bf16.h>
#include <stdint.h>
#include <type_traits>

typedef unsigned short u16;
typedef short bf16x8 __attribute__((ext_vector_type(8)));
typedef short s16x4 __attribute__((ext_vector_type(4)));
typedef float f32x4 __attribute__((ext_vector_type(4)));
typedef float f32x2 __attribute__((ext_vector_type(2)));
typedef unsigned u32x4 __attribute__((ext_vector_type(4)));
typedef unsigned u32x2 __attribute__((ext_vector_type(2)));
typedef __bf16 bf2_t __attribute__((ext_vector_type(2)));

#define LDS_AS __attribute__((address_space(3)))

constexpr int S_ = 2048, D_ = 640, F_ = 1728, H_ = 32, DH_ = 20, NQKV = 1920;
constexpr int KS_O = D_ / 32;
constexpr int KS_H = F_ / 32;

constexpr size_t WS_Q = 0;
constexpr size_t QKV_BYTES = (size_t)H_ * S_ * DH_ * 2;
constexpr size_t WS_K = WS_Q + QKV_BYTES;
constexpr size_t WS_V = WS_K + QKV_BYTES;
constexpr size_t WS_H = WS_V + QKV_BYTES + 4096;
constexpr size_t WS_O = WS_H + (size_t)S_ * F_ * 2;
constexpr size_t WS_WO = WS_O + (size_t)S_ * D_ * 2;
constexpr size_t WS_WD = WS_WO + (size_t)D_ * D_ * 2;
constexpr size_t WS_G = WS_WD + (size_t)F_ * D_ * 2;
constexpr size_t WS_FLAG = WS_G + (size_t)S_ * 4;
constexpr int FLAG_LD = 4352;

__device__ __forceinline__ unsigned pack_bf16(float a, float b) {
    bf2_t v = {(__bf16)a, (__bf16)b};
    return __builtin_bit_cast(unsigned, v);
}
__device__ __forceinline__ float rbf(float x) { return (float)(__bf16)x; }
__device__ __forceinline__ float bf2f(u16 v) { return __uint_as_float(((unsigned)v) << 16); }

template <int CTRL> __device__ __forceinline__ float dpp_add(float x) {
    return x + __builtin_bit_cast(float, __builtin_amdgcn_update_dpp(0, __builtin_bit_cast(int, x), CTRL, 0xf, 0xf, false));
}
__device__ __forceinline__ float wave_sum(float x) {
    x = dpp_add<0xB1>(x); x = dpp_add<0x4E>(x); x = dpp_add<0x141>(x); x = dpp_add<0x140>(x);
    const int xi = __builtin_bit_cast(int, x);
    return (__builtin_bit_cast(float, __builtin_amdgcn_readlane(xi, 0)) + __builtin_bit_cast(float, __builtin_amdgcn_readlane(xi, 16))) +
           (__builtin_bit_cast(float, __builtin_amdgcn_readlane(xi, 32)) + __builtin_bit_cast(float, __builtin_amdgcn_readlane(xi, 48)));
}
__device__ __forceinline__ s16x4 tr_read(const char* lds_ptr) {
    return __builtin_amdgcn_ds_read_tr16_b64_v4i16((LDS_AS s16x4*)(lds_ptr));
}
__device__ __forceinline__ bf16x8 cat4(s16x4 a, s16x4 b) {
    bf16x8 r;
    r[0] = a[0]; r[1] = a[1]; r[2] = a[2]; r[3] = a[3];
    r[4] = b[0]; r[5] = b[1]; r[6] = b[2]; r[7] = b[3];
    return r;
}
__device__ __forceinline__ void glds16(const void* g, void* l) {
    __builtin_amdgcn_global_load_lds((const unsigned*)g, (LDS_AS unsigned*)l, 16, 0, 0);
}
__device__ __forceinline__ void glds16_asm(const void* gsrc, unsigned lds_dst) {
    unsigned keep;
    asm volatile("s_mov_b32 %0, m0\n\ts_mov_b32 m0, %2\n\ts_nop 0\n\tglobal_load_lds_dwordx4 %1, off\n\ts_mov_b32 m0, %0"
                 : "=&s"(keep) : "v"(gsrc), "s"(lds_dst) : "memory");
}
__device__ __forceinline__ void glds4_asm(const void* gsrc, unsigned lds_dst) {
    unsigned keep;
    asm volatile("s_mov_b32 %0, m0\n\ts_mov_b32 m0, %2\n\ts_nop 0\n\tglobal_load_lds_dword %1, off\n\ts_mov_b32 m0, %0"
                 : "=&s"(keep) : "v"(gsrc), "s"(lds_dst) : "memory");
}
__device__ __forceinline__ unsigned lds_addr(const void* p) { return (unsigned)(size_t)(LDS_AS const char*)p; }
typedef __amdgpu_buffer_rsrc_t rsrc_t;
constexpr int AUX_AGENT = 16;
__device__ __forceinline__ rsrc_t mk_rsrc(const void* p, unsigned bytes) { return __builtin_amdgcn_make_buffer_rsrc((void*)p, 0, (int)bytes, 0x00020000); }
template <int AUX> __device__ __forceinline__ void bst4(rsrc_t r, int voff, u32x4 v) { __builtin_amdgcn_raw_buffer_store_b128(v, r, voff, 0, AUX); }
template <int AUX> __device__ __forceinline__ void bst2(rsrc_t r, int voff, u32x2 v) { __builtin_amdgcn_raw_buffer_store_b64(v, r, voff, 0, AUX); }
#define WAIT_VM(n) asm volatile("s_waitcnt vmcnt(%0)" :: "n"(n) : "memory")
#define CBAR() asm volatile("" ::: "memory")
#define STAMP(kid, i) do {} while (0)
#define STAMPK(kid, i) do { if ((i) == 3) STAMP(kid, i); } while (0)

#define ET(i) do {} while (0)
#define ETW() do {} while (0)
#define ENTARG
#define ENTPASS
#define WPG 64
#define OSC 0
#define ESC
#define HAUX 16
#define QAUX 16
#define WSC1
#define K1SB 0
#define K1P0 0
#define K1UNR 1
constexpr int K1_ABYTES = 256 * 64 * 2;
constexpr int K1_BSLOT = 32 * 192 * 2;
constexpr int K1_BOFF = 3 * K1_ABYTES;
constexpr int K1_AUX = K1_BOFF + 5 * K1_BSLOT;
constexpr int K1_FLG = K1_AUX + 2048;
#define PSLP 0
#define PSCH 0
#define BDEP 2
#define EARLYLD 1
constexpr int NPRD = (PSCH == 6) ? 4 : 2;
constexpr int K1_T0 = 32 + 16 * NPRD;
constexpr int K1_LDS = K1_FLG + K1_T0 + 32;
#define K1FJ 2
#define K1FS 6
#define PFL 4
#define MF1(c, b, a) c = __builtin_amdgcn_mfma_f32_16x16x32_bf16(b, a, c, 0, 0, 0)
typedef volatile LDS_AS unsigned* lflag_t;
template <int SLP> __device__ __forceinline__ void k1_wait_rel(unsigned sb0, int lane, unsigned n) {
    lflag_t p = (lflag_t)(size_t)(sb0 + K1_FLG + (lane & 7) * 4);
    while (__builtin_amdgcn_ballot_w64(*p < n) != 0ull) { if (SLP) __builtin_amdgcn_s_sleep(SLP); }
    CBAR();
}
__device__ __forceinline__ void glds_x4(unsigned lds, const void* sb, unsigned v0, unsigned v1, unsigned v2, unsigned v3) {
    unsigned keep;
    asm volatile("s_mov_b32 %0, m0\n\ts_mov_b32 m0, %1\n\ts_nop 0\n\t"
                 "global_load_lds_dwordx4 %3, %2\n\t"
                 "global_load_lds_dwordx4 %4, %2 offset:1024\n\t"
                 "global_load_lds_dwordx4 %5, %2 offset:2048\n\t"
                 "global_load_lds_dwordx4 %6, %2 offset:3072\n\t"
                 "s_mov_b32 m0, %0"
                 : "=&s"(keep) : "s"(lds), "s"(sb), "v"(v0), "v"(v1), "v"(v2), "v"(v3) : "memory");
}
__device__ __forceinline__ void glds_x2(unsigned lds, const void* sb, unsigned v0, unsigned v1) {
    unsigned keep;
    asm volatile("s_mov_b32 %0, m0\n\ts_mov_b32 m0, %1\n\ts_nop 0\n\t"
                 "global_load_lds_dwordx4 %3, %2\n\t"
                 "global_load_lds_dwordx4 %4, %2 offset:1024\n\t"
                 "s_mov_b32 m0, %0"
                 : "=&s"(keep) : "s"(lds), "s"(sb), "v"(v0), "v"(v1) : "memory");
}
__device__ __forceinline__ void glds_x1(unsigned lds, const void* sb, unsigned v0) {
    unsigned keep;
    asm volatile("s_mov_b32 %0, m0\n\ts_mov_b32 m0, %1\n\ts_nop 0\n\t"
                 "global_load_lds_dwordx4 %3, %2\n\t"
                 "s_mov_b32 m0, %0"
                 : "=&s"(keep) : "s"(lds), "s"(sb), "v"(v0) : "memory");
}
__device__ __forceinline__ int lane_id() {
    int l;
    asm volatile("v_mbcnt_lo_u32_b32 %0, -1, 0\n\tv_mbcnt_hi_u32_b32 %0, -1, %0" : "=v"(l));
    return l;
}
__device__ __forceinline__ s16x4 tr_read_l(unsigned a) { return __builtin_amdgcn_ds_read_tr16_b64_v4i16((LDS_AS s16x4*)(size_t)a); }

template <int NJ>
__device__ __forceinline__ void k1_epilogue(f32x4 (&acc)[4][NJ], char* __restrict__ ws, char* smem, int m0, int c0, int nt, int wid ENTARG) {
    const int lane = lane_id();
    const int t = wid * 64 + lane;
    const int wr = wid >> 1, wc = wid & 1;
    const int fr = lane & 15, g = lane >> 4;
    const rsrc_t rw = mk_rsrc(ws, (unsigned)WS_FLAG);
#define EPI5 1
    if (NJ == 5 && EPI5) {
        const unsigned sb0 = lds_addr(smem);
        k1_wait_rel<0>(sb0, lane, 18u);
        char* img = smem + K1_ABYTES + wid * 10240;
        if (nt < 4) {
            constexpr float qs = 0.22360679774997896f * 1.4426950408889634f;
#pragma unroll
            for (int j = 0; j < NJ; ++j)
#pragma unroll
                for (int i = 0; i < 4; ++i) {
                    f32x2 lo = {acc[i][j][0], acc[i][j][1]}, hi = {acc[i][j][2], acc[i][j][3]};
                    lo = lo * qs; hi = hi * qs;
                    acc[i][j] = (f32x4){lo[0], lo[1], hi[0], hi[1]};
                }
        }
#pragma unroll
        for (int j = 0; j < NJ; ++j) {
            const unsigned u = 4 * j + g;
            char* dst = img + fr * 40 + u * 8 + ((u * 13u) >> 6) * 2520u;
#pragma unroll
            for (int i = 0; i < 4; ++i) {
                u32x2 a = {pack_bf16(acc[i][j][0], acc[i][j][1]), pack_bf16(acc[i][j][2], acc[i][j][3])};
                *(u32x2*)(dst + i * 640) = a;
            }
        }
        CBAR();
        u32x4 v[10];
#pragma unroll
        for (int it = 0; it < 10; ++it) v[it] = *(const u32x4*)(img + it * 1024 + lane * 16);
        const int which = nt >> 2;
        const int dofs = (int)(WS_Q + (size_t)which * QKV_BYTES) + (((nt & 3) * 8 + wc * 4) * S_ + m0 + wr * 64) * 40;
        constexpr int HS = S_ * 40 - 2560;
        const int v0 = lane * 16, v1 = v0 + (lane >= 32 ? HS : 0);
#pragma unroll
        for (int it = 0; it < 10; ++it) {
            const int hh = (it / 5) * 2 + ((it % 5) >= 3 ? 1 : 0);
            __builtin_amdgcn_raw_buffer_store_b128(v[it], rw, (it % 5) == 2 ? v1 : v0, dofs + it * 1024 + hh * HS, QAUX);
        }
    } else if (NJ == 5) {
        const unsigned sb0 = lds_addr(smem);
        CBAR(); *(lflag_t)(size_t)(sb0 + K1_FLG + wid * 4) = 21u; CBAR();
        k1_wait_rel<0>(sb0, lane, 21u);
        const float qs = (nt < 4) ? 0.22360679774997896f * 1.4426950408889634f : 1.f;
#pragma unroll
        for (int j = 0; j < NJ; ++j) {
            const int n = wc * 80 + j * 16 + g * 4;
            const int hh = n / DH_, d = n % DH_;
            char* dst = smem + hh * 10240 + (wr * 64 + fr) * 40 + d * 2;
#pragma unroll
            for (int i = 0; i < 4; ++i) {
                u32x2 a = {pack_bf16(acc[i][j][0] * qs, acc[i][j][1] * qs), pack_bf16(acc[i][j][2] * qs, acc[i][j][3] * qs)};
                *(u32x2*)(dst + i * 640) = a;
            }
        }
        CBAR(); *(lflag_t)(size_t)(sb0 + K1_FLG + wid * 4) = 22u; CBAR();
        k1_wait_rel<0>(sb0, lane, 22u);
        const int which = nt >> 2;
        const int h0 = (nt & 3) * 8;
        char* dbase = ws + WS_Q + (size_t)which * QKV_BYTES + ((size_t)h0 * S_ + m0) * 40;
        const int dofs = (int)(WS_Q + (size_t)which * QKV_BYTES + ((size_t)h0 * S_ + m0) * 40);
#pragma unroll
        for (int it = 0; it < 10; ++it) {
            const int e = it * 512 + t;
            const int hh = e / 640, r = e % 640;
            const u32x4 v = *(const u32x4*)(smem + e * 16);
            bst4<QAUX>(rw, dofs + hh * (S_ * 40) + r * 16, v);
        }
    } else {
        u16* hF = (u16*)(ws + WS_H);
#pragma unroll
        for (int j = 0; j < 3; ++j) {
            const int f = c0 + wc * 48 + j * 16 + g * 4;
            const int ks = f >> 5;
            const int lq = (f & 31) >> 3;
            const int hf = f & 4;
#pragma unroll
            for (int i = 0; i < 4; ++i) {
                const int m = m0 + wr * 64 + i * 16 + fr;
                unsigned pk[2];
#pragma unroll
                for (int r = 0; r < 2; ++r) {
                    const f32x2 gt = {acc[i][j][2 * r], acc[i][j][2 * r + 1]};
                    const f32x2 up = {acc[i][(j + 3) % NJ][2 * r], acc[i][(j + 3) % NJ][2 * r + 1]};
                    f32x2 e = gt * -1.4426950408889634f;
                    e = (f32x2){__builtin_amdgcn_exp2f(e[0]), __builtin_amdgcn_exp2f(e[1])};
                    e = e + 1.f;
                    const f32x2 rc = {__builtin_amdgcn_rcpf(e[0]), __builtin_amdgcn_rcpf(e[1])};
                    const f32x2 hv = (gt * up) * rc;
                    pk[r] = pack_bf16(hv[0], hv[1]);
                }
                u32x2 a = {pk[0], pk[1]};
                u16* dst = hF + ((size_t)((m >> 4) * KS_H + ks) * 64 + (m & 15) + 16 * lq) * 8 + hf;
                bst2<HAUX>(rw, (int)WS_H + ((((m >> 4) * KS_H + ks) * 64 + (m & 15) + 16 * lq) * 8 + hf) * 2, a);
            }
        }
    }
}

template <int NJ>
__device__ __forceinline__ void k1_tile(const u16* __restrict__ X, const u16* __restrict__ W0, const u16* __restrict__ W1,
                                        char* __restrict__ ws, char* smem, int mt, int nt, int xs, int xcnt, int uw ENTARG) {
    constexpr int BN = 32 * NJ;
    constexpr int ROWB = (NJ == 5) ? 320 : 192;
    constexpr int UPR = ROWB / 16;
    constexpr int NBH = UPR / 2;
    constexpr int NB = 2 * NJ;
    constexpr int LDB = (NJ == 5) ? NQKV : F_;
    const int m0 = mt * 256;
    const int c0 = (NJ == 5) ? nt * 160 : nt * 96;
    const unsigned sb0 = lds_addr(smem);
    auto boffs = [&](int i, int lane) {
        const int p = i * 64 + lane;
        const int k = p / UPR, u = p % UPR;
        return (unsigned)((k * LDB + (((u >> 1) ^ ((k >> 3) & 1)) << 4) + (u & 1) * 8) * 2);
    };
    auto aoffs_f = [&](int lane) { return (unsigned)((lane >> 3) * (D_ * 2) + (((lane & 7) ^ (lane >> 3)) << 4)); };
    const char* xa = (const char*)(X + (size_t)m0 * D_);
    const char* wb0 = (const char*)(W0 + c0);
    const char* wb1 = (const char*)(W1 + c0);

#define KRA 0
#define KRB 0
    const int rot = (xs * KRA + ((mt & 3) * KRB >> 1)) % 10;
    if (uw >= 8) {
        const int dw = uw & 1;
        const int lane = lane_id();
        const unsigned aoffs = aoffs_f(lane);
        if (uw == 8 && lane < (K1_T0 + 32) / 4) *(lflag_t)(size_t)(sb0 + K1_FLG + lane * 4) = 0u;
        constexpr int NBP = NJ;
        unsigned vb[NBP], va[4];
#pragma unroll
        for (int i = 0; i < NBP; ++i) vb[i] = boffs((NJ == 5) ? 5 * dw + i : i, lane) - (i & 3) * 1024;
#pragma unroll
        for (int r = 0; r < 4; ++r) va[r] = aoffs + r * (8 * D_ * 2 - 1024);
        const char* xs0 = xa + dw * (128 * D_ * 2);
        const char* ws0 = (NJ == 6 && dw) ? wb1 : wb0;
        auto loadA = [&](int kt, int q0) {
            const unsigned base = sb0 + (kt % 3) * K1_ABYTES + dw * 16384;
            const int kr = kt + rot;
            const char* src = xs0 + (kr >= 10 ? kr - 10 : kr) * 128;
#pragma unroll
            for (int q = q0; q < q0 + 2; ++q) glds_x4(base + q * 4096, src + q * (32 * D_ * 2), va[0], va[1], va[2], va[3]);
        };
        auto loadB = [&](int h) {
            const unsigned base = sb0 + K1_BOFF + (h % 5) * K1_BSLOT + dw * (NBP * 1024);
            const int hr = h + 2 * rot;
            const char* s0 = ws0 + (size_t)(hr >= 20 ? hr - 20 : hr) * (32 * LDB * 2);
            glds_x4(base, s0, vb[0], vb[1], vb[2], vb[3]);
            if (NJ == 5) glds_x1(base + 4096, s0, vb[4]);
            else glds_x2(base + 4096, s0, vb[4], vb[5 % NBP]);
        };
        asm volatile("s_waitcnt lgkmcnt(0)" ::: "memory");
        __builtin_amdgcn_s_barrier();
        CBAR();
        ET(1);
        volatile LDS_AS u16* rdy = (volatile LDS_AS u16*)(size_t)(sb0 + K1_FLG + 32 + (lane & 7) * (2 * NPRD) + (uw - 8) * 2);
        loadB(1);
#pragma unroll 1
        for (int kt = 1; kt < 10; ++kt) {
            if (kt >= 3) k1_wait_rel<PSLP>(sb0, lane, 2 * kt - 4);
            loadB(2 * kt); loadA(kt, 0);
            WAIT_VM(2 * NBP + 8);
            *rdy = (u16)(2 * kt - 1);
            loadA(kt, 2);
            WAIT_VM(NBP + 16);
            *rdy = (u16)(2 * kt);
            if (kt >= 2) k1_wait_rel<PSLP>(sb0, lane, 2 * kt - 3);
            loadB(2 * kt + 1);
        }
        WAIT_VM(NBP);
        *rdy = (u16)19;
        WAIT_VM(0);
        *rdy = (u16)30;
        ET(4); ETW();
        return;
    }

    ET(1);
    const int lane = lane_id();
    ET(2);
    __builtin_amdgcn_s_barrier();
    CBAR();
    const int wr = uw >> 1, wc = uw & 1;
    const int fr = lane & 15, g = lane >> 4;
    f32x4 acc[4][NJ];
#pragma unroll
    for (int i = 0; i < 4; ++i)
#pragma unroll
        for (int j = 0; j < NJ; ++j) acc[i][j] = (f32x4){0.f, 0.f, 0.f, 0.f};

    unsigned aa[2][2], be, bo;
    {
        const int row = wr * 64 + fr;
        aa[0][0] = sb0 + row * 128 + ((g ^ (row & 7)) << 4);
        aa[1][0] = sb0 + row * 128 + (((4 + g) ^ (row & 7)) << 4);
        aa[0][1] = aa[0][0] + 2 * K1_ABYTES;
        aa[1][1] = aa[1][0] + 2 * K1_ABYTES;
        const int q4 = (lane & 15) >> 2, p4 = lane & 3;
        const unsigned bb = sb0 + K1_BOFF + (8 * g + q4) * ROWB + p4 * 8 + wc * (NJ == 5 ? 5 : 3) * 32;
        const int sx = ((g & 1) << 5) * (wc ? -1 : 1);
        be = bb + sx; bo = bb - sx;
    }
    unsigned frel = sb0 + K1_FLG + uw * 4;
    unsigned frdy = sb0 + K1_FLG + 32 + uw * (2 * NPRD) + (lane & (NPRD - 1)) * 2;
    asm volatile("" : "+v"(aa[0][0]), "+v"(aa[1][0]), "+v"(aa[0][1]), "+v"(aa[1][1]), "+v"(be), "+v"(bo), "+v"(frel), "+v"(frdy));
    WAIT_VM(0);
    ET(3);
    *(lflag_t)(size_t)(frel + K1_T0) = 1u;
    {
        lflag_t p = (lflag_t)(size_t)(sb0 + K1_FLG + K1_T0 + (lane & 7) * 4);
        while (__builtin_amdgcn_ballot_w64(*p == 0u) != 0ull) {}
    }
    CBAR();
    ET(4);
    bf16x8 af[2][4], bq[NJ];
    auto rdA = [&](int h, int i) {
        const int st = (h >> 1) % 3;
        return *(const LDS_AS bf16x8*)(size_t)(aa[h & 1][st == 2] + (st == 1 ? K1_ABYTES : 0) + i * 2048);
    };
    auto rdB = [&](int h, int j) {
        const int jj = (NJ == 5) ? j : j % 3, sub = (NJ == 5) ? 0 : (j / 3) * 6144;
        const unsigned b = ((jj & 1) ? bo : be) + (h % 5) * K1_BSLOT + jj * 32 + sub;
        s16x4 lo = tr_read_l(b);
        s16x4 hi = tr_read_l(b + 4 * ROWB);
        return cat4(lo, hi);
    };
    auto rd_rdy = [&]() { return (unsigned)*(volatile LDS_AS u16*)(size_t)frdy; };
#define WAIT_RDY(fl, need) asm volatile("v_cmp_gt_u32 vcc, %2, %0\n\ts_cbranch_vccz L_ok_%=\n" \
        "L_spin_%=:\n\tds_read_u16 %0, %1\n\ts_waitcnt lgkmcnt(0)\n\tv_cmp_gt_u32 vcc, %2, %0\n\ts_cbranch_vccnz L_spin_%=\n" \
        "L_ok_%=:" : "+v"(fl) : "v"(frdy), "n"(need) : "vcc", "memory")
    auto release = [&](unsigned n) { CBAR(); *(lflag_t)(size_t)frel = n; CBAR(); };
#pragma unroll
    for (int i = 0; i < 4; ++i) af[0][i] = rdA(0, i);
    __builtin_amdgcn_sched_barrier(0);
#pragma unroll
    for (int j = 0; j < NJ; ++j) { bq[j] = rdB(0, j); __builtin_amdgcn_sched_barrier(0); }
    release(1u);
    unsigned fl = rd_rdy();
    __builtin_amdgcn_sched_barrier(0);
#pragma unroll
    for (int p = 0; p < 20; ++p) {
#pragma unroll
        for (int sl = 0; sl < 4 * NJ; ++sl) {
            const int j = sl >> 2, i = sl & 3;
            MF1(acc[i][j], bq[j], af[p & 1][i]);
            __builtin_amdgcn_sched_barrier(0);
            if (sl == 0 && p > 0) bq[NJ - 1] = rdB(p, NJ - 1);
            if (sl == 1 && p > 0 && p < (NJ == 5 ? 18 : 16)) release(p + 1);
            if (p < 19) {
                if (sl == 2) WAIT_RDY(fl, p + 2);
                if (sl >= 4 && i == 0) bq[j - 1] = rdB(p + 1, j - 1);
                if (sl == 5) af[(p + 1) & 1][0] = rdA(p + 1, 0);
                if (sl == 6) af[(p + 1) & 1][1] = rdA(p + 1, 1);
                if (sl == 7) af[(p + 1) & 1][2] = rdA(p + 1, 2);
                if (sl == 9) af[(p + 1) & 1][3] = rdA(p + 1, 3);
                if (sl == 4 * NJ - K1FS && p < 18) fl = rd_rdy();
            }
            __builtin_amdgcn_sched_barrier(0);
        }
    }

    k1_epilogue<NJ>(acc, ws, smem, m0, c0, nt, uw ENTPASS);
    ET(5); ETW();
}

#define AUXM 0
#define AUXS 12
#define K1NW 12
template <int NU, int NR, bool EARLY>
__device__ __forceinline__ void k1_aux(const u16* __restrict__ X, const u16* __restrict__ Wo, const u16* __restrict__ Wd,
                                       const u16* __restrict__ gate_w, char* __restrict__ ws, char* wl,
                                       int u0, int us, int ue, int rb, int r0, int rs, int re) {
    const int lane = lane_id();
    float* gout = (float*)(ws + WS_G);
    const int q4 = (lane & 15) >> 2, p4 = lane & 3, g = lane >> 4;
    const rsrc_t rw = mk_rsrc(ws, (unsigned)WS_FLAG);
    auto fetch = [&](int unit, u32x4 (&r)[4]) {
        const int ksg = unit / 10, cc = unit % 10;
#define OPERM 1
        const int e = lane >> 3;
#pragma unroll
        for (int i = 0; i < 4; ++i) {
            const int G = ksg * 4 + i, P = G / 5, gi = G % 5;
            const int ro = (gi < 4) ? (2 * P + (gi >> 1)) * DH_ + (e >> 2) * 8 + 4 * (gi & 1) + (e & 3) : (2 * P + (e >> 2)) * DH_ + 16 + (e & 3);
            const u16* src = (ksg < KS_O) ? Wo + (size_t)ro * D_ : Wd + (size_t)((ksg - KS_O) * 32 + i * 8 + e) * D_;
            r[i] = *(const u32x4*)(src + cc * 64 + (lane & 7) * 8);
        }
    };
    auto emit = [&](int unit, const u32x4 (&r)[4]) {
        const int ksg = unit / 10, cc = unit % 10;
#pragma unroll
        for (int hf = 0; hf < 2; ++hf) {
            CBAR();
            if (((lane >> 2) & 1) == hf) {
#pragma unroll
                for (int i = 0; i < 4; ++i) *(u32x4*)(wl + (i * 8 + (lane >> 3)) * 64 + (lane & 3) * 16) = r[i];
            }
            CBAR();
#pragma unroll
            for (int j = 0; j < 2; ++j) {
                const char* a = wl + (8 * g + q4) * 64 + j * 32 + p4 * 8;
                s16x4 lo = tr_read(a);
                s16x4 hi = tr_read(a + 4 * 64);
                bf16x8 v = cat4(lo, hi);
                const int nt = cc * 4 + hf * 2 + j;
                const int dst = (ksg < KS_O) ? (int)WS_WO + ((nt * KS_O + ksg) * 64 + lane) * 16
                                             : (int)WS_WD + ((nt * KS_H + (ksg - KS_O)) * 64 + lane) * 16;
#define WAUX 0
                bst4<WAUX>(rw, dst, __builtin_bit_cast(u32x4, v));
            }
        }
    };
    u32x4 r[NU ? NU : 1][4];
    u32x4 x0[NR ? NR : 1], x1[NR ? NR : 1], w0, w1;
    auto gfetch = [&]() {
        w0 = *(const u32x4*)(gate_w + lane * 8);
        w1 = *(const u32x4*)(gate_w + 512 + (lane & 15) * 8);
#pragma unroll
        for (int i = 0; i < NR; ++i) {
            int row = r0 + rs * i;
            row = rb + (row < re ? row : r0);
            x0[i] = *(const u32x4*)(X + (size_t)row * D_ + lane * 8);
            x1[i] = *(const u32x4*)(X + (size_t)row * D_ + 512 + (lane & 15) * 8);
        }
    };
    if (NU) {
        const bool extra = u0 + (NU - 1) * us < ue;
#pragma unroll
        for (int u = 0; u < NU - 1; ++u) fetch(u0 + u * us, r[u]);
        if (extra) fetch(u0 + (NU - 1) * us, r[NU - 1]);
        if (EARLY && NR) gfetch();
#pragma unroll
        for (int u = 0; u < NU - 1; ++u) emit(u0 + u * us, r[u]);
        if (extra) emit(u0 + (NU - 1) * us, r[NU - 1]);
    }
    CBAR();
    if (NR == 0) return;
    if (!EARLY || !NU) gfetch();
    if (lane >= 16) w1 = (u32x4){0u, 0u, 0u, 0u};
    float sacc[NR ? NR : 1];
#pragma unroll
    for (int i = 0; i < NR; ++i) {
        float a = 0.f;
#pragma unroll
        for (int e = 0; e < 4; ++e) {
            const unsigned xa = x0[i][e], wa = w0[e], xb = x1[i][e], wb = w1[e];
            a = __builtin_amdgcn_fdot2_f32_bf16(__builtin_bit_cast(bf2_t, xa), __builtin_bit_cast(bf2_t, wa), a, false);
            a = __builtin_amdgcn_fdot2_f32_bf16(__builtin_bit_cast(bf2_t, xb), __builtin_bit_cast(bf2_t, wb), a, false);
        }
        sacc[i] = a;
    }
    {
        float z = 0.f;
#pragma unroll
        for (int i = 0; i < NR; ++i) {
            const float tot = wave_sum(sacc[i]);
            z = (lane == i) ? tot : z;
        }
        const int row = r0 + rs * lane;
        z = rbf(z);
        if (lane < NR && row < re) gout[rb + row] = rbf(1.f / (1.f + __expf(-z)));
    }
}

__global__ __launch_bounds__(K1NW * 64) void k1_gemm(const u16* __restrict__ X, const u16* __restrict__ Wqkv,
                                               const u16* __restrict__ Wg, const u16* __restrict__ Wu,
                                               const u16* __restrict__ Wo, const u16* __restrict__ Wd,
                                               const u16* __restrict__ gate_w, char* __restrict__ ws) {
    __shared__ __attribute__((aligned(16))) char smem[K1_LDS];
    const int bid = blockIdx.x;
    const int xcd = bid & 7, idx = bid >> 3;
    const int ng = xcd >> 1, mg = xcd & 1;
    const int cnt = (ng < 2) ? 8 : 7;
#define NBAL 0
    const int nstart = (ng < 2) ? ng * 8 : 16 + (ng - 2) * 7;
    const int uw = __builtin_amdgcn_readfirstlane(threadIdx.x >> 6);
    if (idx < 4 * cnt && uw < 8) {
        const int ln = lane_id();
        const int mt0 = mg * 4 + (idx & 3), nt0 = nstart + (idx >> 2);
        const bool q = nt0 < 12;
        const unsigned sb0 = lds_addr(smem);
        const unsigned ao = (unsigned)((ln >> 3) * (D_ * 2) + (((ln & 7) ^ (ln >> 3)) << 4));
        const int upr = q ? 20 : 12, mgc = q ? 3277 : 5462, ldb = q ? NQKV * 2 : F_ * 2;
        auto bo = [&](int i) {
            const int p = i * 64 + ln;
            const int k = (p * mgc) >> 16, u = p - k * upr;
            return (unsigned)(k * ldb + (((u >> 1) ^ ((k >> 3) & 1)) << 5) + (u & 1) * 16);
        };
        const unsigned va = bo(q ? 2 * uw : uw), vb = bo(q ? 2 * uw + 1 : uw);
        const char* w0 = q ? (const char*)Wqkv + nt0 * 320 : (const char*)Wg + (nt0 - 12) * 192;
        const char* w1 = q ? (const char*)Wqkv + nt0 * 320 : (const char*)Wu + (nt0 - 12) * 192;
        glds_x4(sb0 + uw * 4096, (const char*)X + (size_t)(mt0 * 256 + uw * 32) * (D_ * 2), ao, ao + (8 * D_ * 2 - 1024), ao + 2 * (8 * D_ * 2 - 1024), ao + 3 * (8 * D_ * 2 - 1024));
        if (uw < (q ? 5 : 6)) {
            glds_x1(sb0 + K1_BOFF + (q ? 2 * uw : uw) * 1024, w0, va);
            glds_x1(sb0 + K1_BOFF + (q ? 2 * uw + 1 : 6 + uw) * 1024, w1, vb);
        }
    }
    if (idx >= 4 * cnt) {
        const int sbk = (xcd - 4) * 4 + (idx - 28);
        if (threadIdx.x < 32)
            *(u32x4*)(ws + WS_FLAG + (size_t)(sbk * 2 + (threadIdx.x >> 4)) * FLAG_LD + (threadIdx.x & 15) * 16) = (u32x4){0u, 0u, 0u, 0u};
        constexpr int NWV = 16 * K1NW;
        const int gw = sbk * K1NW + uw, wq = (((xcd - 4) >> 1) * 4 + (idx - 28)) * K1NW + uw;
        constexpr int NRS = (1024 + NWV / 2 - 1) / (NWV / 2);
        if (AUXM == 0) k1_aux<(740 + NWV - 1) / NWV, NRS, false>(X, Wo, Wd, gate_w, ws, smem + uw * 2048, gw, NWV, 740, mg * 1024, wq, NWV / 2, 1024);
        if (AUXM == 1) k1_aux<1, 0, false>(X, Wo, Wd, gate_w, ws, smem + uw * 2048, 720 + uw * 16 + sbk, 0, 740, 0, 0, 0, 0);
        if (AUXM == 2) k1_aux<1, NRS, true>(X, Wo, Wd, gate_w, ws, smem + uw * 2048, 720 + uw * 16 + sbk, 0, 740, mg * 1024, wq, NWV / 2, 1024);
        if (AUXM == 3) k1_aux<(740 + NWV - 1) / NWV, 0, false>(X, Wo, Wd, gate_w, ws, smem + uw * 2048, gw, NWV, 740, mg * 1024, wq, NWV / 2, 1024);
        if (AUXM == 4) k1_aux<(260 + NWV - 1) / NWV, 0, false>(X, Wo, Wd, gate_w, ws, smem + uw * 2048, 480 + gw, NWV, 740, mg * 1024, wq, NWV / 2, 1024);
        return;
    }
    const int mt = mg * 4 + (idx & 3);
    const int nt = nstart + (idx >> 2);
#define L2PF 0
#define PFD 0
#define PFK 2
#define PFS 0
    if (uw >= 8 + NPRD) {
        if (AUXM == 0 || uw > 10) return;
        __builtin_amdgcn_s_barrier();
        CBAR();
        k1_wait_rel<4>(lds_addr(smem), lane_id(), AUXS);
        const int lin = mt * 30 + nt;
        if (AUXM == 1) k1_aux<3, 9, true>(X, Wo, Wd, gate_w, ws, smem + K1_AUX, lin, 240, 720, mt * 256, nt, 30, 256);
        if (AUXM == 2) k1_aux<3, 0, true>(X, Wo, Wd, gate_w, ws, smem + K1_AUX, lin, 240, 720, mt * 256, nt, 30, 256);
        if (AUXM == 3) k1_aux<0, 9, true>(X, Wo, Wd, gate_w, ws, smem + K1_AUX, lin, 240, 720, mt * 256, nt, 30, 256);
        if (AUXM == 4) k1_aux<2, 9, true>(X, Wo, Wd, gate_w, ws, smem + K1_AUX, lin, 240, 480, mt * 256, nt, 30, 256);
        return;
    }
    if (nt < 12) k1_tile<5>(X, Wqkv, Wqkv, ws, smem, mt, nt, idx >> 2, cnt, uw ENTPASS);
    else k1_tile<6>(X, Wg, Wu, ws, smem, mt, nt - 12, idx >> 2, cnt, uw ENTPASS);
}

typedef unsigned long long u64;
#define GDELAY 8
#define PF2 0
#define ACQ 0
#define ROT 0
#define ROLL 0
#define GPRIO 0
#define POLLSLP 4
#define APRIO 2
#define ST2(i) do {} while (0)
#define ST2ID() do {} while (0)
__device__ __forceinline__ void st_agent(void* p, u32x2 v) {
    __hip_atomic_store((u64*)p, __builtin_bit_cast(u64, v), __ATOMIC_RELAXED, __HIP_MEMORY_SCOPE_AGENT);
}
template <int AUX> __device__ __forceinline__ bf16x8 bld(rsrc_t r, int voff, int soff) {
    return __builtin_bit_cast(bf16x8, __builtin_amdgcn_raw_buffer_load_b128(r, voff, soff, AUX));
}

__device__ __forceinline__ float fmx(float a, float b) { return __builtin_elementwise_maximum(a, b); }

typedef float f32x16 __attribute__((ext_vector_type(16)));
#define MF32(a, b, c) __builtin_amdgcn_mfma_f32_32x32x16_bf16(a, b, c, 0, 0, 0)
#define MF8(a, b, c) __builtin_amdgcn_mfma_f32_32x32x8bf16_1k(a, b, c, 0, 0, 0)
constexpr int AT_V = 15360, AT_PAD = 30720, AT_ONE = AT_PAD + 64;
constexpr int AT_LDS = AT_ONE + 35 * 320 + 16;
template <bool EDGE>
__device__ __forceinline__ void k2_attn_wave(char* __restrict__ oF, const char* smem, const bf16x8 q1, const s16x4 q2,
                                             const float gg, int h, int qw, int rbase, int lane) {
    const int hi = lane >> 5, ql = lane & 31;
    const float NEG = -__builtin_inff();
    const int ts = 8 - (qw >> 5);
    const char* kb = smem + (rbase + ql) * 40;
    const char* ka = kb + hi * 16;
    const char* vb;
    {
        const int q4 = (lane & 15) >> 2, p4 = lane & 3, db = (lane >> 4) & 1;
        vb = smem + AT_V + (rbase + 4 * hi + q4) * 40 + db * 32 + p4 * 8;
        if (db == 1 && p4 >= 1) vb = smem + AT_ONE + (p4 >> 1) * 8;
    }
    auto rd16 = [&](const char* p) {
        const u32x2 lo = *(const u32x2*)p, up = *(const u32x2*)(p + 8);
        const u32x4 v = {lo[0], lo[1], up[0], up[1]};
        return __builtin_bit_cast(bf16x8, v);
    };
    auto rdK1 = [&](int t) { return rd16(ka + t * 1280); };
    auto rdK2 = [&](int t) { return *(const s16x4*)(kb + t * 1280 + 32); };
    auto rdV = [&](int t, int a) {
        s16x4 lo = tr_read(vb + t * 1280 + a * 640);
        s16x4 up = tr_read(vb + t * 1280 + a * 640 + 320);
        return cat4(lo, up);
    };
    f32x16 zero;
#pragma unroll
    for (int i = 0; i < 16; ++i) zero[i] = 0.f;
#define SB0() __builtin_amdgcn_sched_barrier(0)
    auto TL = [](int u) { return u < 7 ? u + 1 : (u == 7 ? 0 : 8); };
    float mx = NEG, my = NEG;
    f32x16 s[3];
    bf16x8 k1[2], vf[2][2];
    s16x4 k2[2];
    k1[0] = rdK1(TL(0)); k2[0] = rdK2(TL(0)); k1[1] = rdK1(TL(1)); k2[1] = rdK2(TL(1));
    SB0();
#pragma unroll
    for (int u = -1; u < 9; ++u) {
        if (u + 1 <= 8) {
            s[(u + 1) % 3] = MF32(k1[(u + 1) & 1], q1, zero);
            SB0();
            if (u + 3 <= 8) k1[(u + 1) & 1] = rdK1(TL(u + 3));
            else if (u + 3 == 9) k1[(u + 1) & 1] = rdK1(1);
            else k1[(u + 1) & 1] = rdK1(2);
        }
        SB0();
        const int p = u - 1;
        if (p >= 0 && p < 7 && (!EDGE || p + 1 >= ts)) {
            const f32x16 x = s[p % 3];
            mx = fmx(fmx(mx, x[0]), x[1]); my = fmx(fmx(my, x[2]), x[3]);
            mx = fmx(fmx(mx, x[4]), x[5]); my = fmx(fmx(my, x[6]), x[7]);
        }
        SB0();
        if (u >= 0) {
            s[u % 3] = MF8(k2[u & 1], q2, s[u % 3]);
            SB0();
            if (u + 2 <= 8) k2[u & 1] = rdK2(TL(u + 2));
            else if (u + 2 == 9) k2[u & 1] = rdK2(1);
            else k2[u & 1] = rdK2(2);
        }
        SB0();
        if (p >= 0 && p < 7 && (!EDGE || p + 1 >= ts)) {
            const f32x16 x = s[p % 3];
            mx = fmx(fmx(mx, x[8]), x[9]); my = fmx(fmx(my, x[10]), x[11]);
            mx = fmx(fmx(mx, x[12]), x[13]); my = fmx(fmx(my, x[14]), x[15]);
        }
        SB0();
    }
    vf[0][0] = rdV(8, 0); vf[0][1] = rdV(8, 1);
    if (!EDGE) { vf[1][0] = rdV(0, 0); vf[1][1] = rdV(0, 1); }
    const int z = ql - 4 * hi;
    f32x16 mg;
#pragma unroll
    for (int i = 0; i < 16; ++i) {
        const bool c = 8 * (i >> 2) + (i & 3) > z;
        mg[i] = c ? (EDGE ? NEG : s[1][i]) : s[2][i];
    }
#pragma unroll
    for (int i = 0; i < 16; i += 4) { mx = fmx(fmx(mx, mg[i]), mg[i + 1]); my = fmx(fmx(my, mg[i + 2]), mg[i + 3]); }
    mx = fmx(mx, my);
    {
        const unsigned mi = __builtin_bit_cast(unsigned, mx);
        auto r = __builtin_amdgcn_permlane32_swap(mi, mi, false, false);
        mx = fmx(__builtin_bit_cast(float, (unsigned)r[0]), __builtin_bit_cast(float, (unsigned)r[1]));
    }
    const float nm = -mx;
    f32x16 cm;
#pragma unroll
    for (int i = 0; i < 16; ++i) cm[i] = nm;
    SB0();
    f32x16 o = zero;
    s[1] = MF32(k1[1], q1, cm);
    SB0();
    k1[1] = rdK1(3);
    {
        u32x4 pa, pb;
#pragma unroll
        for (int a = 0; a < 2; ++a) {
#pragma unroll
            for (int e = 0; e < 4; ++e) {
                const int i = 8 * a + 2 * e;
                const float x0 = __builtin_amdgcn_exp2f(mg[i] + nm), x1 = __builtin_amdgcn_exp2f(mg[i + 1] + nm);
                const unsigned pk = pack_bf16(x0, x1);
                if (EDGE) pa[e] = pk;
                else {
                    const bool c0 = 8 * (i >> 2) + (i & 3) > z, c1 = 8 * (i >> 2) + (i & 3) + 1 > z;
                    pb[e] = pack_bf16(c0 ? x0 : 0.f, c1 ? x1 : 0.f);
                    pa[e] = pk ^ pb[e];
                }
                if (a == 0 && e == 1) { SB0(); s[1] = MF8(k2[1], q2, s[1]); SB0(); k2[1] = rdK2(3); }
            }
            SB0();
            o = MF32(vf[0][a], __builtin_bit_cast(bf16x8, pa), o);
            SB0();
            if (!EDGE) {
                if (a == 1) { s[0] = MF32(k1[0], q1, cm); SB0(); k1[0] = rdK1(4); }
                o = MF32(vf[1][a], __builtin_bit_cast(bf16x8, pb), o);
                SB0();
            } else if (a == 1) { s[0] = MF32(k1[0], q1, cm); SB0(); k1[0] = rdK1(4); }
            vf[1][a] = rdV(1, a);
            vf[0][a] = rdV(2, a);
            SB0();
        }
    }
    u32x4 pk;
    float xa, xb;
#pragma unroll
    for (int t = 1; t < 8; ++t) {
        const bool on = !EDGE || t >= ts;
        auto EX = [&](int i) { xa = __builtin_amdgcn_exp2f(s[t & 1][i]); xb = __builtin_amdgcn_exp2f(s[t & 1][i + 1]); };
        float ya, yb;
        if (t < 7) { s[(t + 1) & 1] = MF8(k2[(t + 1) & 1], q2, s[(t + 1) & 1]); SB0(); if (t + 3 < 8) k2[(t + 1) & 1] = rdK2(t + 3); }
        SB0();
        if (on) {
            EX(0); SB0();
            ya = xa; yb = xb; EX(2); pk[0] = pack_bf16(ya, yb); SB0();
            ya = xa; yb = xb; EX(4); pk[1] = pack_bf16(ya, yb); SB0();
            ya = xa; yb = xb; EX(6); pk[2] = pack_bf16(ya, yb); SB0();
            ya = xa; yb = xb; EX(8); pk[3] = pack_bf16(ya, yb); SB0();
            ya = xa; yb = xb; EX(10);
            const unsigned n0 = pack_bf16(ya, yb); SB0();
            o = MF32(vf[t & 1][0], __builtin_bit_cast(bf16x8, pk), o);
            SB0();
            if (t + 2 < 8) vf[t & 1][0] = rdV(t + 2, 0);
            u32x4 pn;
            pn[0] = n0;
            ya = xa; yb = xb; EX(12); pn[1] = pack_bf16(ya, yb); SB0();
            ya = xa; yb = xb; EX(14); pn[2] = pack_bf16(ya, yb); SB0();
            if (!EDGE && t + 2 < 8) { s[t & 1] = MF32(k1[t & 1], q1, cm); SB0(); if (t + 4 < 8) k1[t & 1] = rdK1(t + 4); }
            pn[3] = pack_bf16(xa, xb); SB0();
            o = MF32(vf[t & 1][1], __builtin_bit_cast(bf16x8, pn), o);
            SB0();
            if (t + 2 < 8) vf[t & 1][1] = rdV(t + 2, 1);
        } else if (t + 2 < 8) { vf[t & 1][0] = rdV(t + 2, 0); vf[t & 1][1] = rdV(t + 2, 1); }
        SB0();
        if (EDGE && t + 2 < 8) { s[t & 1] = MF32(k1[t & 1], q1, cm); SB0(); if (t + 4 < 8) k1[t & 1] = rdK1(t + 4); }
        SB0();
    }
    float sum;
    {
        const float o8 = o[8];
        const unsigned si = __float_as_uint(o8);
        auto r = __builtin_amdgcn_permlane32_swap(si, si, false, false);
        sum = __builtin_bit_cast(float, (unsigned)r[1]);
    }
    const float inv = gg * __builtin_amdgcn_rcpf(sum);
    const int m = qw + ql;
    {
        const rsrc_t ro = mk_rsrc(oF, (unsigned)(S_ * D_ * 2));
        const int ob = ((m >> 4) * KS_O * 64 + (m & 15)) * 16 + (h >> 1) * 1280;
        const u32x4 a = {pack_bf16(o[0] * inv, o[1] * inv), pack_bf16(o[2] * inv, o[3] * inv), pack_bf16(o[4] * inv, o[5] * inv), pack_bf16(o[6] * inv, o[7] * inv)};
        bst4<AUX_AGENT>(ro, ob + (2 * (h & 1) + hi) * 256, a);
        const u32x2 b = {pack_bf16(o[8] * inv, o[9] * inv), pack_bf16(o[10] * inv, o[11] * inv)};
        if (hi == 0) bst2<AUX_AGENT>(ro, ob + 1024 + (h & 1) * 8, b);
    }
}

__device__ __forceinline__ void k2_attn(char* __restrict__ ws, char* smem, int bid) {
    const int h = (bid & 7) + 8 * ((bid >> 3) & 3);
    const int qb = bid >> 5;
    const int q0 = qb * 128;
    const int t = threadIdx.x, lane = t & 63;
    const int w = __builtin_amdgcn_readfirstlane(t >> 6);
    const char* Qb = ws + WS_Q + (size_t)h * S_ * 40;
    const char* Kb = ws + WS_K + (size_t)h * S_ * 40;
    const char* Vb = ws + WS_V + (size_t)h * S_ * 40;
    char* sK = smem;
    char* sV = smem + AT_V;

    ST2(0); ST2ID();
    if (APRIO) __builtin_amdgcn_s_setprio(APRIO);
#pragma unroll
    for (int i = 0; i < 4; ++i) {
        const int p = i * 256 + t;
        if (i < 3 || w < 3) {
            int off = (q0 - 256) * 40 + p * 16;
            off = off < 0 ? 0 : off;
            glds16(Kb + off, sK + p * 16);
            glds16(Vb + off, sV + p * 16);
        }
    }
    if (t < 4) *(u32x4*)(smem + AT_PAD + t * 16) = (u32x4){0u, 0u, 0u, 0u};
    if (t >= 64 && t < 100) *(u32x4*)(smem + AT_ONE + (t - 64) * 320) = (u32x4){0x3f80u, 0u, 0u, 0u};
    const int hi = lane >> 5;
    const int qw = q0 + 32 * w;
    bf16x8 q1;
    s16x4 q2;
    {
        const char* qp = Qb + (size_t)(qw + (lane & 31)) * 40;
        const u32x2 a = *(const u32x2*)(qp + hi * 16), b = *(const u32x2*)(qp + hi * 16 + 8);
        u32x2 c = *(const u32x2*)(qp + 32);
        if (hi) c = (u32x2){0u, 0u};
        const u32x4 v1 = {a[0], a[1], b[0], b[1]};
        q1 = __builtin_bit_cast(bf16x8, v1); q2 = __builtin_bit_cast(s16x4, c);
    }
    const float gg = ((const float*)(ws + WS_G))[qw + (lane & 31)];
    __syncthreads();
    ST2(1);
    char* oF = ws + WS_O;
    if (qb >= 2) k2_attn_wave<false>(oF, smem, q1, q2, gg, h, qw, 32 * w, lane);
    else k2_attn_wave<true>(oF, smem, q1, q2, gg, h, qw, 32 * w, lane);
    ST2(2);
    asm volatile("s_waitcnt vmcnt(0)" ::: "memory");
    ST2(3);
    if (lane == 0)
        __hip_atomic_store((unsigned*)(ws + WS_FLAG + (qw >> 6) * FLAG_LD) + h * 2 + ((qw >> 5) & 1), 1u, __ATOMIC_RELAXED, __HIP_MEMORY_SCOPE_AGENT);
}

__device__ __forceinline__ void k2_gemm(char* __restrict__ ws, u16* __restrict__ out, char* smem, int bid) {
    const int xcd = bid & 7, idx = bid >> 3;
    const int mt = (xcd >> 1) * 8 + (idx & 7);
    const int nt = (xcd & 1) * 4 + (idx >> 3);
    const int t = threadIdx.x, lane = t & 63;
    const int w = __builtin_amdgcn_readfirstlane(t >> 6);
    const int fr = lane & 15, g = lane >> 4;
    const int nsteps = (w < 3) ? 14 : 12;
    const rsrc_t rs = __builtin_amdgcn_make_buffer_rsrc(ws, 0, (int)WS_FLAG, 0x00020000);
    const int vo = lane * 16;
    int ap[4];
#pragma unroll
#define K2SAME 0
    for (int i = 0; i < 4; ++i) ap[i] = (int)WS_H + ((((K2SAME & 1) ? 0 : (K2SAME & 4) ? (mt & 24) : mt) * 4 + (i ^ w)) * KS_H + 14 * w) * 1024;
    int bp = (int)WS_WD + ((((K2SAME & 2) ? 0 : nt) * 5) * KS_H + 14 * w) * 1024;
#define RTA 0
#define RTB 0
    int cur = ((RTA * (nt & 3) + (RTB * (mt & 7) >> 2)) % nsteps) * 1024;
    auto adv = [&]() { cur += 1024; cur = (cur == nsteps * 1024) ? 0 : cur; };

    f32x4 acc[4][5];
#pragma unroll
    for (int i = 0; i < 4; ++i)
#pragma unroll
        for (int j = 0; j < 5; ++j) acc[i][j] = (f32x4){0.f, 0.f, 0.f, 0.f};
    bf16x8 a0[4], b0[5], a1[4], b1[5];
#define SB() __builtin_amdgcn_sched_barrier(0)
#define MFA(c, b, a) asm volatile("v_mfma_f32_16x16x32_bf16 %0, %1, %2, %0" : "+v"(c) : "v"(b), "v"(a))
    const u32x4 rq = {(unsigned)(size_t)ws, (unsigned)((size_t)ws >> 32) & 0xffffu, (unsigned)WS_FLAG, 0x00020000u};
#define LDN(r, so) asm volatile("buffer_load_dwordx4 %0, %1, %2, %3 offen" : "=v"(r) : "v"(vo), "s"(rq), "s"(so) : "memory")
#define LDR(r, so) asm volatile("buffer_load_dwordx4 %0, %1, %2, %3 offen" : "+v"(r) : "v"(vo), "s"(rq), "s"(so) : "memory")
#define LDC(r, so) asm volatile("buffer_load_dwordx4 %0, %1, %2, %3 offen sc1" : "+v"(r) : "v"(vo), "s"(rq), "s"(so) : "memory")
    auto ld = [&](bf16x8 (&a)[4], bf16x8 (&b)[5]) {
#pragma unroll
        for (int j = 0; j < 4; ++j) LDN(b[j], bp + j * KS_H * 1024 + cur);
#pragma unroll
        for (int i = 0; i < 4; ++i) LDN(a[i], ap[i] + cur);
        LDN(b[4], bp + 4 * KS_H * 1024 + cur);
        adv();
    };
    auto mmx = [&](auto w0, auto w4, bf16x8 (&a)[4], bf16x8 (&b)[5], auto fa, auto fb) {
        constexpr int W0 = decltype(w0)::value, W4 = decltype(w4)::value;
#pragma unroll
        for (int j = 0; j < 5; ++j) {
#pragma unroll
            for (int i = 0; i < 4; ++i) {
                if (j == 0) { if (i == 0) WAIT_VM(W0); if (i == 1) WAIT_VM(W0 - 1); if (i == 2) WAIT_VM(W0 - 2); if (i == 3) WAIT_VM(W0 - 3); }
                if (j == 4 && i == 0 && W4 >= 0) WAIT_VM(W4 >= 0 ? W4 : 0);
                MFA(acc[i][j], b[j], a[i]);
                if (j == 4) fa(a[i], i);
            }
            fb(b[j], j);
        }
    };
#define IC(n) std::integral_constant<int, n>{}
    auto nof = [&](bf16x8&, int) {};
    auto fha = [&](bf16x8& r, int i) { LDR(r, ap[i] + cur); };
    auto fhb = [&](bf16x8& r, int j) { LDR(r, bp + j * KS_H * 1024 + cur); };
    ST2(0); ST2ID();
    if (GPRIO) __builtin_amdgcn_s_setprio(GPRIO);
#pragma unroll 1
    for (int i = 0; i < GDELAY; ++i) __builtin_amdgcn_s_sleep(8);
    float gl;
    asm volatile("global_load_dword %0, %1, %2" : "=v"(gl) : "v"(vo >> 2), "s"(ws + WS_G + mt * 256) : "memory");
    ld(a0, b0);
    ld(a1, b1);
#pragma unroll
    for (int i = 0; i < 4; ++i) asm volatile("s_nop 1" : "+v"(acc[i][0]), "+v"(acc[i][1]), "+v"(acc[i][2]), "+v"(acc[i][3]), "+v"(acc[i][4]));
#pragma unroll 1
    for (int s = 0; s < nsteps - 4; s += 2) {
        mmx(IC(13), IC(13), a0, b0, fha, fhb); adv();
        mmx(IC(13), IC(13), a1, b1, fha, fhb); adv();
    }
    const char* fb_ = ws + WS_FLAG + mt * FLAG_LD + 64 * w;
    const int fv = (vo >> 2) & 60;
    mmx(IC(13), IC(13), a0, b0, fha, fhb); adv();
    unsigned fl;
    asm volatile("global_load_dword %0, %1, %2 sc1" : "=v"(fl) : "v"(fv), "s"(fb_) : "memory");
    mmx(IC(14), IC(14), a1, b1, fha, fhb);
    ST2(1);
    int op[4];
#pragma unroll
    for (int i = 0; i < 4; ++i) op[i] = (int)WS_O + ((mt * 4 + (i ^ w)) * KS_O + 5 * w) * 1024;
    const int wp = (int)WS_WO + ((nt * 5) * KS_O + 5 * w) * 1024;
    int so = 0;
    auto foa = [&](bf16x8& r, int i) { LDC(r, op[i] + so); };
    auto fob = [&](bf16x8& r, int j) { LDR(r, wp + j * KS_O * 1024 + so); };
    mmx(IC(14), IC(14), a0, b0, nof, fob);
    ST2(2);
    asm volatile("s_waitcnt vmcnt(14)\n\tv_cmp_ne_u32 vcc, 1, %0\n\ts_cbranch_vccz L_pok_%=\n"
                 "L_pspin_%=:\n\ts_sleep %3\n\tglobal_load_dword %0, %1, %2 sc1\n\ts_waitcnt vmcnt(0)\n\t"
                 "v_cmp_ne_u32 vcc, 1, %0\n\ts_cbranch_vccnz L_pspin_%=\n"
                 "L_pok_%=:" : "+v"(fl) : "v"(fv), "s"(fb_), "n"(POLLSLP) : "vcc", "memory");
    ST2(3);
#pragma unroll
    for (int i = 0; i < 4; ++i) foa(a0[i], i);
    so = 1024;
    mmx(IC(13), IC(13), a1, b1, foa, fob);
    asm volatile("s_nop 7\n\ts_nop 7" : "+v"(gl) :: "memory");
#pragma unroll
    for (int i = 0; i < 4; ++i) {
        const float sc = rbf(1.f - __builtin_bit_cast(float, __builtin_amdgcn_ds_bpermute((i ^ w) * 64 + fv, __builtin_bit_cast(int, gl))));
        asm volatile("" : "+v"(acc[i][0]), "+v"(acc[i][1]), "+v"(acc[i][2]), "+v"(acc[i][3]), "+v"(acc[i][4]));
#pragma unroll
        for (int j = 0; j < 5; ++j) acc[i][j] = acc[i][j] * sc;
        asm volatile("" : "+v"(acc[i][0]), "+v"(acc[i][1]), "+v"(acc[i][2]), "+v"(acc[i][3]), "+v"(acc[i][4]));
    }
    asm volatile("s_nop 3" ::: "memory");
    so = 2048; mmx(IC(12), IC(-1), a0, b0, foa, fob);
    so = 3072; mmx(IC(13), IC(13), a1, b1, foa, fob);
    so = 4096; mmx(IC(13), IC(13), a0, b0, foa, fob);
    mmx(IC(13), IC(9), a1, b1, nof, nof);
    mmx(IC(4), IC(0), a0, b0, nof, nof);
    asm volatile("s_nop 7\n\ts_nop 7" ::: "memory");
#pragma unroll
    for (int i = 0; i < 4; ++i) asm volatile("" : "+v"(acc[i][0]), "+v"(acc[i][1]), "+v"(acc[i][2]), "+v"(acc[i][3]), "+v"(acc[i][4]));
    ST2(4);
    const int ln = lane_id();
    char* mine = smem + w * 10240 + ln * 16;
    char* peer = smem + (w ^ 2) * 10240 + ln * 16;
#pragma unroll
    for (int i = 0; i < 2; ++i)
#pragma unroll
        for (int j = 0; j < 5; ++j) *(f32x4*)(mine + (i * 5 + j) * 1024) = acc[2 + i][j];
    __syncthreads();
#pragma unroll
    for (int j = 0; j < 5; ++j) acc[1][j] += *(const f32x4*)(peer + (5 + j) * 1024);
#pragma unroll
    for (int j = 0; j < 5; ++j) *(f32x4*)(peer + (5 + j) * 1024) = acc[1][j];
#pragma unroll
    for (int j = 0; j < 5; ++j) acc[0][j] += *(const f32x4*)(peer + j * 1024);
    __syncthreads();
    {
        const char* q = smem + (w ^ 3) * 10240 + ln * 16 + 5 * 1024;
        u16* dst = out + (size_t)(mt * 64 + w * 16 + (ln & 15)) * D_ + nt * 80 + (ln >> 4) * 4;
#pragma unroll
        for (int j = 0; j < 5; ++j) {
            const f32x4 v = acc[0][j] + *(const f32x4*)(q + j * 1024);
            u32x2 pk = {pack_bf16(v[0], v[1]), pack_bf16(v[2], v[3])};
            *(u32x2*)(dst + j * 16) = pk;
        }
    }
    ST2(5);
}

__global__ __launch_bounds__(256, 3) void k2_mix(char* __restrict__ ws, u16* __restrict__ out) {
    __shared__ __attribute__((aligned(16))) char smem[AT_LDS > 40960 ? AT_LDS : 40960];
    if (blockIdx.x < 256) k2_gemm(ws, out, smem, blockIdx.x);
    else k2_attn(ws, smem, blockIdx.x - 256);
}

extern "C" void kernel_launch(void* const* d_in, const int* in_sizes, int n_in,
                              void* d_out, int out_size, void* d_ws, size_t ws_size,
                              hipStream_t stream) {
    const u16* X = (const u16*)d_in[0];
    const u16* Wqkv = (const u16*)d_in[1];
    const u16* Wo = (const u16*)d_in[2];
    const u16* Wg = (const u16*)d_in[4];
    const u16* Wu = (const u16*)d_in[5];
    const u16* Wd = (const u16*)d_in[6];
    const u16* gw = (const u16*)d_in[7];
    char* ws = (char*)d_ws;
    k1_gemm<<<256, K1NW * 64, 0, stream>>>(X, Wqkv, Wg, Wu, Wo, Wd, gw, ws);
    k2_mix<<<768, 256, 0, stream>>>(ws, (u16*)d_out);
}
```

```cpp
#include <hip/hip_runtime.h>
#include <hip/hip_bf16.h>
#include <stdint.h>
#include <type_traits>

typedef unsigned short u16;
typedef short bf16x8 __attribute__((ext_vector_type(8)));
typedef short s16x4 __attribute__((ext_vector_type(4)));
typedef float f32x4 __attribute__((ext_vector_type(4)));
typedef float f32x2 __attribute__((ext_vector_type(2)));
typedef unsigned u32x4 __attribute__((ext_vector_type(4)));
typedef unsigned u32x2 __attribute__((ext_vector_type(2)));
typedef __bf16 bf2_t __attribute__((ext_vector_type(2)));

#define LDS_AS __attribute__((address_space(3)))

constexpr int S_ = 2048, D_ = 640, F_ = 1728, H_ = 32, DH_ = 20, NQKV = 1920;
constexpr int KS_O = D_ / 32;
constexpr int KS_H = F_ / 32;

constexpr size_t WS_Q = 0;
constexpr size_t QKV_BYTES = (size_t)H_ * S_ * DH_ * 2;
constexpr size_t WS_K = WS_Q + QKV_BYTES;
constexpr size_t WS_V = WS_K + QKV_BYTES;
constexpr size_t WS_H = WS_V + QKV_BYTES + 4096;
constexpr size_t WS_O = WS_H + (size_t)S_ * F_ * 2;
constexpr size_t WS_WO = WS_O + (size_t)S_ * D_ * 2;
constexpr size_t WS_WD = WS_WO + (size_t)D_ * D_ * 2;
constexpr size_t WS_G = WS_WD + (size_t)F_ * D_ * 2;
constexpr size_t WS_FLAG = WS_G + (size_t)S_ * 4;
constexpr int FLAG_LD = 4352;

__device__ __forceinline__ unsigned pack_bf16(float a, float b) {
    bf2_t v = {(__bf16)a, (__bf16)b};
    return __builtin_bit_cast(unsigned, v);
}
__device__ __forceinline__ float rbf(float x) { return (float)(__bf16)x; }
__device__ __forceinline__ float bf2f(u16 v) { return __uint_as_float(((unsigned)v) << 16); }

template <int CTRL> __device__ __forceinline__ float dpp_add(float x) {
    return x + __builtin_bit_cast(float, __builtin_amdgcn_update_dpp(0, __builtin_bit_cast(int, x), CTRL, 0xf, 0xf, false));
}
__device__ __forceinline__ float wave_sum(float x) {
    x = dpp_add<0xB1>(x); x = dpp_add<0x4E>(x); x = dpp_add<0x141>(x); x = dpp_add<0x140>(x);
    const int xi = __builtin_bit_cast(int, x);
    return (__builtin_bit_cast(float, __builtin_amdgcn_readlane(xi, 0)) + __builtin_bit_cast(float, __builtin_amdgcn_readlane(xi, 16))) +
           (__builtin_bit_cast(float, __builtin_amdgcn_readlane(xi, 32)) + __builtin_bit_cast(float, __builtin_amdgcn_readlane(xi, 48)));
}
__device__ __forceinline__ s16x4 tr_read(const char* lds_ptr) {
    return __builtin_amdgcn_ds_read_tr16_b64_v4i16((LDS_AS s16x4*)(lds_ptr));
}
__device__ __forceinline__ bf16x8 cat4(s16x4 a, s16x4 b) {
    bf16x8 r;
    r[0] = a[0]; r[1] = a[1]; r[2] = a[2]; r[3] = a[3];
    r[4] = b[0]; r[5] = b[1]; r[6] = b[2]; r[7] = b[3];
    return r;
}
__device__ __forceinline__ void glds16(const void* g, void* l) {
    __builtin_amdgcn_global_load_lds((const unsigned*)g, (LDS_AS unsigned*)l, 16, 0, 0);
}
__device__ __forceinline__ void glds16_asm(const void* gsrc, unsigned lds_dst) {
    unsigned keep;
    asm volatile("s_mov_b32 %0, m0\n\ts_mov_b32 m0, %2\n\ts_nop 0\n\tglobal_load_lds_dwordx4 %1, off\n\ts_mov_b32 m0, %0"
                 : "=&s"(keep) : "v"(gsrc), "s"(lds_dst) : "memory");
}
__device__ __forceinline__ void glds4_asm(const void* gsrc, unsigned lds_dst) {
    unsigned keep;
    asm volatile("s_mov_b32 %0, m0\n\ts_mov_b32 m0, %2\n\ts_nop 0\n\tglobal_load_lds_dword %1, off\n\ts_mov_b32 m0, %0"
                 : "=&s"(keep) : "v"(gsrc), "s"(lds_dst) : "memory");
}
__device__ __forceinline__ unsigned lds_addr(const void* p) { return (unsigned)(size_t)(LDS_AS const char*)p; }
typedef __amdgpu_buffer_rsrc_t rsrc_t;
constexpr int AUX_AGENT = 16;
__device__ __forceinline__ rsrc_t mk_rsrc(const void* p, unsigned bytes) { return __builtin_amdgcn_make_buffer_rsrc((void*)p, 0, (int)bytes, 0x00020000); }
template <int AUX> __device__ __forceinline__ void bst4(rsrc_t r, int voff, u32x4 v) { __builtin_amdgcn_raw_buffer_store_b128(v, r, voff, 0, AUX); }
template <int AUX> __device__ __forceinline__ void bst2(rsrc_t r, int voff, u32x2 v) { __builtin_amdgcn_raw_buffer_store_b64(v, r, voff, 0, AUX); }
#define WAIT_VM(n) asm volatile("s_waitcnt vmcnt(%0)" :: "n"(n) : "memory")
#define CBAR() asm volatile("" ::: "memory")
#define STAMP(kid, i) do {} while (0)
#define STAMPK(kid, i) do { if ((i) == 3) STAMP(kid, i); } while (0)

#define ET(i) do {} while (0)
#define ETW() do {} while (0)
#define ENTARG
#define ENTPASS
#define WPG 64
#define OSC 0
#define ESC
#define HAUX 16
#define QAUX 16
#define WSC1
#define K1SB 0
#define K1P0 0
#define K1UNR 1
constexpr int K1_ABYTES = 256 * 64 * 2;
constexpr int K1_BSLOT = 32 * 192 * 2;
constexpr int K1_BOFF = 3 * K1_ABYTES;
constexpr int K1_AUX = K1_BOFF + 5 * K1_BSLOT;
constexpr int K1_FLG = K1_AUX + 2048;
#define PSLP 0
#define PSCH 0
#define BDEP 2
#define EARLYLD 0
constexpr int NPRD = (PSCH == 6) ? 4 : 2;
constexpr int K1_T0 = 32 + 16 * NPRD;
constexpr int K1_LDS = K1_FLG + K1_T0 + 32;
#define K1FJ 2
#define K1FS 6
#define PFL 4
#define MF1(c, b, a) c = __builtin_amdgcn_mfma_f32_16x16x32_bf16(b, a, c, 0, 0, 0)
typedef volatile LDS_AS unsigned* lflag_t;
template <int SLP> __device__ __forceinline__ void k1_wait_rel(unsigned sb0, int lane, unsigned n) {
    lflag_t p = (lflag_t)(size_t)(sb0 + K1_FLG + (lane & 7) * 4);
    while (__builtin_amdgcn_ballot_w64(*p < n) != 0ull) { if (SLP) __builtin_amdgcn_s_sleep(SLP); }
    CBAR();
}
__device__ __forceinline__ void glds_x4(unsigned lds, const void* sb, unsigned v0, unsigned v1, unsigned v2, unsigned v3) {
    unsigned keep;
    asm volatile("s_mov_b32 %0, m0\n\ts_mov_b32 m0, %1\n\ts_nop 0\n\t"
                 "global_load_lds_dwordx4 %3, %2\n\t"
                 "global_load_lds_dwordx4 %4, %2 offset:1024\n\t"
                 "global_load_lds_dwordx4 %5, %2 offset:2048\n\t"
                 "global_load_lds_dwordx4 %6, %2 offset:3072\n\t"
                 "s_mov_b32 m0, %0"
                 : "=&s"(keep) : "s"(lds), "s"(sb), "v"(v0), "v"(v1), "v"(v2), "v"(v3) : "memory");
}
__device__ __forceinline__ void glds_x2(unsigned lds, const void* sb, unsigned v0, unsigned v1) {
    unsigned keep;
    asm volatile("s_mov_b32 %0, m0\n\ts_mov_b32 m0, %1\n\ts_nop 0\n\t"
                 "global_load_lds_dwordx4 %3, %2\n\t"
                 "global_load_lds_dwordx4 %4, %2 offset:1024\n\t"
                 "s_mov_b32 m0, %0"
                 : "=&s"(keep) : "s"(lds), "s"(sb), "v"(v0), "v"(v1) : "memory");
}
__device__ __forceinline__ void glds_x1(unsigned lds, const void* sb, unsigned v0) {
    unsigned keep;
    asm volatile("s_mov_b32 %0, m0\n\ts_mov_b32 m0, %1\n\ts_nop 0\n\t"
                 "global_load_lds_dwordx4 %3, %2\n\t"
                 "s_mov_b32 m0, %0"
                 : "=&s"(keep) : "s"(lds), "s"(sb), "v"(v0) : "memory");
}
__device__ __forceinline__ int lane_id() {
    int l;
    asm volatile("v_mbcnt_lo_u32_b32 %0, -1, 0\n\tv_mbcnt_hi_u32_b32 %0, -1, %0" : "=v"(l));
    return l;
}
__device__ __forceinline__ s16x4 tr_read_l(unsigned a) { return __builtin_amdgcn_ds_read_tr16_b64_v4i16((LDS_AS s16x4*)(size_t)a); }

template <int NJ>
__device__ __forceinline__ void k1_epilogue(f32x4 (&acc)[4][NJ], char* __restrict__ ws, char* smem, int m0, int c0, int nt, int wid ENTARG) {
    const int lane = lane_id();
    const int t = wid * 64 + lane;
    const int wr = wid >> 1, wc = wid & 1;
    const int fr = lane & 15, g = lane >> 4;
    const rsrc_t rw = mk_rsrc(ws, (unsigned)WS_FLAG);
#define EPI5 1
    if (NJ == 5 && EPI5) {
        const unsigned sb0 = lds_addr(smem);
        k1_wait_rel<0>(sb0, lane, 18u);
        char* img = smem + K1_ABYTES + wid * 10240;
        if (nt < 4) {
            constexpr float qs = 0.22360679774997896f * 1.4426950408889634f;
#pragma unroll
            for (int j = 0; j < NJ; ++j)
#pragma unroll
                for (int i = 0; i < 4; ++i) {
                    f32x2 lo = {acc[i][j][0], acc[i][j][1]}, hi = {acc[i][j][2], acc[i][j][3]};
                    lo = lo * qs; hi = hi * qs;
                    acc[i][j] = (f32x4){lo[0], lo[1], hi[0], hi[1]};
                }
        }
#pragma unroll
        for (int j = 0; j < NJ; ++j) {
            const unsigned u = 4 * j + g;
            char* dst = img + fr * 40 + u * 8 + ((u * 13u) >> 6) * 2520u;
#pragma unroll
            for (int i = 0; i < 4; ++i) {
                u32x2 a = {pack_bf16(acc[i][j][0], acc[i][j][1]), pack_bf16(acc[i][j][2], acc[i][j][3])};
                *(u32x2*)(dst + i * 640) = a;
            }
        }
        CBAR();
        u32x4 v[10];
#pragma unroll
        for (int it = 0; it < 10; ++it) v[it] = *(const u32x4*)(img + it * 1024 + lane * 16);
        const int which = nt >> 2;
        const int dofs = (int)(WS_Q + (size_t)which * QKV_BYTES) + (((nt & 3) * 8 + wc * 4) * S_ + m0 + wr * 64) * 40;
        constexpr int HS = S_ * 40 - 2560;
        const int v0 = lane * 16, v1 = v0 + (lane >= 32 ? HS : 0);
#pragma unroll
        for (int it = 0; it < 10; ++it) {
            const int hh = (it / 5) * 2 + ((it % 5) >= 3 ? 1 : 0);
            __builtin_amdgcn_raw_buffer_store_b128(v[it], rw, (it % 5) == 2 ? v1 : v0, dofs + it * 1024 + hh * HS, QAUX);
        }
    } else if (NJ == 5) {
        const unsigned sb0 = lds_addr(smem);
        CBAR(); *(lflag_t)(size_t)(sb0 + K1_FLG + wid * 4) = 21u; CBAR();
        k1_wait_rel<0>(sb0, lane, 21u);
        const float qs = (nt < 4) ? 0.22360679774997896f * 1.4426950408889634f : 1.f;
#pragma unroll
        for (int j = 0; j < NJ; ++j) {
            const int n = wc * 80 + j * 16 + g * 4;
            const int hh = n / DH_, d = n % DH_;
            char* dst = smem + hh * 10240 + (wr * 64 + fr) * 40 + d * 2;
#pragma unroll
            for (int i = 0; i < 4; ++i) {
                u32x2 a = {pack_bf16(acc[i][j][0] * qs, acc[i][j][1] * qs), pack_bf16(acc[i][j][2] * qs, acc[i][j][3] * qs)};
                *(u32x2*)(dst + i * 640) = a;
            }
        }
        CBAR(); *(lflag_t)(size_t)(sb0 + K1_FLG + wid * 4) = 22u; CBAR();
        k1_wait_rel<0>(sb0, lane, 22u);
        const int which = nt >> 2;
        const int h0 = (nt & 3) * 8;
        char* dbase = ws + WS_Q + (size_t)which * QKV_BYTES + ((size_t)h0 * S_ + m0) * 40;
        const int dofs = (int)(WS_Q + (size_t)which * QKV_BYTES + ((size_t)h0 * S_ + m0) * 40);
#pragma unroll
        for (int it = 0; it < 10; ++it) {
            const int e = it * 512 + t;
            const int hh = e / 640, r = e % 640;
            const u32x4 v = *(const u32x4*)(smem + e * 16);
            bst4<QAUX>(rw, dofs + hh * (S_ * 40) + r * 16, v);
        }
    } else {
        u16* hF = (u16*)(ws + WS_H);
#pragma unroll
        for (int j = 0; j < 3; ++j) {
            const int f = c0 + wc * 48 + j * 16 + g * 4;
            const int ks = f >> 5;
            const int lq = (f & 31) >> 3;
            const int hf = f & 4;
#pragma unroll
            for (int i = 0; i < 4; ++i) {
                const int m = m0 + wr * 64 + i * 16 + fr;
                unsigned pk[2];
#pragma unroll
                for (int r = 0; r < 2; ++r) {
                    const f32x2 gt = {acc[i][j][2 * r], acc[i][j][2 * r + 1]};
                    const f32x2 up = {acc[i][(j + 3) % NJ][2 * r], acc[i][(j + 3) % NJ][2 * r + 1]};
                    f32x2 e = gt * -1.4426950408889634f;
                    e = (f32x2){__builtin_amdgcn_exp2f(e[0]), __builtin_amdgcn_exp2f(e[1])};
                    e = e + 1.f;
                    const f32x2 rc = {__builtin_amdgcn_rcpf(e[0]), __builtin_amdgcn_rcpf(e[1])};
                    const f32x2 hv = (gt * up) * rc;
                    pk[r] = pack_bf16(hv[0], hv[1]);
                }
                u32x2 a = {pk[0], pk[1]};
                u16* dst = hF + ((size_t)((m >> 4) * KS_H + ks) * 64 + (m & 15) + 16 * lq) * 8 + hf;
                bst2<HAUX>(rw, (int)WS_H + ((((m >> 4) * KS_H + ks) * 64 + (m & 15) + 16 * lq) * 8 + hf) * 2, a);
            }
        }
    }
}

template <int NJ>
__device__ __forceinline__ void k1_tile(const u16* __restrict__ X, const u16* __restrict__ W0, const u16* __restrict__ W1,
                                        char* __restrict__ ws, char* smem, int mt, int nt, int xs, int xcnt, int uw ENTARG) {
    constexpr int BN = 32 * NJ;
    constexpr int ROWB = (NJ == 5) ? 320 : 192;
    constexpr int UPR = ROWB / 16;
    constexpr int NBH = UPR / 2;
    constexpr int NB = 2 * NJ;
    constexpr int LDB = (NJ == 5) ? NQKV : F_;
    const int m0 = mt * 256;
    const int c0 = (NJ == 5) ? nt * 160 : nt * 96;
    const unsigned sb0 = lds_addr(smem);
    auto boffs = [&](int i, int lane) {
        const int p = i * 64 + lane;
        const int k = p / UPR, u = p % UPR;
        return (unsigned)((k * LDB + (((u >> 1) ^ ((k >> 3) & 1)) << 4) + (u & 1) * 8) * 2);
    };
    auto aoffs_f = [&](int lane) { return (unsigned)((lane >> 3) * (D_ * 2) + (((lane & 7) ^ (lane >> 3)) << 4)); };
    const char* xa = (const char*)(X + (size_t)m0 * D_);
    const char* wb0 = (const char*)(W0 + c0);
    const char* wb1 = (const char*)(W1 + c0);

#define KRA 0
#define KRB 0
    const int rot = (xs * KRA + ((mt & 3) * KRB >> 1)) % 10;
    if (uw >= 8) {
        const int dw = uw & 1;
        const int lane = lane_id();
        const unsigned aoffs = aoffs_f(lane);
        if (uw == 8 && lane < (K1_T0 + 32) / 4) *(lflag_t)(size_t)(sb0 + K1_FLG + lane * 4) = 0u;
        constexpr int NBP = NJ;
        unsigned vb[NBP], va[4];
#pragma unroll
        for (int i = 0; i < NBP; ++i) vb[i] = boffs((NJ == 5) ? 5 * dw + i : i, lane) - (i & 3) * 1024;
#pragma unroll
        for (int r = 0; r < 4; ++r) va[r] = aoffs + r * (8 * D_ * 2 - 1024);
        const char* xs0 = xa + dw * (128 * D_ * 2);
        const char* ws0 = (NJ == 6 && dw) ? wb1 : wb0;
        auto loadA = [&](int kt, int q0) {
            const unsigned base = sb0 + (kt % 3) * K1_ABYTES + dw * 16384;
            const int kr = kt + rot;
            const char* src = xs0 + (kr >= 10 ? kr - 10 : kr) * 128;
#pragma unroll
            for (int q = q0; q < q0 + 2; ++q) glds_x4(base + q * 4096, src + q * (32 * D_ * 2), va[0], va[1], va[2], va[3]);
        };
        auto loadB = [&](int h) {
            const unsigned base = sb0 + K1_BOFF + (h % 5) * K1_BSLOT + dw * (NBP * 1024);
            const int hr = h + 2 * rot;
            const char* s0 = ws0 + (size_t)(hr >= 20 ? hr - 20 : hr) * (32 * LDB * 2);
            glds_x4(base, s0, vb[0], vb[1], vb[2], vb[3]);
            if (NJ == 5) glds_x1(base + 4096, s0, vb[4]);
            else glds_x2(base + 4096, s0, vb[4], vb[5 % NBP]);
        };
        asm volatile("s_waitcnt lgkmcnt(0)" ::: "memory");
        __builtin_amdgcn_s_barrier();
        CBAR();
        ET(1);
        volatile LDS_AS u16* rdy = (volatile LDS_AS u16*)(size_t)(sb0 + K1_FLG + 32 + (lane & 7) * (2 * NPRD) + (uw - 8) * 2);
        loadB(1);
#pragma unroll 1
        for (int kt = 1; kt < 10; ++kt) {
            if (kt >= 3) k1_wait_rel<PSLP>(sb0, lane, 2 * kt - 4);
            loadB(2 * kt); loadA(kt, 0);
            WAIT_VM(2 * NBP + 8);
            *rdy = (u16)(2 * kt - 1);
            loadA(kt, 2);
            WAIT_VM(NBP + 16);
            *rdy = (u16)(2 * kt);
            if (kt >= 2) k1_wait_rel<PSLP>(sb0, lane, 2 * kt - 3);
            loadB(2 * kt + 1);
        }
        WAIT_VM(NBP);
        *rdy = (u16)19;
        WAIT_VM(0);
        *rdy = (u16)30;
        ET(4); ETW();
        return;
    }

    ET(1);
    const int lane = lane_id();
    {
        const unsigned aoffs = aoffs_f(lane);
        wb0 += (size_t)rot * (64 * LDB * 2); wb1 += (size_t)rot * (64 * LDB * 2);
        glds_x4(sb0 + uw * 4096, xa + uw * (32 * D_ * 2) + rot * 128, aoffs, aoffs + (8 * D_ * 2 - 1024), aoffs + 2 * (8 * D_ * 2 - 1024), aoffs + 3 * (8 * D_ * 2 - 1024));
        if (NJ == 5) {
            if (uw < 5) glds_x2(sb0 + K1_BOFF + uw * 2048, wb0, boffs(2 * uw, lane), boffs(2 * uw + 1, lane) - 1024);
        } else {
            if (uw < 6) {
                const unsigned v = boffs(uw, lane);
                glds_x1(sb0 + K1_BOFF + uw * 1024, wb0, v);
                glds_x1(sb0 + K1_BOFF + 6144 + uw * 1024, wb1, v);
            }
        }
    }
    ET(2);
    __builtin_amdgcn_s_barrier();
    CBAR();
    const int wr = uw >> 1, wc = uw & 1;
    const int fr = lane & 15, g = lane >> 4;
    f32x4 acc[4][NJ];
#pragma unroll
    for (int i = 0; i < 4; ++i)
#pragma unroll
        for (int j = 0; j < NJ; ++j) acc[i][j] = (f32x4){0.f, 0.f, 0.f, 0.f};

    unsigned aa[2][2], be, bo;
    {
        const int row = wr * 64 + fr;
        aa[0][0] = sb0 + row * 128 + ((g ^ (row & 7)) << 4);
        aa[1][0] = sb0 + row * 128 + (((4 + g) ^ (row & 7)) << 4);
        aa[0][1] = aa[0][0] + 2 * K1_ABYTES;
        aa[1][1] = aa[1][0] + 2 * K1_ABYTES;
        const int q4 = (lane & 15) >> 2, p4 = lane & 3;
        const unsigned bb = sb0 + K1_BOFF + (8 * g + q4) * ROWB + p4 * 8 + wc * (NJ == 5 ? 5 : 3) * 32;
        const int sx = ((g & 1) << 5) * (wc ? -1 : 1);
        be = bb + sx; bo = bb - sx;
    }
    unsigned frel = sb0 + K1_FLG + uw * 4;
    unsigned frdy = sb0 + K1_FLG + 32 + uw * (2 * NPRD) + (lane & (NPRD - 1)) * 2;
    asm volatile("" : "+v"(aa[0][0]), "+v"(aa[1][0]), "+v"(aa[0][1]), "+v"(aa[1][1]), "+v"(be), "+v"(bo), "+v"(frel), "+v"(frdy));
    WAIT_VM(0);
    ET(3);
    *(lflag_t)(size_t)(frel + K1_T0) = 1u;
    {
        lflag_t p = (lflag_t)(size_t)(sb0 + K1_FLG + K1_T0 + (lane & 7) * 4);
        while (__builtin_amdgcn_ballot_w64(*p == 0u) != 0ull) {}
    }
    CBAR();
    ET(4);
    bf16x8 af[2][4], bq[NJ];
    auto rdA = [&](int h, int i) {
        const int st = (h >> 1) % 3;
        return *(const LDS_AS bf16x8*)(size_t)(aa[h & 1][st == 2] + (st == 1 ? K1_ABYTES : 0) + i * 2048);
    };
    auto rdB = [&](int h, int j) {
        const int jj = (NJ == 5) ? j : j % 3, sub = (NJ == 5) ? 0 : (j / 3) * 6144;
        const unsigned b = ((jj & 1) ? bo : be) + (h % 5) * K1_BSLOT + jj * 32 + sub;
        s16x4 lo = tr_read_l(b);
        s16x4 hi = tr_read_l(b + 4 * ROWB);
        return cat4(lo, hi);
    };
    auto rd_rdy = [&]() { return (unsigned)*(volatile LDS_AS u16*)(size_t)frdy; };
#define WAIT_RDY(fl, need) asm volatile("v_cmp_gt_u32 vcc, %2, %0\n\ts_cbranch_vccz L_ok_%=\n" \
        "L_spin_%=:\n\tds_read_u16 %0, %1\n\ts_waitcnt lgkmcnt(0)\n\tv_cmp_gt_u32 vcc, %2, %0\n\ts_cbranch_vccnz L_spin_%=\n" \
        "L_ok_%=:" : "+v"(fl) : "v"(frdy), "n"(need) : "vcc", "memory")
    auto release = [&](unsigned n) { CBAR(); *(lflag_t)(size_t)frel = n; CBAR(); };
#pragma unroll
    for (int i = 0; i < 4; ++i) af[0][i] = rdA(0, i);
    __builtin_amdgcn_sched_barrier(0);
#pragma unroll
    for (int j = 0; j < NJ; ++j) { bq[j] = rdB(0, j); __builtin_amdgcn_sched_barrier(0); }
    release(1u);
    unsigned fl = rd_rdy();
    __builtin_amdgcn_sched_barrier(0);
#pragma unroll
    for (int p = 0; p < 20; ++p) {
#pragma unroll
        for (int sl = 0; sl < 4 * NJ; ++sl) {
            const int j = sl >> 2, i = sl & 3;
            MF1(acc[i][j], bq[j], af[p & 1][i]);
            __builtin_amdgcn_sched_barrier(0);
            if (sl == 0 && p > 0) bq[NJ - 1] = rdB(p, NJ - 1);
            if (sl == 1 && p > 0 && p < (NJ == 5 ? 18 : 16)) release(p + 1);
            if (p < 19) {
                if (sl == 2) WAIT_RDY(fl, p + 2);
                if (sl >= 4 && i == 0) bq[j - 1] = rdB(p + 1, j - 1);
                if (sl == 5) af[(p + 1) & 1][0] = rdA(p + 1, 0);
                if (sl == 6) af[(p + 1) & 1][1] = rdA(p + 1, 1);
                if (sl == 7) af[(p + 1) & 1][2] = rdA(p + 1, 2);
                if (sl == 9) af[(p + 1) & 1][3] = rdA(p + 1, 3);
                if (sl == 4 * NJ - K1FS && p < 18) fl = rd_rdy();
            }
            __builtin_amdgcn_sched_barrier(0);
        }
    }

    k1_epilogue<NJ>(acc, ws, smem, m0, c0, nt, uw ENTPASS);
    ET(5); ETW();
}

#define AUXM 0
#define AUXS 12
#define K1NW 12
template <int NU, int NR, bool EARLY>
__device__ __forceinline__ void k1_aux(const u16* __restrict__ X, const u16* __restrict__ Wo, const u16* __restrict__ Wd,
                                       const u16* __restrict__ gate_w, char* __restrict__ ws, char* wl,
                                       int u0, int us, int ue, int rb, int r0, int rs, int re) {
    const int lane = lane_id();
    float* gout = (float*)(ws + WS_G);
    const int q4 = (lane & 15) >> 2, p4 = lane & 3, g = lane >> 4;
    const rsrc_t rw = mk_rsrc(ws, (unsigned)WS_FLAG);
    auto fetch = [&](int unit, u32x4 (&r)[4]) {
        const int ksg = unit / 10, cc = unit % 10;
#define OPERM 1
        const int e = lane >> 3;
#pragma unroll
        for (int i = 0; i < 4; ++i) {
            const int G = ksg * 4 + i, P = G / 5, gi = G % 5;
            const int ro = (gi < 4) ? (2 * P + (gi >> 1)) * DH_ + (e >> 2) * 8 + 4 * (gi & 1) + (e & 3) : (2 * P + (e >> 2)) * DH_ + 16 + (e & 3);
            const u16* src = (ksg < KS_O) ? Wo + (size_t)ro * D_ : Wd + (size_t)((ksg - KS_O) * 32 + i * 8 + e) * D_;
            r[i] = *(const u32x4*)(src + cc * 64 + (lane & 7) * 8);
        }
    };
    auto emit = [&](int unit, const u32x4 (&r)[4]) {
        const int ksg = unit / 10, cc = unit % 10;
#pragma unroll
        for (int hf = 0; hf < 2; ++hf) {
            CBAR();
            if (((lane >> 2) & 1) == hf) {
#pragma unroll
                for (int i = 0; i < 4; ++i) *(u32x4*)(wl + (i * 8 + (lane >> 3)) * 64 + (lane & 3) * 16) = r[i];
            }
            CBAR();
#pragma unroll
            for (int j = 0; j < 2; ++j) {
                const char* a = wl + (8 * g + q4) * 64 + j * 32 + p4 * 8;
                s16x4 lo = tr_read(a);
                s16x4 hi = tr_read(a + 4 * 64);
                bf16x8 v = cat4(lo, hi);
                const int nt = cc * 4 + hf * 2 + j;
                const int dst = (ksg < KS_O) ? (int)WS_WO + ((nt * KS_O + ksg) * 64 + lane) * 16
                                             : (int)WS_WD + ((nt * KS_H + (ksg - KS_O)) * 64 + lane) * 16;
#define WAUX 0
                bst4<WAUX>(rw, dst, __builtin_bit_cast(u32x4, v));
            }
        }
    };
    u32x4 r[NU ? NU : 1][4];
    u32x4 x0[NR ? NR : 1], x1[NR ? NR : 1], w0, w1;
    auto gfetch = [&]() {
        w0 = *(const u32x4*)(gate_w + lane * 8);
        w1 = *(const u32x4*)(gate_w + 512 + (lane & 15) * 8);
#pragma unroll
        for (int i = 0; i < NR; ++i) {
            int row = r0 + rs * i;
            row = rb + (row < re ? row : r0);
            x0[i] = *(const u32x4*)(X + (size_t)row * D_ + lane * 8);
            x1[i] = *(const u32x4*)(X + (size_t)row * D_ + 512 + (lane & 15) * 8);
        }
    };
    if (NU) {
        const bool extra = u0 + (NU - 1) * us < ue;
#pragma unroll
        for (int u = 0; u < NU - 1; ++u) fetch(u0 + u * us, r[u]);
        if (extra) fetch(u0 + (NU - 1) * us, r[NU - 1]);
        if (EARLY && NR) gfetch();
#pragma unroll
        for (int u = 0; u < NU - 1; ++u) emit(u0 + u * us, r[u]);
        if (extra) emit(u0 + (NU - 1) * us, r[NU - 1]);
    }
    CBAR();
    if (NR == 0) return;
    if (!EARLY || !NU) gfetch();
    if (lane >= 16) w1 = (u32x4){0u, 0u, 0u, 0u};
    float sacc[NR ? NR : 1];
#pragma unroll
    for (int i = 0; i < NR; ++i) {
        float a = 0.f;
#pragma unroll
        for (int e = 0; e < 4; ++e) {
            const unsigned xa = x0[i][e], wa = w0[e], xb = x1[i][e], wb = w1[e];
            a = __builtin_amdgcn_fdot2_f32_bf16(__builtin_bit_cast(bf2_t, xa), __builtin_bit_cast(bf2_t, wa), a, false);
            a = __builtin_amdgcn_fdot2_f32_bf16(__builtin_bit_cast(bf2_t, xb), __builtin_bit_cast(bf2_t, wb), a, false);
        }
        sacc[i] = a;
    }
    {
        float z = 0.f;
#pragma unroll
        for (int i = 0; i < NR; ++i) {
            const float tot = wave_sum(sacc[i]);
            z = (lane == i) ? tot : z;
        }
        const int row = r0 + rs * lane;
        z = rbf(z);
        if (lane < NR && row < re) gout[rb + row] = rbf(1.f / (1.f + __expf(-z)));
    }
}

__global__ __launch_bounds__(K1NW * 64) void k1_gemm(const u16* __restrict__ X, const u16* __restrict__ Wqkv,
                                               const u16* __restrict__ Wg, const u16* __restrict__ Wu,
                                               const u16* __restrict__ Wo, const u16* __restrict__ Wd,
                                               const u16* __restrict__ gate_w, char* __restrict__ ws) {
    __shared__ __attribute__((aligned(16))) char smem[K1_LDS];
    const int bid = blockIdx.x;
    const int xcd = bid & 7, idx = bid >> 3;
    const int ng = xcd >> 1, mg = xcd & 1;
    const int cnt = (ng < 2) ? 8 : 7;
#define NBAL 1
    const int nstart = ((idx >> 2) < 3) ? ng * 3 : ((ng < 2) ? 9 + ng * 5 : 11 + ng * 4);
    const int uw = __builtin_amdgcn_readfirstlane(threadIdx.x >> 6);
    if (idx >= 4 * cnt) {
        const int sbk = (xcd - 4) * 4 + (idx - 28);
        if (threadIdx.x < 32)
            *(u32x4*)(ws + WS_FLAG + (size_t)(sbk * 2 + (threadIdx.x >> 4)) * FLAG_LD + (threadIdx.x & 15) * 16) = (u32x4){0u, 0u, 0u, 0u};
        constexpr int NWV = 16 * K1NW;
        const int gw = sbk * K1NW + uw, wq = (((xcd - 4) >> 1) * 4 + (idx - 28)) * K1NW + uw;
        constexpr int NRS = (1024 + NWV / 2 - 1) / (NWV / 2);
        if (AUXM == 0) k1_aux<(740 + NWV - 1) / NWV, NRS, false>(X, Wo, Wd, gate_w, ws, smem + uw * 2048, gw, NWV, 740, mg * 1024, wq, NWV / 2, 1024);
        if (AUXM == 1) k1_aux<1, 0, false>(X, Wo, Wd, gate_w, ws, smem + uw * 2048, 720 + uw * 16 + sbk, 0, 740, 0, 0, 0, 0);
        if (AUXM == 2) k1_aux<1, NRS, true>(X, Wo, Wd, gate_w, ws, smem + uw * 2048, 720 + uw * 16 + sbk, 0, 740, mg * 1024, wq, NWV / 2, 1024);
        if (AUXM == 3) k1_aux<(740 + NWV - 1) / NWV, 0, false>(X, Wo, Wd, gate_w, ws, smem + uw * 2048, gw, NWV, 740, mg * 1024, wq, NWV / 2, 1024);
        if (AUXM == 4) k1_aux<(260 + NWV - 1) / NWV, 0, false>(X, Wo, Wd, gate_w, ws, smem + uw * 2048, 480 + gw, NWV, 740, mg * 1024, wq, NWV / 2, 1024);
        return;
    }
    const int mt = mg * 4 + (idx & 3);
    const int nt = nstart + (idx >> 2);
#define L2PF 0
#define PFD 0
#define PFK 2
#define PFS 0
    if (uw >= 8 + NPRD) {
        if (AUXM == 0 || uw > 10) return;
        __builtin_amdgcn_s_barrier();
        CBAR();
        k1_wait_rel<4>(lds_addr(smem), lane_id(), AUXS);
        const int lin = mt * 30 + nt;
        if (AUXM == 1) k1_aux<3, 9, true>(X, Wo, Wd, gate_w, ws, smem + K1_AUX, lin, 240, 720, mt * 256, nt, 30, 256);
        if (AUXM == 2) k1_aux<3, 0, true>(X, Wo, Wd, gate_w, ws, smem + K1_AUX, lin, 240, 720, mt * 256, nt, 30, 256);
        if (AUXM == 3) k1_aux<0, 9, true>(X, Wo, Wd, gate_w, ws, smem + K1_AUX, lin, 240, 720, mt * 256, nt, 30, 256);
        if (AUXM == 4) k1_aux<2, 9, true>(X, Wo, Wd, gate_w, ws, smem + K1_AUX, lin, 240, 480, mt * 256, nt, 30, 256);
        return;
    }
    if (nt < 12) k1_tile<5>(X, Wqkv, Wqkv, ws, smem, mt, nt, idx >> 2, cnt, uw ENTPASS);
    else k1_tile<6>(X, Wg, Wu, ws, smem, mt, nt - 12, idx >> 2, cnt, uw ENTPASS);
}

typedef unsigned long long u64;
#define GDELAY 8
#define PF2 0
#define ACQ 0
#define ROT 0
#define ROLL 0
#define GPRIO 0
#define POLLSLP 4
#define APRIO 2
#define ST2(i) do {} while (0)
#define ST2ID() do {} while (0)
__device__ __forceinline__ void st_agent(void* p, u32x2 v) {
    __hip_atomic_store((u64*)p, __builtin_bit_cast(u64, v), __ATOMIC_RELAXED, __HIP_MEMORY_SCOPE_AGENT);
}
template <int AUX> __device__ __forceinline__ bf16x8 bld(rsrc_t r, int voff, int soff) {
    return __builtin_bit_cast(bf16x8, __builtin_amdgcn_raw_buffer_load_b128(r, voff, soff, AUX));
}

__device__ __forceinline__ float fmx(float a, float b) { return __builtin_elementwise_maximum(a, b); }

typedef float f32x16 __attribute__((ext_vector_type(16)));
#define MF32(a, b, c) __builtin_amdgcn_mfma_f32_32x32x16_bf16(a, b, c, 0, 0, 0)
#define MF8(a, b, c) __builtin_amdgcn_mfma_f32_32x32x8bf16_1k(a, b, c, 0, 0, 0)
constexpr int AT_V = 15360, AT_PAD = 30720, AT_ONE = AT_PAD + 64;
constexpr int AT_LDS = AT_ONE + 35 * 320 + 16;
template <bool EDGE>
__device__ __forceinline__ void k2_attn_wave(char* __restrict__ oF, const char* smem, const bf16x8 q1, const s16x4 q2,
                                             const float gg, int h, int qw, int rbase, int lane) {
    const int hi = lane >> 5, ql = lane & 31;
    const float NEG = -__builtin_inff();
    const int ts = 8 - (qw >> 5);
    const char* kb = smem + (rbase + ql) * 40;
    const char* ka = kb + hi * 16;
    const char* vb;
    {
        const int q4 = (lane & 15) >> 2, p4 = lane & 3, db = (lane >> 4) & 1;
        vb = smem + AT_V + (rbase + 4 * hi + q4) * 40 + db * 32 + p4 * 8;
        if (db == 1 && p4 >= 1) vb = smem + AT_ONE + (p4 >> 1) * 8;
    }
    auto rd16 = [&](const char* p) {
        const u32x2 lo = *(const u32x2*)p, up = *(const u32x2*)(p + 8);
        const u32x4 v = {lo[0], lo[1], up[0], up[1]};
        return __builtin_bit_cast(bf16x8, v);
    };
    auto rdK1 = [&](int t) { return rd16(ka + t * 1280); };
    auto rdK2 = [&](int t) { return *(const s16x4*)(kb + t * 1280 + 32); };
    auto rdV = [&](int t, int a) {
        s16x4 lo = tr_read(vb + t * 1280 + a * 640);
        s16x4 up = tr_read(vb + t * 1280 + a * 640 + 320);
        return cat4(lo, up);
    };
    f32x16 zero;
#pragma unroll
    for (int i = 0; i < 16; ++i) zero[i] = 0.f;
#define SB0() __builtin_amdgcn_sched_barrier(0)
    auto TL = [](int u) { return u < 7 ? u + 1 : (u == 7 ? 0 : 8); };
    float mx = NEG, my = NEG;
    f32x16 s[3];
    bf16x8 k1[2], vf[2][2];
    s16x4 k2[2];
    k1[0] = rdK1(TL(0)); k2[0] = rdK2(TL(0)); k1[1] = rdK1(TL(1)); k2[1] = rdK2(TL(1));
    SB0();
#pragma unroll
    for (int u = -1; u < 9; ++u) {
        if (u + 1 <= 8) {
            s[(u + 1) % 3] = MF32(k1[(u + 1) & 1], q1, zero);
            SB0();
            if (u + 3 <= 8) k1[(u + 1) & 1] = rdK1(TL(u + 3));
            else if (u + 3 == 9) k1[(u + 1) & 1] = rdK1(1);
            else k1[(u + 1) & 1] = rdK1(2);
        }
        SB0();
        const int p = u - 1;
        if (p >= 0 && p < 7 && (!EDGE || p + 1 >= ts)) {
            const f32x16 x = s[p % 3];
            mx = fmx(fmx(mx, x[0]), x[1]); my = fmx(fmx(my, x[2]), x[3]);
            mx = fmx(fmx(mx, x[4]), x[5]); my = fmx(fmx(my, x[6]), x[7]);
        }
        SB0();
        if (u >= 0) {
            s[u % 3] = MF8(k2[u & 1], q2, s[u % 3]);
            SB0();
            if (u + 2 <= 8) k2[u & 1] = rdK2(TL(u + 2));
            else if (u + 2 == 9) k2[u & 1] = rdK2(1);
            else k2[u & 1] = rdK2(2);
        }
        SB0();
        if (p >= 0 && p < 7 && (!EDGE || p + 1 >= ts)) {
            const f32x16 x = s[p % 3];
            mx = fmx(fmx(mx, x[8]), x[9]); my = fmx(fmx(my, x[10]), x[11]);
            mx = fmx(fmx(mx, x[12]), x[13]); my = fmx(fmx(my, x[14]), x[15]);
        }
        SB0();
    }
    vf[0][0] = rdV(8, 0); vf[0][1] = rdV(8, 1);
    if (!EDGE) { vf[1][0] = rdV(0, 0); vf[1][1] = rdV(0, 1); }
    const int z = ql - 4 * hi;
    f32x16 mg;
#pragma unroll
    for (int i = 0; i < 16; ++i) {
        const bool c = 8 * (i >> 2) + (i & 3) > z;
        mg[i] = c ? (EDGE ? NEG : s[1][i]) : s[2][i];
    }
#pragma unroll
    for (int i = 0; i < 16; i += 4) { mx = fmx(fmx(mx, mg[i]), mg[i + 1]); my = fmx(fmx(my, mg[i + 2]), mg[i + 3]); }
    mx = fmx(mx, my);
    {
        const unsigned mi = __builtin_bit_cast(unsigned, mx);
        auto r = __builtin_amdgcn_permlane32_swap(mi, mi, false, false);
        mx = fmx(__builtin_bit_cast(float, (unsigned)r[0]), __builtin_bit_cast(float, (unsigned)r[1]));
    }
    const float nm = -mx;
    f32x16 cm;
#pragma unroll
    for (int i = 0; i < 16; ++i) cm[i] = nm;
    SB0();
    f32x16 o = zero;
    s[1] = MF32(k1[1], q1, cm);
    SB0();
    k1[1] = rdK1(3);
    {
        u32x4 pa, pb;
#pragma unroll
        for (int a = 0; a < 2; ++a) {
#pragma unroll
            for (int e = 0; e < 4; ++e) {
                const int i = 8 * a + 2 * e;
                const float x0 = __builtin_amdgcn_exp2f(mg[i] + nm), x1 = __builtin_amdgcn_exp2f(mg[i + 1] + nm);
                const unsigned pk = pack_bf16(x0, x1);
                if (EDGE) pa[e] = pk;
                else {
                    const bool c0 = 8 * (i >> 2) + (i & 3) > z, c1 = 8 * (i >> 2) + (i & 3) + 1 > z;
                    pb[e] = pack_bf16(c0 ? x0 : 0.f, c1 ? x1 : 0.f);
                    pa[e] = pk ^ pb[e];
                }
                if (a == 0 && e == 1) { SB0(); s[1] = MF8(k2[1], q2, s[1]); SB0(); k2[1] = rdK2(3); }
            }
            SB0();
            o = MF32(vf[0][a], __builtin_bit_cast(bf16x8, pa), o);
            SB0();
            if (!EDGE) {
                if (a == 1) { s[0] = MF32(k1[0], q1, cm); SB0(); k1[0] = rdK1(4); }
                o = MF32(vf[1][a], __builtin_bit_cast(bf16x8, pb), o);
                SB0();
            } else if (a == 1) { s[0] = MF32(k1[0], q1, cm); SB0(); k1[0] = rdK1(4); }
            vf[1][a] = rdV(1, a);
            vf[0][a] = rdV(2, a);
            SB0();
        }
    }
    u32x4 pk;
    float xa, xb;
#pragma unroll
    for (int t = 1; t < 8; ++t) {
        const bool on = !EDGE || t >= ts;
        auto EX = [&](int i) { xa = __builtin_amdgcn_exp2f(s[t & 1][i]); xb = __builtin_amdgcn_exp2f(s[t & 1][i + 1]); };
        float ya, yb;
        if (t < 7) { s[(t + 1) & 1] = MF8(k2[(t + 1) & 1], q2, s[(t + 1) & 1]); SB0(); if (t + 3 < 8) k2[(t + 1) & 1] = rdK2(t + 3); }
        SB0();
        if (on) {
            EX(0); SB0();
            ya = xa; yb = xb; EX(2); pk[0] = pack_bf16(ya, yb); SB0();
            ya = xa; yb = xb; EX(4); pk[1] = pack_bf16(ya, yb); SB0();
            ya = xa; yb = xb; EX(6); pk[2] = pack_bf16(ya, yb); SB0();
            ya = xa; yb = xb; EX(8); pk[3] = pack_bf16(ya, yb); SB0();
            ya = xa; yb = xb; EX(10);
            const unsigned n0 = pack_bf16(ya, yb); SB0();
            o = MF32(vf[t & 1][0], __builtin_bit_cast(bf16x8, pk), o);
            SB0();
            if (t + 2 < 8) vf[t & 1][0] = rdV(t + 2, 0);
            u32x4 pn;
            pn[0] = n0;
            ya = xa; yb = xb; EX(12); pn[1] = pack_bf16(ya, yb); SB0();
            ya = xa; yb = xb; EX(14); pn[2] = pack_bf16(ya, yb); SB0();
            if (!EDGE && t + 2 < 8) { s[t & 1] = MF32(k1[t & 1], q1, cm); SB0(); if (t + 4 < 8) k1[t & 1] = rdK1(t + 4); }
            pn[3] = pack_bf16(xa, xb); SB0();
            o = MF32(vf[t & 1][1], __builtin_bit_cast(bf16x8, pn), o);
            SB0();
            if (t + 2 < 8) vf[t & 1][1] = rdV(t + 2, 1);
        } else if (t + 2 < 8) { vf[t & 1][0] = rdV(t + 2, 0); vf[t & 1][1] = rdV(t + 2, 1); }
        SB0();
        if (EDGE && t + 2 < 8) { s[t & 1] = MF32(k1[t & 1], q1, cm); SB0(); if (t + 4 < 8) k1[t & 1] = rdK1(t + 4); }
        SB0();
    }
    float sum;
    {
        const float o8 = o[8];
        const unsigned si = __float_as_uint(o8);
        auto r = __builtin_amdgcn_permlane32_swap(si, si, false, false);
        sum = __builtin_bit_cast(float, (unsigned)r[1]);
    }
    const float inv = gg * __builtin_amdgcn_rcpf(sum);
    const int m = qw + ql;
    {
        const rsrc_t ro = mk_rsrc(oF, (unsigned)(S_ * D_ * 2));
        const int ob = ((m >> 4) * KS_O * 64 + (m & 15)) * 16 + (h >> 1) * 1280;
        const u32x4 a = {pack_bf16(o[0] * inv, o[1] * inv), pack_bf16(o[2] * inv, o[3] * inv), pack_bf16(o[4] * inv, o[5] * inv), pack_bf16(o[6] * inv, o[7] * inv)};
        bst4<AUX_AGENT>(ro, ob + (2 * (h & 1) + hi) * 256, a);
        const u32x2 b = {pack_bf16(o[8] * inv, o[9] * inv), pack_bf16(o[10] * inv, o[11] * inv)};
        if (hi == 0) bst2<AUX_AGENT>(ro, ob + 1024 + (h & 1) * 8, b);
    }
}

__device__ __forceinline__ void k2_attn(char* __restrict__ ws, char* smem, int bid) {
    const int h = (bid & 7) + 8 * ((bid >> 3) & 3);
    const int qb = bid >> 5;
    const int q0 = qb * 128;
    const int t = threadIdx.x, lane = t & 63;
    const int w = __builtin_amdgcn_readfirstlane(t >> 6);
    const char* Qb = ws + WS_Q + (size_t)h * S_ * 40;
    const char* Kb = ws + WS_K + (size_t)h * S_ * 40;
    const char* Vb = ws + WS_V + (size_t)h * S_ * 40;
    char* sK = smem;
    char* sV = smem + AT_V;

    ST2(0); ST2ID();
    if (APRIO) __builtin_amdgcn_s_setprio(APRIO);
#pragma unroll
    for (int i = 0; i < 4; ++i) {
        const int p = i * 256 + t;
        if (i < 3 || w < 3) {
            int off = (q0 - 256) * 40 + p * 16;
            off = off < 0 ? 0 : off;
            glds16(Kb + off, sK + p * 16);
            glds16(Vb + off, sV + p * 16);
        }
    }
    if (t < 4) *(u32x4*)(smem + AT_PAD + t * 16) = (u32x4){0u, 0u, 0u, 0u};
    if (t >= 64 && t < 100) *(u32x4*)(smem + AT_ONE + (t - 64) * 320) = (u32x4){0x3f80u, 0u, 0u, 0u};
    const int hi = lane >> 5;
    const int qw = q0 + 32 * w;
    bf16x8 q1;
    s16x4 q2;
    {
        const char* qp = Qb + (size_t)(qw + (lane & 31)) * 40;
        const u32x2 a = *(const u32x2*)(qp + hi * 16), b = *(const u32x2*)(qp + hi * 16 + 8);
        u32x2 c = *(const u32x2*)(qp + 32);
        if (hi) c = (u32x2){0u, 0u};
        const u32x4 v1 = {a[0], a[1], b[0], b[1]};
        q1 = __builtin_bit_cast(bf16x8, v1); q2 = __builtin_bit_cast(s16x4, c);
    }
    const float gg = ((const float*)(ws + WS_G))[qw + (lane & 31)];
    __syncthreads();
    ST2(1);
    char* oF = ws + WS_O;
    if (qb >= 2) k2_attn_wave<false>(oF, smem, q1, q2, gg, h, qw, 32 * w, lane);
    else k2_attn_wave<true>(oF, smem, q1, q2, gg, h, qw, 32 * w, lane);
    ST2(2);
    asm volatile("s_waitcnt vmcnt(0)" ::: "memory");
    ST2(3);
    if (lane == 0)
        __hip_atomic_store((unsigned*)(ws + WS_FLAG + (qw >> 6) * FLAG_LD) + h * 2 + ((qw >> 5) & 1), 1u, __ATOMIC_RELAXED, __HIP_MEMORY_SCOPE_AGENT);
}

__device__ __forceinline__ void k2_gemm(char* __restrict__ ws, u16* __restrict__ out, char* smem, int bid) {
    const int xcd = bid & 7, idx = bid >> 3;
    const int mt = (xcd >> 1) * 8 + (idx & 7);
    const int nt = (xcd & 1) * 4 + (idx >> 3);
    const int t = threadIdx.x, lane = t & 63;
    const int w = __builtin_amdgcn_readfirstlane(t >> 6);
    const int fr = lane & 15, g = lane >> 4;
    const int nsteps = (w < 3) ? 14 : 12;
    const rsrc_t rs = __builtin_amdgcn_make_buffer_rsrc(ws, 0, (int)WS_FLAG, 0x00020000);
    const int vo = lane * 16;
    int ap[4];
#pragma unroll
#define K2SAME 0
    for (int i = 0; i < 4; ++i) ap[i] = (int)WS_H + ((((K2SAME & 1) ? 0 : (K2SAME & 4) ? (mt & 24) : mt) * 4 + (i ^ w)) * KS_H + 14 * w) * 1024;
    int bp = (int)WS_WD + ((((K2SAME & 2) ? 0 : nt) * 5) * KS_H + 14 * w) * 1024;
#define RTA 0
#define RTB 0
    int cur = ((RTA * (nt & 3) + (RTB * (mt & 7) >> 2)) % nsteps) * 1024;
    auto adv = [&]() { cur += 1024; cur = (cur == nsteps * 1024) ? 0 : cur; };

    f32x4 acc[4][5];
#pragma unroll
    for (int i = 0; i < 4; ++i)
#pragma unroll
        for (int j = 0; j < 5; ++j) acc[i][j] = (f32x4){0.f, 0.f, 0.f, 0.f};
    bf16x8 a0[4], b0[5], a1[4], b1[5];
#define SB() __builtin_amdgcn_sched_barrier(0)
#define MFA(c, b, a) asm volatile("v_mfma_f32_16x16x32_bf16 %0, %1, %2, %0" : "+v"(c) : "v"(b), "v"(a))
    const u32x4 rq = {(unsigned)(size_t)ws, (unsigned)((size_t)ws >> 32) & 0xffffu, (unsigned)WS_FLAG, 0x00020000u};
#define LDN(r, so) asm volatile("buffer_load_dwordx4 %0, %1, %2, %3 offen" : "=v"(r) : "v"(vo), "s"(rq), "s"(so) : "memory")
#define LDR(r, so) asm volatile("buffer_load_dwordx4 %0, %1, %2, %3 offen" : "+v"(r) : "v"(vo), "s"(rq), "s"(so) : "memory")
#define LDC(r, so) asm volatile("buffer_load_dwordx4 %0, %1, %2, %3 offen sc1" : "+v"(r) : "v"(vo), "s"(rq), "s"(so) : "memory")
    auto ld = [&](bf16x8 (&a)[4], bf16x8 (&b)[5]) {
#pragma unroll
        for (int j = 0; j < 4; ++j) LDN(b[j], bp + j * KS_H * 1024 + cur);
#pragma unroll
        for (int i = 0; i < 4; ++i) LDN(a[i], ap[i] + cur);
        LDN(b[4], bp + 4 * KS_H * 1024 + cur);
        adv();
    };
    auto mmx = [&](auto w0, auto w4, bf16x8 (&a)[4], bf16x8 (&b)[5], auto fa, auto fb) {
        constexpr int W0 = decltype(w0)::value, W4 = decltype(w4)::value;
#pragma unroll
        for (int j = 0; j < 5; ++j) {
#pragma unroll
            for (int i = 0; i < 4; ++i) {
                if (j == 0) { if (i == 0) WAIT_VM(W0); if (i == 1) WAIT_VM(W0 - 1); if (i == 2) WAIT_VM(W0 - 2); if (i == 3) WAIT_VM(W0 - 3); }
                if (j == 4 && i == 0 && W4 >= 0) WAIT_VM(W4 >= 0 ? W4 : 0);
                MFA(acc[i][j], b[j], a[i]);
                if (j == 4) fa(a[i], i);
            }
            fb(b[j], j);
        }
    };
#define IC(n) std::integral_constant<int, n>{}
    auto nof = [&](bf16x8&, int) {};
    auto fha = [&](bf16x8& r, int i) { LDR(r, ap[i] + cur); };
    auto fhb = [&](bf16x8& r, int j) { LDR(r, bp + j * KS_H * 1024 + cur); };
    ST2(0); ST2ID();
    if (GPRIO) __builtin_amdgcn_s_setprio(GPRIO);
#pragma unroll 1
    for (int i = 0; i < GDELAY; ++i) __builtin_amdgcn_s_sleep(8);
    float gl;
    asm volatile("global_load_dword %0, %1, %2" : "=v"(gl) : "v"(vo >> 2), "s"(ws + WS_G + mt * 256) : "memory");
    ld(a0, b0);
    ld(a1, b1);
#pragma unroll
    for (int i = 0; i < 4; ++i) asm volatile("s_nop 1" : "+v"(acc[i][0]), "+v"(acc[i][1]), "+v"(acc[i][2]), "+v"(acc[i][3]), "+v"(acc[i][4]));
#pragma unroll 1
    for (int s = 0; s < nsteps - 4; s += 2) {
        mmx(IC(13), IC(13), a0, b0, fha, fhb); adv();
        mmx(IC(13), IC(13), a1, b1, fha, fhb); adv();
    }
    const char* fb_ = ws + WS_FLAG + mt * FLAG_LD + 64 * w;
    const int fv = (vo >> 2) & 60;
    mmx(IC(13), IC(13), a0, b0, fha, fhb); adv();
    unsigned fl;
    asm volatile("global_load_dword %0, %1, %2 sc1" : "=v"(fl) : "v"(fv), "s"(fb_) : "memory");
    mmx(IC(14), IC(14), a1, b1, fha, fhb);
    ST2(1);
    int op[4];
#pragma unroll
    for (int i = 0; i < 4; ++i) op[i] = (int)WS_O + ((mt * 4 + (i ^ w)) * KS_O + 5 * w) * 1024;
    const int wp = (int)WS_WO + ((nt * 5) * KS_O + 5 * w) * 1024;
    int so = 0;
    auto foa = [&](bf16x8& r, int i) { LDC(r, op[i] + so); };
    auto fob = [&](bf16x8& r, int j) { LDR(r, wp + j * KS_O * 1024 + so); };
    mmx(IC(14), IC(14), a0, b0, nof, fob);
    ST2(2);
    asm volatile("s_waitcnt vmcnt(14)\n\tv_cmp_ne_u32 vcc, 1, %0\n\ts_cbranch_vccz L_pok_%=\n"
                 "L_pspin_%=:\n\ts_sleep %3\n\tglobal_load_dword %0, %1, %2 sc1\n\ts_waitcnt vmcnt(0)\n\t"
                 "v_cmp_ne_u32 vcc, 1, %0\n\ts_cbranch_vccnz L_pspin_%=\n"
                 "L_pok_%=:" : "+v"(fl) : "v"(fv), "s"(fb_), "n"(POLLSLP) : "vcc", "memory");
    ST2(3);
#pragma unroll
    for (int i = 0; i < 4; ++i) foa(a0[i], i);
    so = 1024;
    mmx(IC(13), IC(13), a1, b1, foa, fob);
    asm volatile("s_nop 7\n\ts_nop 7" : "+v"(gl) :: "memory");
#pragma unroll
    for (int i = 0; i < 4; ++i) {
        const float sc = rbf(1.f - __builtin_bit_cast(float, __builtin_amdgcn_ds_bpermute((i ^ w) * 64 + fv, __builtin_bit_cast(int, gl))));
        asm volatile("" : "+v"(acc[i][0]), "+v"(acc[i][1]), "+v"(acc[i][2]), "+v"(acc[i][3]), "+v"(acc[i][4]));
#pragma unroll
        for (int j = 0; j < 5; ++j) acc[i][j] = acc[i][j] * sc;
        asm volatile("" : "+v"(acc[i][0]), "+v"(acc[i][1]), "+v"(acc[i][2]), "+v"(acc[i][3]), "+v"(acc[i][4]));
    }
    asm volatile("s_nop 3" ::: "memory");
    so = 2048; mmx(IC(12), IC(-1), a0, b0, foa, fob);
    so = 3072; mmx(IC(13), IC(13), a1, b1, foa, fob);
    so = 4096; mmx(IC(13), IC(13), a0, b0, foa, fob);
    mmx(IC(13), IC(9), a1, b1, nof, nof);
    mmx(IC(4), IC(0), a0, b0, nof, nof);
    asm volatile("s_nop 7\n\ts_nop 7" ::: "memory");
#pragma unroll
    for (int i = 0; i < 4; ++i) asm volatile("" : "+v"(acc[i][0]), "+v"(acc[i][1]), "+v"(acc[i][2]), "+v"(acc[i][3]), "+v"(acc[i][4]));
    ST2(4);
    const int ln = lane_id();
    char* mine = smem + w * 10240 + ln * 16;
    char* peer = smem + (w ^ 2) * 10240 + ln * 16;
#pragma unroll
    for (int i = 0; i < 2; ++i)
#pragma unroll
        for (int j = 0; j < 5; ++j) *(f32x4*)(mine + (i * 5 + j) * 1024) = acc[2 + i][j];
    __syncthreads();
#pragma unroll
    for (int j = 0; j < 5; ++j) acc[1][j] += *(const f32x4*)(peer + (5 + j) * 1024);
#pragma unroll
    for (int j = 0; j < 5; ++j) *(f32x4*)(peer + (5 + j) * 1024) = acc[1][j];
#pragma unroll
    for (int j = 0; j < 5; ++j) acc[0][j] += *(const f32x4*)(peer + j * 1024);
    __syncthreads();
    {
        const char* q = smem + (w ^ 3) * 10240 + ln * 16 + 5 * 1024;
        u16* dst = out + (size_t)(mt * 64 + w * 16 + (ln & 15)) * D_ + nt * 80 + (ln >> 4) * 4;
#pragma unroll
        for (int j = 0; j < 5; ++j) {
            const f32x4 v = acc[0][j] + *(const f32x4*)(q + j * 1024);
            u32x2 pk = {pack_bf16(v[0], v[1]), pack_bf16(v[2], v[3])};
            *(u32x2*)(dst + j * 16) = pk;
        }
    }
    ST2(5);
}

__global__ __launch_bounds__(256, 3) void k2_mix(char* __restrict__ ws, u16* __restrict__ out) {
    __shared__ __attribute__((aligned(16))) char smem[AT_LDS > 40960 ? AT_LDS : 40960];
    if (blockIdx.x < 256) k2_gemm(ws, out, smem, blockIdx.x);
    else k2_attn(ws, smem, blockIdx.x - 256);
}

extern "C" void kernel_launch(void* const* d_in, const int* in_sizes, int n_in,
                              void* d_out, int out_size, void* d_ws, size_t ws_size,
                              hipStream_t stream) {
    const u16* X = (const u16*)d_in[0];
    const u16* Wqkv = (const u16*)d_in[1];
    const u16* Wo = (const u16*)d_in[2];
    const u16* Wg = (const u16*)d_in[4];
    const u16* Wu = (const u16*)d_in[5];
    const u16* Wd = (const u16*)d_in[6];
    const u16* gw = (const u16*)d_in[7];
    char* ws = (char*)d_ws;
    k1_gemm<<<256, K1NW * 64, 0, stream>>>(X, Wqkv, Wg, Wu, Wo, Wd, gw, ws);
    k2_mix<<<768, 256, 0, stream>>>(ws, (u16*)d_out);
}
```

```cpp
#include <hip/hip_runtime.h>
#include <hip/hip_bf16.h>
#include <stdint.h>
#include <type_traits>

typedef unsigned short u16;
typedef short bf16x8 __attribute__((ext_vector_type(8)));
typedef short s16x4 __attribute__((ext_vector_type(4)));
typedef float f32x4 __attribute__((ext_vector_type(4)));
typedef float f32x2 __attribute__((ext_vector_type(2)));
typedef unsigned u32x4 __attribute__((ext_vector_type(4)));
typedef unsigned u32x2 __attribute__((ext_vector_type(2)));
typedef __bf16 bf2_t __attribute__((ext_vector_type(2)));

#define LDS_AS __attribute__((address_space(3)))

constexpr int S_ = 2048, D_ = 640, F_ = 1728, H_ = 32, DH_ = 20, NQKV = 1920;
constexpr int KS_O = D_ / 32;
constexpr int KS_H = F_ / 32;

constexpr size_t WS_Q = 0;
constexpr size_t QKV_BYTES = (size_t)H_ * S_ * DH_ * 2;
constexpr size_t WS_K = WS_Q + QKV_BYTES;
constexpr size_t WS_V = WS_K + QKV_BYTES;
constexpr size_t WS_H = WS_V + QKV_BYTES + 4096;
constexpr size_t WS_O = WS_H + (size_t)S_ * F_ * 2;
constexpr size_t WS_WO = WS_O + (size_t)S_ * D_ * 2;
constexpr size_t WS_WD = WS_WO + (size_t)D_ * D_ * 2;
constexpr size_t WS_G = WS_WD + (size_t)F_ * D_ * 2;
constexpr size_t WS_FLAG = WS_G + (size_t)S_ * 4;
constexpr int FLAG_LD = 4352;

__device__ __forceinline__ unsigned pack_bf16(float a, float b) {
    bf2_t v = {(__bf16)a, (__bf16)b};
    return __builtin_bit_cast(unsigned, v);
}
__device__ __forceinline__ float rbf(float x) { return (float)(__bf16)x; }
__device__ __forceinline__ float bf2f(u16 v) { return __uint_as_float(((unsigned)v) << 16); }

template <int CTRL> __device__ __forceinline__ float dpp_add(float x) {
    return x + __builtin_bit_cast(float, __builtin_amdgcn_update_dpp(0, __builtin_bit_cast(int, x), CTRL, 0xf, 0xf, false));
}
__device__ __forceinline__ float wave_sum(float x) {
    x = dpp_add<0xB1>(x); x = dpp_add<0x4E>(x); x = dpp_add<0x141>(x); x = dpp_add<0x140>(x);
    const int xi = __builtin_bit_cast(int, x);
    return (__builtin_bit_cast(float, __builtin_amdgcn_readlane(xi, 0)) + __builtin_bit_cast(float, __builtin_amdgcn_readlane(xi, 16))) +
           (__builtin_bit_cast(float, __builtin_amdgcn_readlane(xi, 32)) + __builtin_bit_cast(float, __builtin_amdgcn_readlane(xi, 48)));
}
__device__ __forceinline__ s16x4 tr_read(const char* lds_ptr) {
    return __builtin_amdgcn_ds_read_tr16_b64_v4i16((LDS_AS s16x4*)(lds_ptr));
}
__device__ __forceinline__ bf16x8 cat4(s16x4 a, s16x4 b) {
    bf16x8 r;
    r[0] = a[0]; r[1] = a[1]; r[2] = a[2]; r[3] = a[3];
    r[4] = b[0]; r[5] = b[1]; r[6] = b[2]; r[7] = b[3];
    return r;
}
__device__ __forceinline__ void glds16(const void* g, void* l) {
    __builtin_amdgcn_global_load_lds((const unsigned*)g, (LDS_AS unsigned*)l, 16, 0, 0);
}
__device__ __forceinline__ void glds16_asm(const void* gsrc, unsigned lds_dst) {
    unsigned keep;
    asm volatile("s_mov_b32 %0, m0\n\ts_mov_b32 m0, %2\n\ts_nop 0\n\tglobal_load_lds_dwordx4 %1, off\n\ts_mov_b32 m0, %0"
                 : "=&s"(keep) : "v"(gsrc), "s"(lds_dst) : "memory");
}
__device__ __forceinline__ void glds4_asm(const void* gsrc, unsigned lds_dst) {
    unsigned keep;
    asm volatile("s_mov_b32 %0, m0\n\ts_mov_b32 m0, %2\n\ts_nop 0\n\tglobal_load_lds_dword %1, off\n\ts_mov_b32 m0, %0"
                 : "=&s"(keep) : "v"(gsrc), "s"(lds_dst) : "memory");
}
__device__ __forceinline__ unsigned lds_addr(const void* p) { return (unsigned)(size_t)(LDS_AS const char*)p; }
typedef __amdgpu_buffer_rsrc_t rsrc_t;
constexpr int AUX_AGENT = 16;
__device__ __forceinline__ rsrc_t mk_rsrc(const void* p, unsigned bytes) { return __builtin_amdgcn_make_buffer_rsrc((void*)p, 0, (int)bytes, 0x00020000); }
template <int AUX> __device__ __forceinline__ void bst4(rsrc_t r, int voff, u32x4 v) { __builtin_amdgcn_raw_buffer_store_b128(v, r, voff, 0, AUX); }
template <int AUX> __device__ __forceinline__ void bst2(rsrc_t r, int voff, u32x2 v) { __builtin_amdgcn_raw_buffer_store_b64(v, r, voff, 0, AUX); }
#define WAIT_VM(n) asm volatile("s_waitcnt vmcnt(%0)" :: "n"(n) : "memory")
#define CBAR() asm volatile("" ::: "memory")
#define STAMP(kid, i) do {} while (0)
#define STAMPK(kid, i) do { if ((i) == 3) STAMP(kid, i); } while (0)

#define ET(i) do {} while (0)
#define ETW() do {} while (0)
#define ENTARG
#define ENTPASS
#define WPG 64
#define OSC 0
#define ESC
#define HAUX 16
#define QAUX 16
#define WSC1
#define K1SB 0
#define K1P0 0
#define K1UNR 1
constexpr int K1_ABYTES = 256 * 64 * 2;
constexpr int K1_BSLOT = 32 * 192 * 2;
constexpr int K1_BOFF = 3 * K1_ABYTES;
constexpr int K1_AUX = K1_BOFF + 5 * K1_BSLOT;
constexpr int K1_FLG = K1_AUX + 2048;
#define PSLP 0
#define PSCH 0
#define BDEP 2
#define EARLYLD 0
constexpr int NPRD = (PSCH == 6) ? 4 : 2;
constexpr int K1_T0 = 32 + 16 * NPRD;
constexpr int K1_LDS = K1_FLG + K1_T0 + 32;
#define K1FJ 2
#define K1FS 6
#define PFL 4
#define MF1(c, b, a) c = __builtin_amdgcn_mfma_f32_16x16x32_bf16(b, a, c, 0, 0, 0)
typedef volatile LDS_AS unsigned* lflag_t;
template <int SLP> __device__ __forceinline__ void k1_wait_rel(unsigned sb0, int lane, unsigned n) {
    lflag_t p = (lflag_t)(size_t)(sb0 + K1_FLG + (lane & 7) * 4);
    while (__builtin_amdgcn_ballot_w64(*p < n) != 0ull) { if (SLP) __builtin_amdgcn_s_sleep(SLP); }
    CBAR();
}
__device__ __forceinline__ void glds_x4(unsigned lds, const void* sb, unsigned v0, unsigned v1, unsigned v2, unsigned v3) {
    unsigned keep;
    asm volatile("s_mov_b32 %0, m0\n\ts_mov_b32 m0, %1\n\ts_nop 0\n\t"
                 "global_load_lds_dwordx4 %3, %2\n\t"
                 "global_load_lds_dwordx4 %4, %2 offset:1024\n\t"
                 "global_load_lds_dwordx4 %5, %2 offset:2048\n\t"
                 "global_load_lds_dwordx4 %6, %2 offset:3072\n\t"
                 "s_mov_b32 m0, %0"
                 : "=&s"(keep) : "s"(lds), "s"(sb), "v"(v0), "v"(v1), "v"(v2), "v"(v3) : "memory");
}
__device__ __forceinline__ void glds_x2(unsigned lds, const void* sb, unsigned v0, unsigned v1) {
    unsigned keep;
    asm volatile("s_mov_b32 %0, m0\n\ts_mov_b32 m0, %1\n\ts_nop 0\n\t"
                 "global_load_lds_dwordx4 %3, %2\n\t"
                 "global_load_lds_dwordx4 %4, %2 offset:1024\n\t"
                 "s_mov_b32 m0, %0"
                 : "=&s"(keep) : "s"(lds), "s"(sb), "v"(v0), "v"(v1) : "memory");
}
__device__ __forceinline__ void glds_x1(unsigned lds, const void* sb, unsigned v0) {
    unsigned keep;
    asm volatile("s_mov_b32 %0, m0\n\ts_mov_b32 m0, %1\n\ts_nop 0\n\t"
                 "global_load_lds_dwordx4 %3, %2\n\t"
                 "s_mov_b32 m0, %0"
                 : "=&s"(keep) : "s"(lds), "s"(sb), "v"(v0) : "memory");
}
__device__ __forceinline__ int lane_id() {
    int l;
    asm volatile("v_mbcnt_lo_u32_b32 %0, -1, 0\n\tv_mbcnt_hi_u32_b32 %0, -1, %0" : "=v"(l));
    return l;
}
__device__ __forceinline__ s16x4 tr_read_l(unsigned a) { return __builtin_amdgcn_ds_read_tr16_b64_v4i16((LDS_AS s16x4*)(size_t)a); }

template <int NJ>
__device__ __forceinline__ void k1_epilogue(f32x4 (&acc)[4][NJ], char* __restrict__ ws, char* smem, int m0, int c0, int nt, int wid ENTARG) {
    const int lane = lane_id();
    const int t = wid * 64 + lane;
    const int wr = wid >> 1, wc = wid & 1;
    const int fr = lane & 15, g = lane >> 4;
    const rsrc_t rw = mk_rsrc(ws, (unsigned)WS_FLAG);
#define EPI5 1
    if (NJ == 5 && EPI5) {
        const unsigned sb0 = lds_addr(smem);
        k1_wait_rel<0>(sb0, lane, 18u);
        char* img = smem + K1_ABYTES + wid * 10240;
        if (nt < 4) {
            constexpr float qs = 0.22360679774997896f * 1.4426950408889634f;
#pragma unroll
            for (int j = 0; j < NJ; ++j)
#pragma unroll
                for (int i = 0; i < 4; ++i) {
                    f32x2 lo = {acc[i][j][0], acc[i][j][1]}, hi = {acc[i][j][2], acc[i][j][3]};
                    lo = lo * qs; hi = hi * qs;
                    acc[i][j] = (f32x4){lo[0], lo[1], hi[0], hi[1]};
                }
        }
#pragma unroll
        for (int j = 0; j < NJ; ++j) {
            const unsigned u = 4 * j + g;
            char* dst = img + fr * 40 + u * 8 + ((u * 13u) >> 6) * 2520u;
#pragma unroll
            for (int i = 0; i < 4; ++i) {
                u32x2 a = {pack_bf16(acc[i][j][0], acc[i][j][1]), pack_bf16(acc[i][j][2], acc[i][j][3])};
                *(u32x2*)(dst + i * 640) = a;
            }
        }
        CBAR();
        u32x4 v[10];
#pragma unroll
        for (int it = 0; it < 10; ++it) v[it] = *(const u32x4*)(img + it * 1024 + lane * 16);
        const int which = nt >> 2;
        const int dofs = (int)(WS_Q + (size_t)which * QKV_BYTES) + (((nt & 3) * 8 + wc * 4) * S_ + m0 + wr * 64) * 40;
        constexpr int HS = S_ * 40 - 2560;
        const int v0 = lane * 16, v1 = v0 + (lane >= 32 ? HS : 0);
#pragma unroll
        for (int it = 0; it < 10; ++it) {
            const int hh = (it / 5) * 2 + ((it % 5) >= 3 ? 1 : 0);
            __builtin_amdgcn_raw_buffer_store_b128(v[it], rw, (it % 5) == 2 ? v1 : v0, dofs + it * 1024 + hh * HS, QAUX);
        }
    } else if (NJ == 5) {
        const unsigned sb0 = lds_addr(smem);
        CBAR(); *(lflag_t)(size_t)(sb0 + K1_FLG + wid * 4) = 21u; CBAR();
        k1_wait_rel<0>(sb0, lane, 21u);
        const float qs = (nt < 4) ? 0.22360679774997896f * 1.4426950408889634f : 1.f;
#pragma unroll
        for (int j = 0; j < NJ; ++j) {
            const int n = wc * 80 + j * 16 + g * 4;
            const int hh = n / DH_, d = n % DH_;
            char* dst = smem + hh * 10240 + (wr * 64 + fr) * 40 + d * 2;
#pragma unroll
            for (int i = 0; i < 4; ++i) {
                u32x2 a = {pack_bf16(acc[i][j][0] * qs, acc[i][j][1] * qs), pack_bf16(acc[i][j][2] * qs, acc[i][j][3] * qs)};
                *(u32x2*)(dst + i * 640) = a;
            }
        }
        CBAR(); *(lflag_t)(size_t)(sb0 + K1_FLG + wid * 4) = 22u; CBAR();
        k1_wait_rel<0>(sb0, lane, 22u);
        const int which = nt >> 2;
        const int h0 = (nt & 3) * 8;
        char* dbase = ws + WS_Q + (size_t)which * QKV_BYTES + ((size_t)h0 * S_ + m0) * 40;
        const int dofs = (int)(WS_Q + (size_t)which * QKV_BYTES + ((size_t)h0 * S_ + m0) * 40);
#pragma unroll
        for (int it = 0; it < 10; ++it) {
            const int e = it * 512 + t;
            const int hh = e / 640, r = e % 640;
            const u32x4 v = *(const u32x4*)(smem + e * 16);
            bst4<QAUX>(rw, dofs + hh * (S_ * 40) + r * 16, v);
        }
    } else {
        u16* hF = (u16*)(ws + WS_H);
#pragma unroll
        for (int j = 0; j < 3; ++j) {
            const int f = c0 + wc * 48 + j * 16 + g * 4;
            const int ks = f >> 5;
            const int lq = (f & 31) >> 3;
            const int hf = f & 4;
#pragma unroll
            for (int i = 0; i < 4; ++i) {
                const int m = m0 + wr * 64 + i * 16 + fr;
                unsigned pk[2];
#pragma unroll
                for (int r = 0; r < 2; ++r) {
                    const f32x2 gt = {acc[i][j][2 * r], acc[i][j][2 * r + 1]};
                    const f32x2 up = {acc[i][(j + 3) % NJ][2 * r], acc[i][(j + 3) % NJ][2 * r + 1]};
                    f32x2 e = gt * -1.4426950408889634f;
                    e = (f32x2){__builtin_amdgcn_exp2f(e[0]), __builtin_amdgcn_exp2f(e[1])};
                    e = e + 1.f;
                    const f32x2 rc = {__builtin_amdgcn_rcpf(e[0]), __builtin_amdgcn_rcpf(e[1])};
                    const f32x2 hv = (gt * up) * rc;
                    pk[r] = pack_bf16(hv[0], hv[1]);
                }
                u32x2 a = {pk[0], pk[1]};
                u16* dst = hF + ((size_t)((m >> 4) * KS_H + ks) * 64 + (m & 15) + 16 * lq) * 8 + hf;
                bst2<HAUX>(rw, (int)WS_H + ((((m >> 4) * KS_H + ks) * 64 + (m & 15) + 16 * lq) * 8 + hf) * 2, a);
            }
        }
    }
}

template <int NJ>
__device__ __forceinline__ void k1_tile(const u16* __restrict__ X, const u16* __restrict__ W0, const u16* __restrict__ W1,
                                        char* __restrict__ ws, char* smem, int mt, int nt, int xs, int xcnt, int uw ENTARG) {
    constexpr int BN = 32 * NJ;
    constexpr int ROWB = (NJ == 5) ? 320 : 192;
    constexpr int UPR = ROWB / 16;
    constexpr int NBH = UPR / 2;
    constexpr int NB = 2 * NJ;
    constexpr int LDB = (NJ == 5) ? NQKV : F_;
    const int m0 = mt * 256;
    const int c0 = (NJ == 5) ? nt * 160 : nt * 96;
    const unsigned sb0 = lds_addr(smem);
    auto boffs = [&](int i, int lane) {
        const int p = i * 64 + lane;
        const int k = p / UPR, u = p % UPR;
        return (unsigned)((k * LDB + (((u >> 1) ^ ((k >> 3) & 1)) << 4) + (u & 1) * 8) * 2);
    };
    auto aoffs_f = [&](int lane) { return (unsigned)((lane >> 3) * (D_ * 2) + (((lane & 7) ^ (lane >> 3)) << 4)); };
    const char* xa = (const char*)(X + (size_t)m0 * D_);
    const char* wb0 = (const char*)(W0 + c0);
    const char* wb1 = (const char*)(W1 + c0);

#define KRA 0
#define KRB 0
    const int rot = (xs * KRA + ((mt & 3) * KRB >> 1)) % 10;
    if (uw >= 8) {
        const int dw = uw & 1;
        const int lane = lane_id();
        const unsigned aoffs = aoffs_f(lane);
        if (uw == 8 && lane < (K1_T0 + 32) / 4) *(lflag_t)(size_t)(sb0 + K1_FLG + lane * 4) = 0u;
        constexpr int NBP = NJ;
        unsigned vb[NBP], va[4];
#pragma unroll
        for (int i = 0; i < NBP; ++i) vb[i] = boffs((NJ == 5) ? 5 * dw + i : i, lane) - (i & 3) * 1024;
#pragma unroll
        for (int r = 0; r < 4; ++r) va[r] = aoffs + r * (8 * D_ * 2 - 1024);
        const char* xs0 = xa + dw * (128 * D_ * 2);
        const char* ws0 = (NJ == 6 && dw) ? wb1 : wb0;
        auto loadA = [&](int kt, int q0) {
            const unsigned base = sb0 + (kt % 3) * K1_ABYTES + dw * 16384;
            const int kr = kt + rot;
            const char* src = xs0 + (kr >= 10 ? kr - 10 : kr) * 128;
#pragma unroll
            for (int q = q0; q < q0 + 2; ++q) glds_x4(base + q * 4096, src + q * (32 * D_ * 2), va[0], va[1], va[2], va[3]);
        };
        auto loadB = [&](int h) {
            const unsigned base = sb0 + K1_BOFF + (h % 5) * K1_BSLOT + dw * (NBP * 1024);
            const int hr = h + 2 * rot;
            const char* s0 = ws0 + (size_t)(hr >= 20 ? hr - 20 : hr) * (32 * LDB * 2);
            glds_x4(base, s0, vb[0], vb[1], vb[2], vb[3]);
            if (NJ == 5) glds_x1(base + 4096, s0, vb[4]);
            else glds_x2(base + 4096, s0, vb[4], vb[5 % NBP]);
        };
        asm volatile("s_waitcnt lgkmcnt(0)" ::: "memory");
        __builtin_amdgcn_s_barrier();
        CBAR();
        ET(1);
        volatile LDS_AS u16* rdy = (volatile LDS_AS u16*)(size_t)(sb0 + K1_FLG + 32 + (lane & 7) * (2 * NPRD) + (uw - 8) * 2);
        loadB(1);
#pragma unroll 1
        for (int kt = 1; kt < 10; ++kt) {
            if (kt >= 3) k1_wait_rel<PSLP>(sb0, lane, 2 * kt - 4);
            loadB(2 * kt); loadA(kt, 0);
            WAIT_VM(2 * NBP + 8);
            *rdy = (u16)(2 * kt - 1);
            loadA(kt, 2);
            WAIT_VM(NBP + 16);
            *rdy = (u16)(2 * kt);
            if (kt >= 2) k1_wait_rel<PSLP>(sb0, lane, 2 * kt - 3);
            loadB(2 * kt + 1);
        }
        WAIT_VM(NBP);
        *rdy = (u16)19;
        WAIT_VM(0);
        *rdy = (u16)30;
        ET(4); ETW();
        return;
    }

    ET(1);
    const int lane = lane_id();
    {
        const unsigned aoffs = aoffs_f(lane);
        wb0 += (size_t)rot * (64 * LDB * 2); wb1 += (size_t)rot * (64 * LDB * 2);
        glds_x4(sb0 + uw * 4096, xa + uw * (32 * D_ * 2) + rot * 128, aoffs, aoffs + (8 * D_ * 2 - 1024), aoffs + 2 * (8 * D_ * 2 - 1024), aoffs + 3 * (8 * D_ * 2 - 1024));
        if (NJ == 5) {
            if (uw < 5) glds_x2(sb0 + K1_BOFF + uw * 2048, wb0, boffs(2 * uw, lane), boffs(2 * uw + 1, lane) - 1024);
        } else {
            if (uw < 6) {
                const unsigned v = boffs(uw, lane);
                glds_x1(sb0 + K1_BOFF + uw * 1024, wb0, v);
                glds_x1(sb0 + K1_BOFF + 6144 + uw * 1024, wb1, v);
            }
        }
    }
    ET(2);
    __builtin_amdgcn_s_barrier();
    CBAR();
    const int wr = uw >> 1, wc = uw & 1;
    const int fr = lane & 15, g = lane >> 4;
    f32x4 acc[4][NJ];
#pragma unroll
    for (int i = 0; i < 4; ++i)
#pragma unroll
        for (int j = 0; j < NJ; ++j) acc[i][j] = (f32x4){0.f, 0.f, 0.f, 0.f};

    unsigned aa[2][2], be, bo;
    {
        const int row = wr * 64 + fr;
        aa[0][0] = sb0 + row * 128 + ((g ^ (row & 7)) << 4);
        aa[1][0] = sb0 + row * 128 + (((4 + g) ^ (row & 7)) << 4);
        aa[0][1] = aa[0][0] + 2 * K1_ABYTES;
        aa[1][1] = aa[1][0] + 2 * K1_ABYTES;
        const int q4 = (lane & 15) >> 2, p4 = lane & 3;
        const unsigned bb = sb0 + K1_BOFF + (8 * g + q4) * ROWB + p4 * 8 + wc * (NJ == 5 ? 5 : 3) * 32;
        const int sx = ((g & 1) << 5) * (wc ? -1 : 1);
        be = bb + sx; bo = bb - sx;
    }
    unsigned frel = sb0 + K1_FLG + uw * 4;
    unsigned frdy = sb0 + K1_FLG + 32 + uw * (2 * NPRD) + (lane & (NPRD - 1)) * 2;
    asm volatile("" : "+v"(aa[0][0]), "+v"(aa[1][0]), "+v"(aa[0][1]), "+v"(aa[1][1]), "+v"(be), "+v"(bo), "+v"(frel), "+v"(frdy));
    WAIT_VM(0);
    ET(3);
    *(lflag_t)(size_t)(frel + K1_T0) = 1u;
    {
        lflag_t p = (lflag_t)(size_t)(sb0 + K1_FLG + K1_T0 + (lane & 7) * 4);
        while (__builtin_amdgcn_ballot_w64(*p == 0u) != 0ull) {}
    }
    CBAR();
    ET(4);
    bf16x8 af[2][4], bq[NJ];
    auto rdA = [&](int h, int i) {
        const int st = (h >> 1) % 3;
        return *(const LDS_AS bf16x8*)(size_t)(aa[h & 1][st == 2] + (st == 1 ? K1_ABYTES : 0) + i * 2048);
    };
    auto rdB = [&](int h, int j) {
        const int jj = (NJ == 5) ? j : j % 3, sub = (NJ == 5) ? 0 : (j / 3) * 6144;
        const unsigned b = ((jj & 1) ? bo : be) + (h % 5) * K1_BSLOT + jj * 32 + sub;
        s16x4 lo = tr_read_l(b);
        s16x4 hi = tr_read_l(b + 4 * ROWB);
        return cat4(lo, hi);
    };
    auto rd_rdy = [&]() { return (unsigned)*(volatile LDS_AS u16*)(size_t)frdy; };
#define WAIT_RDY(fl, need) asm volatile("v_cmp_gt_u32 vcc, %2, %0\n\ts_cbranch_vccz L_ok_%=\n" \
        "L_spin_%=:\n\tds_read_u16 %0, %1\n\ts_waitcnt lgkmcnt(0)\n\tv_cmp_gt_u32 vcc, %2, %0\n\ts_cbranch_vccnz L_spin_%=\n" \
        "L_ok_%=:" : "+v"(fl) : "v"(frdy), "n"(need) : "vcc", "memory")
    auto release = [&](unsigned n) { CBAR(); *(lflag_t)(size_t)frel = n; CBAR(); };
#pragma unroll
    for (int i = 0; i < 4; ++i) af[0][i] = rdA(0, i);
    __builtin_amdgcn_sched_barrier(0);
#pragma unroll
    for (int j = 0; j < NJ; ++j) { bq[j] = rdB(0, j); __builtin_amdgcn_sched_barrier(0); }
    release(1u);
    unsigned fl = rd_rdy();
    __builtin_amdgcn_sched_barrier(0);
#pragma unroll
    for (int p = 0; p < 20; ++p) {
#pragma unroll
        for (int sl = 0; sl < 4 * NJ; ++sl) {
            const int j = sl >> 2, i = sl & 3;
            MF1(acc[i][j], bq[j], af[p & 1][i]);
            __builtin_amdgcn_sched_barrier(0);
            if (sl == 0 && p > 0) bq[NJ - 1] = rdB(p, NJ - 1);
            if (sl == 1 && p > 0 && p < (NJ == 5 ? 18 : 16)) release(p + 1);
            if (p < 19) {
                if (sl == 2) WAIT_RDY(fl, p + 2);
                if (sl >= 4 && i == 0) bq[j - 1] = rdB(p + 1, j - 1);
                if (sl == 5) af[(p + 1) & 1][0] = rdA(p + 1, 0);
                if (sl == 6) af[(p + 1) & 1][1] = rdA(p + 1, 1);
                if (sl == 7) af[(p + 1) & 1][2] = rdA(p + 1, 2);
                if (sl == 9) af[(p + 1) & 1][3] = rdA(p + 1, 3);
                if (sl == 4 * NJ - K1FS && p < 18) fl = rd_rdy();
            }
            __builtin_amdgcn_sched_barrier(0);
        }
    }

    k1_epilogue<NJ>(acc, ws, smem, m0, c0, nt, uw ENTPASS);
    ET(5); ETW();
}

#define AUXM 0
#define AUXS 12
#define K1NW 12
template <int NU, int NR, bool EARLY>
__device__ __forceinline__ void k1_aux(const u16* __restrict__ X, const u16* __restrict__ Wo, const u16* __restrict__ Wd,
                                       const u16* __restrict__ gate_w, char* __restrict__ ws, char* wl,
                                       int u0, int us, int ue, int rb, int r0, int rs, int re) {
    const int lane = lane_id();
    float* gout = (float*)(ws + WS_G);
    const int q4 = (lane & 15) >> 2, p4 = lane & 3, g = lane >> 4;
    const rsrc_t rw = mk_rsrc(ws, (unsigned)WS_FLAG);
    auto fetch = [&](int unit, u32x4 (&r)[4]) {
        const int ksg = unit / 10, cc = unit % 10;
#define OPERM 1
        const int e = lane >> 3;
#pragma unroll
        for (int i = 0; i < 4; ++i) {
            const int G = ksg * 4 + i, P = G / 5, gi = G % 5;
            const int ro = (gi < 4) ? (2 * P + (gi >> 1)) * DH_ + (e >> 2) * 8 + 4 * (gi & 1) + (e & 3) : (2 * P + (e >> 2)) * DH_ + 16 + (e & 3);
            const u16* src = (ksg < KS_O) ? Wo + (size_t)ro * D_ : Wd + (size_t)((ksg - KS_O) * 32 + i * 8 + e) * D_;
            r[i] = *(const u32x4*)(src + cc * 64 + (lane & 7) * 8);
        }
    };
    auto emit = [&](int unit, const u32x4 (&r)[4]) {
        const int ksg = unit / 10, cc = unit % 10;
#pragma unroll
        for (int hf = 0; hf < 2; ++hf) {
            CBAR();
            if (((lane >> 2) & 1) == hf) {
#pragma unroll
                for (int i = 0; i < 4; ++i) *(u32x4*)(wl + (i * 8 + (lane >> 3)) * 64 + (lane & 3) * 16) = r[i];
            }
            CBAR();
#pragma unroll
            for (int j = 0; j < 2; ++j) {
                const char* a = wl + (8 * g + q4) * 64 + j * 32 + p4 * 8;
                s16x4 lo = tr_read(a);
                s16x4 hi = tr_read(a + 4 * 64);
                bf16x8 v = cat4(lo, hi);
                const int nt = cc * 4 + hf * 2 + j;
                const int dst = (ksg < KS_O) ? (int)WS_WO + ((nt * KS_O + ksg) * 64 + lane) * 16
                                             : (int)WS_WD + ((nt * KS_H + (ksg - KS_O)) * 64 + lane) * 16;
#define WAUX 0
                bst4<WAUX>(rw, dst, __builtin_bit_cast(u32x4, v));
            }
        }
    };
    u32x4 r[NU ? NU : 1][4];
    u32x4 x0[NR ? NR : 1], x1[NR ? NR : 1], w0, w1;
    auto gfetch = [&]() {
        w0 = *(const u32x4*)(gate_w + lane * 8);
        w1 = *(const u32x4*)(gate_w + 512 + (lane & 15) * 8);
#pragma unroll
        for (int i = 0; i < NR; ++i) {
            int row = r0 + rs * i;
            row = rb + (row < re ? row : r0);
            x0[i] = *(const u32x4*)(X + (size_t)row * D_ + lane * 8);
            x1[i] = *(const u32x4*)(X + (size_t)row * D_ + 512 + (lane & 15) * 8);
        }
    };
    if (NU) {
        const bool extra = u0 + (NU - 1) * us < ue;
#pragma unroll
        for (int u = 0; u < NU - 1; ++u) fetch(u0 + u * us, r[u]);
        if (extra) fetch(u0 + (NU - 1) * us, r[NU - 1]);
        if (EARLY && NR) gfetch();
#pragma unroll
        for (int u = 0; u < NU - 1; ++u) emit(u0 + u * us, r[u]);
        if (extra) emit(u0 + (NU - 1) * us, r[NU - 1]);
    }
    CBAR();
    if (NR == 0) return;
    if (!EARLY || !NU) gfetch();
    if (lane >= 16) w1 = (u32x4){0u, 0u, 0u, 0u};
    float sacc[NR ? NR : 1];
#pragma unroll
    for (int i = 0; i < NR; ++i) {
        float a = 0.f;
#pragma unroll
        for (int e = 0; e < 4; ++e) {
            const unsigned xa = x0[i][e], wa = w0[e], xb = x1[i][e], wb = w1[e];
            a = __builtin_amdgcn_fdot2_f32_bf16(__builtin_bit_cast(bf2_t, xa), __builtin_bit_cast(bf2_t, wa), a, false);
            a = __builtin_amdgcn_fdot2_f32_bf16(__builtin_bit_cast(bf2_t, xb), __builtin_bit_cast(bf2_t, wb), a, false);
        }
        sacc[i] = a;
    }
    {
        float z = 0.f;
#pragma unroll
        for (int i = 0; i < NR; ++i) {
            const float tot = wave_sum(sacc[i]);
            z = (lane == i) ? tot : z;
        }
        const int row = r0 + rs * lane;
        z = rbf(z);
        if (lane < NR && row < re) gout[rb + row] = rbf(1.f / (1.f + __expf(-z)));
    }
}

__global__ __launch_bounds__(K1NW * 64) void k1_gemm(const u16* __restrict__ X, const u16* __restrict__ Wqkv,
                                               const u16* __restrict__ Wg, const u16* __restrict__ Wu,
                                               const u16* __restrict__ Wo, const u16* __restrict__ Wd,
                                               const u16* __restrict__ gate_w, char* __restrict__ ws) {
    __shared__ __attribute__((aligned(16))) char smem[K1_LDS];
    const int bid = blockIdx.x;
    const int xcd = bid & 7, idx = bid >> 3;
    const int ng = xcd >> 1, mg = xcd & 1;
    const int cnt = (ng < 2) ? 8 : 7;
#define NBAL 1
    const int nstart = ((idx >> 2) < 3) ? ng * 3 : ((ng < 2) ? 9 + ng * 5 : 11 + ng * 4);
    const int uw = __builtin_amdgcn_readfirstlane(threadIdx.x >> 6);
#define CPF 3
#define CPB 2
#define CPP 12
#define CPX 1
    if (CPF && __builtin_expect(uw >= 10 && idx < CPB, CPX)) {
        const int ln = lane_id();
        unsigned l1, h1, l2, h2;
        asm volatile("s_getpc_b64 s[20:21]\n\ts_add_u32 %0, s20, _Z7k1_gemmPKtS0_S0_S0_S0_S0_S0_Pc@rel32@lo+4\n\ts_addc_u32 %1, s21, _Z7k1_gemmPKtS0_S0_S0_S0_S0_S0_Pc@rel32@hi+12\n\t"
                     "s_getpc_b64 s[20:21]\n\ts_add_u32 %2, s20, _Z6k2_mixPcPt@rel32@lo+4\n\ts_addc_u32 %3, s21, _Z6k2_mixPcPt@rel32@hi+12"
                     : "=&s"(l1), "=&s"(h1), "=&s"(l2), "=&s"(h2) :: "s20", "s21", "scc");
        const unsigned long long a1 = ((unsigned long long)h1 << 32) | l1, a2 = ((unsigned long long)h2 << 32) | l2;
        unsigned d = 0;
        if (uw == 10) {
            if (CPF & 1) {
                const unsigned len = (a2 > a1 && a2 - a1 < 65536ull) ? (unsigned)(a2 - a1) : 0u;
#pragma unroll
                for (int q = 0; q < 4; ++q) {
                    const unsigned o = (unsigned)(q * 64 + ln) * 128u;
                    if (o < len) asm volatile("global_load_dword %0, %1, %2" : "+v"(d) : "v"(o), "s"(a1) : "memory");
                }
            }
        } else if (CPF & 2) {
            if (CPP) {
                __builtin_amdgcn_s_barrier();
                CBAR();
                k1_wait_rel<4>(lds_addr(smem), ln, CPP);
            }
#pragma unroll
            for (int q = 0; q < 2; ++q) {
                const unsigned o = (unsigned)(q * 64 + ln) * 128u;
                if (o < 13824u) asm volatile("global_load_dword %0, %1, %2" : "+v"(d) : "v"(o), "s"(a2) : "memory");
            }
        }
        asm volatile("s_waitcnt vmcnt(0)" : "+v"(d) :: "memory");
        return;
    }
    if (idx >= 4 * cnt) {
        const int sbk = (xcd - 4) * 4 + (idx - 28);
        if (threadIdx.x < 32)
            *(u32x4*)(ws + WS_FLAG + (size_t)(sbk * 2 + (threadIdx.x >> 4)) * FLAG_LD + (threadIdx.x & 15) * 16) = (u32x4){0u, 0u, 0u, 0u};
        constexpr int NWV = 16 * K1NW;
        const int gw = sbk * K1NW + uw, wq = (((xcd - 4) >> 1) * 4 + (idx - 28)) * K1NW + uw;
        constexpr int NRS = (1024 + NWV / 2 - 1) / (NWV / 2);
        if (AUXM == 0) k1_aux<(740 + NWV - 1) / NWV, NRS, false>(X, Wo, Wd, gate_w, ws, smem + uw * 2048, gw, NWV, 740, mg * 1024, wq, NWV / 2, 1024);
        if (AUXM == 1) k1_aux<1, 0, false>(X, Wo, Wd, gate_w, ws, smem + uw * 2048, 720 + uw * 16 + sbk, 0, 740, 0, 0, 0, 0);
        if (AUXM == 2) k1_aux<1, NRS, true>(X, Wo, Wd, gate_w, ws, smem + uw * 2048, 720 + uw * 16 + sbk, 0, 740, mg * 1024, wq, NWV / 2, 1024);
        if (AUXM == 3) k1_aux<(740 + NWV - 1) / NWV, 0, false>(X, Wo, Wd, gate_w, ws, smem + uw * 2048, gw, NWV, 740, mg * 1024, wq, NWV / 2, 1024);
        if (AUXM == 4) k1_aux<(260 + NWV - 1) / NWV, 0, false>(X, Wo, Wd, gate_w, ws, smem + uw * 2048, 480 + gw, NWV, 740, mg * 1024, wq, NWV / 2, 1024);
        return;
    }
    const int mt = mg * 4 + (idx & 3);
    const int nt = nstart + (idx >> 2);
#define L2PF 0
#define PFD 0
#define PFK 2
#define PFS 0
    if (uw >= 8 + NPRD) {
        if (AUXM == 0 || uw > 10) return;
        __builtin_amdgcn_s_barrier();
        CBAR();
        k1_wait_rel<4>(lds_addr(smem), lane_id(), AUXS);
        const int lin = mt * 30 + nt;
        if (AUXM == 1) k1_aux<3, 9, true>(X, Wo, Wd, gate_w, ws, smem + K1_AUX, lin, 240, 720, mt * 256, nt, 30, 256);
        if (AUXM == 2) k1_aux<3, 0, true>(X, Wo, Wd, gate_w, ws, smem + K1_AUX, lin, 240, 720, mt * 256, nt, 30, 256);
        if (AUXM == 3) k1_aux<0, 9, true>(X, Wo, Wd, gate_w, ws, smem + K1_AUX, lin, 240, 720, mt * 256, nt, 30, 256);
        if (AUXM == 4) k1_aux<2, 9, true>(X, Wo, Wd, gate_w, ws, smem + K1_AUX, lin, 240, 480, mt * 256, nt, 30, 256);
        return;
    }
    if (nt < 12) k1_tile<5>(X, Wqkv, Wqkv, ws, smem, mt, nt, idx >> 2, cnt, uw ENTPASS);
    else k1_tile<6>(X, Wg, Wu, ws, smem, mt, nt - 12, idx >> 2, cnt, uw ENTPASS);
}

typedef unsigned long long u64;
#define GDELAY 8
#define PF2 0
#define ACQ 0
#define ROT 0
#define ROLL 0
#define GPRIO 0
#define POLLSLP 4
#define APRIO 2
#define ST2(i) do {} while (0)
#define ST2ID() do {} while (0)
__device__ __forceinline__ void st_agent(void* p, u32x2 v) {
    __hip_atomic_store((u64*)p, __builtin_bit_cast(u64, v), __ATOMIC_RELAXED, __HIP_MEMORY_SCOPE_AGENT);
}
template <int AUX> __device__ __forceinline__ bf16x8 bld(rsrc_t r, int voff, int soff) {
    return __builtin_bit_cast(bf16x8, __builtin_amdgcn_raw_buffer_load_b128(r, voff, soff, AUX));
}

__device__ __forceinline__ float fmx(float a, float b) { return __builtin_elementwise_maximum(a, b); }

typedef float f32x16 __attribute__((ext_vector_type(16)));
#define MF32(a, b, c) __builtin_amdgcn_mfma_f32_32x32x16_bf16(a, b, c, 0, 0, 0)
#define MF8(a, b, c) __builtin_amdgcn_mfma_f32_32x32x8bf16_1k(a, b, c, 0, 0, 0)
constexpr int AT_V = 15360, AT_PAD = 30720, AT_ONE = AT_PAD + 64;
constexpr int AT_LDS = AT_ONE + 35 * 320 + 16;
template <bool EDGE>
__device__ __forceinline__ void k2_attn_wave(char* __restrict__ oF, const char* smem, const bf16x8 q1, const s16x4 q2,
                                             const float gg, int h, int qw, int rbase, int lane) {
    const int hi = lane >> 5, ql = lane & 31;
    const float NEG = -__builtin_inff();
    const int ts = 8 - (qw >> 5);
    const char* kb = smem + (rbase + ql) * 40;
    const char* ka = kb + hi * 16;
    const char* vb;
    {
        const int q4 = (lane & 15) >> 2, p4 = lane & 3, db = (lane >> 4) & 1;
        vb = smem + AT_V + (rbase + 4 * hi + q4) * 40 + db * 32 + p4 * 8;
        if (db == 1 && p4 >= 1) vb = smem + AT_ONE + (p4 >> 1) * 8;
    }
    auto rd16 = [&](const char* p) {
        const u32x2 lo = *(const u32x2*)p, up = *(const u32x2*)(p + 8);
        const u32x4 v = {lo[0], lo[1], up[0], up[1]};
        return __builtin_bit_cast(bf16x8, v);
    };
    auto rdK1 = [&](int t) { return rd16(ka + t * 1280); };
    auto rdK2 = [&](int t) { return *(const s16x4*)(kb + t * 1280 + 32); };
    auto rdV = [&](int t, int a) {
        s16x4 lo = tr_read(vb + t * 1280 + a * 640);
        s16x4 up = tr_read(vb + t * 1280 + a * 640 + 320);
        return cat4(lo, up);
    };
    f32x16 zero;
#pragma unroll
    for (int i = 0; i < 16; ++i) zero[i] = 0.f;
#define SB0() __builtin_amdgcn_sched_barrier(0)
    auto TL = [](int u) { return u < 7 ? u + 1 : (u == 7 ? 0 : 8); };
    float mx = NEG, my = NEG;
    f32x16 s[3];
    bf16x8 k1[2], vf[2][2];
    s16x4 k2[2];
    k1[0] = rdK1(TL(0)); k2[0] = rdK2(TL(0)); k1[1] = rdK1(TL(1)); k2[1] = rdK2(TL(1));
    SB0();
#pragma unroll
    for (int u = -1; u < 9; ++u) {
        if (u + 1 <= 8) {
            s[(u + 1) % 3] = MF32(k1[(u + 1) & 1], q1, zero);
            SB0();
            if (u + 3 <= 8) k1[(u + 1) & 1] = rdK1(TL(u + 3));
            else if (u + 3 == 9) k1[(u + 1) & 1] = rdK1(1);
            else k1[(u + 1) & 1] = rdK1(2);
        }
        SB0();
        const int p = u - 1;
        if (p >= 0 && p < 7 && (!EDGE || p + 1 >= ts)) {
            const f32x16 x = s[p % 3];
            mx = fmx(fmx(mx, x[0]), x[1]); my = fmx(fmx(my, x[2]), x[3]);
            mx = fmx(fmx(mx, x[4]), x[5]); my = fmx(fmx(my, x[6]), x[7]);
        }
        SB0();
        if (u >= 0) {
            s[u % 3] = MF8(k2[u & 1], q2, s[u % 3]);
            SB0();
            if (u + 2 <= 8) k2[u & 1] = rdK2(TL(u + 2));
            else if (u + 2 == 9) k2[u & 1] = rdK2(1);
            else k2[u & 1] = rdK2(2);
        }
        SB0();
        if (p >= 0 && p < 7 && (!EDGE || p + 1 >= ts)) {
            const f32x16 x = s[p % 3];
            mx = fmx(fmx(mx, x[8]), x[9]); my = fmx(fmx(my, x[10]), x[11]);
            mx = fmx(fmx(mx, x[12]), x[13]); my = fmx(fmx(my, x[14]), x[15]);
        }
        SB0();
    }
    vf[0][0] = rdV(8, 0); vf[0][1] = rdV(8, 1);
    if (!EDGE) { vf[1][0] = rdV(0, 0); vf[1][1] = rdV(0, 1); }
    const int z = ql - 4 * hi;
    f32x16 mg;
#pragma unroll
    for (int i = 0; i < 16; ++i) {
        const bool c = 8 * (i >> 2) + (i & 3) > z;
        mg[i] = c ? (EDGE ? NEG : s[1][i]) : s[2][i];
    }
#pragma unroll
    for (int i = 0; i < 16; i += 4) { mx = fmx(fmx(mx, mg[i]), mg[i + 1]); my = fmx(fmx(my, mg[i + 2]), mg[i + 3]); }
    mx = fmx(mx, my);
    {
        const unsigned mi = __builtin_bit_cast(unsigned, mx);
        auto r = __builtin_amdgcn_permlane32_swap(mi, mi, false, false);
        mx = fmx(__builtin_bit_cast(float, (unsigned)r[0]), __builtin_bit_cast(float, (unsigned)r[1]));
    }
    const float nm = -mx;
    f32x16 cm;
#pragma unroll
    for (int i = 0; i < 16; ++i) cm[i] = nm;
    SB0();
    f32x16 o = zero;
    s[1] = MF32(k1[1], q1, cm);
    SB0();
    k1[1] = rdK1(3);
    {
        u32x4 pa, pb;
#pragma unroll
        for (int a = 0; a < 2; ++a) {
#pragma unroll
            for (int e = 0; e < 4; ++e) {
                const int i = 8 * a + 2 * e;
                const float x0 = __builtin_amdgcn_exp2f(mg[i] + nm), x1 = __builtin_amdgcn_exp2f(mg[i + 1] + nm);
                const unsigned pk = pack_bf16(x0, x1);
                if (EDGE) pa[e] = pk;
                else {
                    const bool c0 = 8 * (i >> 2) + (i & 3) > z, c1 = 8 * (i >> 2) + (i & 3) + 1 > z;
                    pb[e] = pack_bf16(c0 ? x0 : 0.f, c1 ? x1 : 0.f);
                    pa[e] = pk ^ pb[e];
                }
                if (a == 0 && e == 1) { SB0(); s[1] = MF8(k2[1], q2, s[1]); SB0(); k2[1] = rdK2(3); }
            }
            SB0();
            o = MF32(vf[0][a], __builtin_bit_cast(bf16x8, pa), o);
            SB0();
            if (!EDGE) {
                if (a == 1) { s[0] = MF32(k1[0], q1, cm); SB0(); k1[0] = rdK1(4); }
                o = MF32(vf[1][a], __builtin_bit_cast(bf16x8, pb), o);
                SB0();
            } else if (a == 1) { s[0] = MF32(k1[0], q1, cm); SB0(); k1[0] = rdK1(4); }
            vf[1][a] = rdV(1, a);
            vf[0][a] = rdV(2, a);
            SB0();
        }
    }
    u32x4 pk;
    float xa, xb;
#pragma unroll
    for (int t = 1; t < 8; ++t) {
        const bool on = !EDGE || t >= ts;
        auto EX = [&](int i) { xa = __builtin_amdgcn_exp2f(s[t & 1][i]); xb = __builtin_amdgcn_exp2f(s[t & 1][i + 1]); };
        float ya, yb;
        if (t < 7) { s[(t + 1) & 1] = MF8(k2[(t + 1) & 1], q2, s[(t + 1) & 1]); SB0(); if (t + 3 < 8) k2[(t + 1) & 1] = rdK2(t + 3); }
        SB0();
        if (on) {
            EX(0); SB0();
            ya = xa; yb = xb; EX(2); pk[0] = pack_bf16(ya, yb); SB0();
            ya = xa; yb = xb; EX(4); pk[1] = pack_bf16(ya, yb); SB0();
            ya = xa; yb = xb; EX(6); pk[2] = pack_bf16(ya, yb); SB0();
            ya = xa; yb = xb; EX(8); pk[3] = pack_bf16(ya, yb); SB0();
            ya = xa; yb = xb; EX(10);
            const unsigned n0 = pack_bf16(ya, yb); SB0();
            o = MF32(vf[t & 1][0], __builtin_bit_cast(bf16x8, pk), o);
            SB0();
            if (t + 2 < 8) vf[t & 1][0] = rdV(t + 2, 0);
            u32x4 pn;
            pn[0] = n0;
            ya = xa; yb = xb; EX(12); pn[1] = pack_bf16(ya, yb); SB0();
            ya = xa; yb = xb; EX(14); pn[2] = pack_bf16(ya, yb); SB0();
            if (!EDGE && t + 2 < 8) { s[t & 1] = MF32(k1[t & 1], q1, cm); SB0(); if (t + 4 < 8) k1[t & 1] = rdK1(t + 4); }
            pn[3] = pack_bf16(xa, xb); SB0();
            o = MF32(vf[t & 1][1], __builtin_bit_cast(bf16x8, pn), o);
            SB0();
            if (t + 2 < 8) vf[t & 1][1] = rdV(t + 2, 1);
        } else if (t + 2 < 8) { vf[t & 1][0] = rdV(t + 2, 0); vf[t & 1][1] = rdV(t + 2, 1); }
        SB0();
        if (EDGE && t + 2 < 8) { s[t & 1] = MF32(k1[t & 1], q1, cm); SB0(); if (t + 4 < 8) k1[t & 1] = rdK1(t + 4); }
        SB0();
    }
    float sum;
    {
        const float o8 = o[8];
        const unsigned si = __float_as_uint(o8);
        auto r = __builtin_amdgcn_permlane32_swap(si, si, false, false);
        sum = __builtin_bit_cast(float, (unsigned)r[1]);
    }
    const float inv = gg * __builtin_amdgcn_rcpf(sum);
    const int m = qw + ql;
    {
        const rsrc_t ro = mk_rsrc(oF, (unsigned)(S_ * D_ * 2));
        const int ob = ((m >> 4) * KS_O * 64 + (m & 15)) * 16 + (h >> 1) * 1280;
        const u32x4 a = {pack_bf16(o[0] * inv, o[1] * inv), pack_bf16(o[2] * inv, o[3] * inv), pack_bf16(o[4] * inv, o[5] * inv), pack_bf16(o[6] * inv, o[7] * inv)};
        bst4<AUX_AGENT>(ro, ob + (2 * (h & 1) + hi) * 256, a);
        const u32x2 b = {pack_bf16(o[8] * inv, o[9] * inv), pack_bf16(o[10] * inv, o[11] * inv)};
        if (hi == 0) bst2<AUX_AGENT>(ro, ob + 1024 + (h & 1) * 8, b);
    }
}

__device__ __forceinline__ void k2_attn(char* __restrict__ ws, char* smem, int bid) {
    const int h = (bid & 7) + 8 * ((bid >> 3) & 3);
    const int qb = bid >> 5;
    const int q0 = qb * 128;
    const int t = threadIdx.x, lane = t & 63;
    const int w = __builtin_amdgcn_readfirstlane(t >> 6);
    const char* Qb = ws + WS_Q + (size_t)h * S_ * 40;
    const char* Kb = ws + WS_K + (size_t)h * S_ * 40;
    const char* Vb = ws + WS_V + (size_t)h * S_ * 40;
    char* sK = smem;
    char* sV = smem + AT_V;

    ST2(0); ST2ID();
    if (APRIO) __builtin_amdgcn_s_setprio(APRIO);
#pragma unroll
    for (int i = 0; i < 4; ++i) {
        const int p = i * 256 + t;
        if (i < 3 || w < 3) {
            int off = (q0 - 256) * 40 + p * 16;
            off = off < 0 ? 0 : off;
            glds16(Kb + off, sK + p * 16);
            glds16(Vb + off, sV + p * 16);
        }
    }
    if (t < 4) *(u32x4*)(smem + AT_PAD + t * 16) = (u32x4){0u, 0u, 0u, 0u};
    if (t >= 64 && t < 100) *(u32x4*)(smem + AT_ONE + (t - 64) * 320) = (u32x4){0x3f80u, 0u, 0u, 0u};
    const int hi = lane >> 5;
    const int qw = q0 + 32 * w;
    bf16x8 q1;
    s16x4 q2;
    {
        const char* qp = Qb + (size_t)(qw + (lane & 31)) * 40;
        const u32x2 a = *(const u32x2*)(qp + hi * 16), b = *(const u32x2*)(qp + hi * 16 + 8);
        u32x2 c = *(const u32x2*)(qp + 32);
        if (hi) c = (u32x2){0u, 0u};
        const u32x4 v1 = {a[0], a[1], b[0], b[1]};
        q1 = __builtin_bit_cast(bf16x8, v1); q2 = __builtin_bit_cast(s16x4, c);
    }
    const float gg = ((const float*)(ws + WS_G))[qw + (lane & 31)];
    __syncthreads();
    ST2(1);
    char* oF = ws + WS_O;
    if (qb >= 2) k2_attn_wave<false>(oF, smem, q1, q2, gg, h, qw, 32 * w, lane);
    else k2_attn_wave<true>(oF, smem, q1, q2, gg, h, qw, 32 * w, lane);
    ST2(2);
    asm volatile("s_waitcnt vmcnt(0)" ::: "memory");
    ST2(3);
    if (lane == 0)
        __hip_atomic_store((unsigned*)(ws + WS_FLAG + (qw >> 6) * FLAG_LD) + h * 2 + ((qw >> 5) & 1), 1u, __ATOMIC_RELAXED, __HIP_MEMORY_SCOPE_AGENT);
}

__device__ __forceinline__ void k2_gemm(char* __restrict__ ws, u16* __restrict__ out, char* smem, int bid) {
    const int xcd = bid & 7, idx = bid >> 3;
    const int mt = (xcd >> 1) * 8 + (idx & 7);
    const int nt = (xcd & 1) * 4 + (idx >> 3);
    const int t = threadIdx.x, lane = t & 63;
    const int w = __builtin_amdgcn_readfirstlane(t >> 6);
    const int fr = lane & 15, g = lane >> 4;
    const int nsteps = (w < 3) ? 14 : 12;
    const rsrc_t rs = __builtin_amdgcn_make_buffer_rsrc(ws, 0, (int)WS_FLAG, 0x00020000);
    const int vo = lane * 16;
    int ap[4];
#pragma unroll
#define K2SAME 0
    for (int i = 0; i < 4; ++i) ap[i] = (int)WS_H + ((((K2SAME & 1) ? 0 : (K2SAME & 4) ? (mt & 24) : mt) * 4 + (i ^ w)) * KS_H + 14 * w) * 1024;
    int bp = (int)WS_WD + ((((K2SAME & 2) ? 0 : nt) * 5) * KS_H + 14 * w) * 1024;
#define RTA 0
#define RTB 0
    int cur = ((RTA * (nt & 3) + (RTB * (mt & 7) >> 2)) % nsteps) * 1024;
    auto adv = [&]() { cur += 1024; cur = (cur == nsteps * 1024) ? 0 : cur; };

    f32x4 acc[4][5];
#pragma unroll
    for (int i = 0; i < 4; ++i)
#pragma unroll
        for (int j = 0; j < 5; ++j) acc[i][j] = (f32x4){0.f, 0.f, 0.f, 0.f};
    bf16x8 a0[4], b0[5], a1[4], b1[5];
#define SB() __builtin_amdgcn_sched_barrier(0)
#define MFA(c, b, a) asm volatile("v_mfma_f32_16x16x32_bf16 %0, %1, %2, %0" : "+v"(c) : "v"(b), "v"(a))
    const u32x4 rq = {(unsigned)(size_t)ws, (unsigned)((size_t)ws >> 32) & 0xffffu, (unsigned)WS_FLAG, 0x00020000u};
#define LDN(r, so) asm volatile("buffer_load_dwordx4 %0, %1, %2, %3 offen" : "=v"(r) : "v"(vo), "s"(rq), "s"(so) : "memory")
#define LDR(r, so) asm volatile("buffer_load_dwordx4 %0, %1, %2, %3 offen" : "+v"(r) : "v"(vo), "s"(rq), "s"(so) : "memory")
#define LDC(r, so) asm volatile("buffer_load_dwordx4 %0, %1, %2, %3 offen sc1" : "+v"(r) : "v"(vo), "s"(rq), "s"(so) : "memory")
    auto ld = [&](bf16x8 (&a)[4], bf16x8 (&b)[5]) {
#pragma unroll
        for (int j = 0; j < 4; ++j) LDN(b[j], bp + j * KS_H * 1024 + cur);
#pragma unroll
        for (int i = 0; i < 4; ++i) LDN(a[i], ap[i] + cur);
        LDN(b[4], bp + 4 * KS_H * 1024 + cur);
        adv();
    };
    auto mmx = [&](auto w0, auto w4, bf16x8 (&a)[4], bf16x8 (&b)[5], auto fa, auto fb) {
        constexpr int W0 = decltype(w0)::value, W4 = decltype(w4)::value;
#pragma unroll
        for (int j = 0; j < 5; ++j) {
#pragma unroll
            for (int i = 0; i < 4; ++i) {
                if (j == 0) { if (i == 0) WAIT_VM(W0); if (i == 1) WAIT_VM(W0 - 1); if (i == 2) WAIT_VM(W0 - 2); if (i == 3) WAIT_VM(W0 - 3); }
                if (j == 4 && i == 0 && W4 >= 0) WAIT_VM(W4 >= 0 ? W4 : 0);
                MFA(acc[i][j], b[j], a[i]);
                if (j == 4) fa(a[i], i);
            }
            fb(b[j], j);
        }
    };
#define IC(n) std::integral_constant<int, n>{}
    auto nof = [&](bf16x8&, int) {};
    auto fha = [&](bf16x8& r, int i) { LDR(r, ap[i] + cur); };
    auto fhb = [&](bf16x8& r, int j) { LDR(r, bp + j * KS_H * 1024 + cur); };
    ST2(0); ST2ID();
    if (GPRIO) __builtin_amdgcn_s_setprio(GPRIO);
#pragma unroll 1
    for (int i = 0; i < GDELAY; ++i) __builtin_amdgcn_s_sleep(8);
    float gl;
    asm volatile("global_load_dword %0, %1, %2" : "=v"(gl) : "v"(vo >> 2), "s"(ws + WS_G + mt * 256) : "memory");
    ld(a0, b0);
    ld(a1, b1);
#pragma unroll
    for (int i = 0; i < 4; ++i) asm volatile("s_nop 1" : "+v"(acc[i][0]), "+v"(acc[i][1]), "+v"(acc[i][2]), "+v"(acc[i][3]), "+v"(acc[i][4]));
#pragma unroll 1
    for (int s = 0; s < nsteps - 4; s += 2) {
        mmx(IC(13), IC(13), a0, b0, fha, fhb); adv();
        mmx(IC(13), IC(13), a1, b1, fha, fhb); adv();
    }
    const char* fb_ = ws + WS_FLAG + mt * FLAG_LD + 64 * w;
    const int fv = (vo >> 2) & 60;
    mmx(IC(13), IC(13), a0, b0, fha, fhb); adv();
    unsigned fl;
    asm volatile("global_load_dword %0, %1, %2 sc1" : "=v"(fl) : "v"(fv), "s"(fb_) : "memory");
    mmx(IC(14), IC(14), a1, b1, fha, fhb);
    ST2(1);
    int op[4];
#pragma unroll
    for (int i = 0; i < 4; ++i) op[i] = (int)WS_O + ((mt * 4 + (i ^ w)) * KS_O + 5 * w) * 1024;
    const int wp = (int)WS_WO + ((nt * 5) * KS_O + 5 * w) * 1024;
    int so = 0;
    auto foa = [&](bf16x8& r, int i) { LDC(r, op[i] + so); };
    auto fob = [&](bf16x8& r, int j) { LDR(r, wp + j * KS_O * 1024 + so); };
    mmx(IC(14), IC(14), a0, b0, nof, fob);
    ST2(2);
    asm volatile("s_waitcnt vmcnt(14)\n\tv_cmp_ne_u32 vcc, 1, %0\n\ts_cbranch_vccz L_pok_%=\n"
                 "L_pspin_%=:\n\ts_sleep %3\n\tglobal_load_dword %0, %1, %2 sc1\n\ts_waitcnt vmcnt(0)\n\t"
                 "v_cmp_ne_u32 vcc, 1, %0\n\ts_cbranch_vccnz L_pspin_%=\n"
                 "L_pok_%=:" : "+v"(fl) : "v"(fv), "s"(fb_), "n"(POLLSLP) : "vcc", "memory");
    ST2(3);
#pragma unroll
    for (int i = 0; i < 4; ++i) foa(a0[i], i);
    so = 1024;
    mmx(IC(13), IC(13), a1, b1, foa, fob);
    asm volatile("s_nop 7\n\ts_nop 7" : "+v"(gl) :: "memory");
#pragma unroll
    for (int i = 0; i < 4; ++i) {
        const float sc = rbf(1.f - __builtin_bit_cast(float, __builtin_amdgcn_ds_bpermute((i ^ w) * 64 + fv, __builtin_bit_cast(int, gl))));
        asm volatile("" : "+v"(acc[i][0]), "+v"(acc[i][1]), "+v"(acc[i][2]), "+v"(acc[i][3]), "+v"(acc[i][4]));
#pragma unroll
        for (int j = 0; j < 5; ++j) acc[i][j] = acc[i][j] * sc;
        asm volatile("" : "+v"(acc[i][0]), "+v"(acc[i][1]), "+v"(acc[i][2]), "+v"(acc[i][3]), "+v"(acc[i][4]));
    }
    asm volatile("s_nop 3" ::: "memory");
    so = 2048; mmx(IC(12), IC(-1), a0, b0, foa, fob);
    so = 3072; mmx(IC(13), IC(13), a1, b1, foa, fob);
    so = 4096; mmx(IC(13), IC(13), a0, b0, foa, fob);
    mmx(IC(13), IC(9), a1, b1, nof, nof);
    mmx(IC(4), IC(0), a0, b0, nof, nof);
    asm volatile("s_nop 7\n\ts_nop 7" ::: "memory");
#pragma unroll
    for (int i = 0; i < 4; ++i) asm volatile("" : "+v"(acc[i][0]), "+v"(acc[i][1]), "+v"(acc[i][2]), "+v"(acc[i][3]), "+v"(acc[i][4]));
    ST2(4);
    const int ln = lane_id();
    char* mine = smem + w * 10240 + ln * 16;
    char* peer = smem + (w ^ 2) * 10240 + ln * 16;
#pragma unroll
    for (int i = 0; i < 2; ++i)
#pragma unroll
        for (int j = 0; j < 5; ++j) *(f32x4*)(mine + (i * 5 + j) * 1024) = acc[2 + i][j];
    __syncthreads();
#pragma unroll
    for (int j = 0; j < 5; ++j) acc[1][j] += *(const f32x4*)(peer + (5 + j) * 1024);
#pragma unroll
    for (int j = 0; j < 5; ++j) *(f32x4*)(peer + (5 + j) * 1024) = acc[1][j];
#pragma unroll
    for (int j = 0; j < 5; ++j) acc[0][j] += *(const f32x4*)(peer + j * 1024);
    __syncthreads();
    {
        const char* q = smem + (w ^ 3) * 10240 + ln * 16 + 5 * 1024;
        u16* dst = out + (size_t)(mt * 64 + w * 16 + (ln & 15)) * D_ + nt * 80 + (ln >> 4) * 4;
#pragma unroll
        for (int j = 0; j < 5; ++j) {
            const f32x4 v = acc[0][j] + *(const f32x4*)(q + j * 1024);
            u32x2 pk = {pack_bf16(v[0], v[1]), pack_bf16(v[2], v[3])};
            *(u32x2*)(dst + j * 16) = pk;
        }
    }
    ST2(5);
}

__global__ __launch_bounds__(256, 3) void k2_mix(char* __restrict__ ws, u16* __restrict__ out) {
    __shared__ __attribute__((aligned(16))) char smem[AT_LDS > 40960 ? AT_LDS : 40960];
    if (blockIdx.x < 256) k2_gemm(ws, out, smem, blockIdx.x);
    else k2_attn(ws, smem, blockIdx.x - 256);
}

extern "C" void kernel_launch(void* const* d_in, const int* in_sizes, int n_in,
                              void* d_out, int out_size, void* d_ws, size_t ws_size,
                              hipStream_t stream) {
    const u16* X = (const u16*)d_in[0];
    const u16* Wqkv = (const u16*)d_in[1];
    const u16* Wo = (const u16*)d_in[2];
    const u16* Wg = (const u16*)d_in[4];
    const u16* Wu = (const u16*)d_in[5];
    const u16* Wd = (const u16*)d_in[6];
    const u16* gw = (const u16*)d_in[7];
    char* ws = (char*)d_ws;
    k1_gemm<<<256, K1NW * 64, 0, stream>>>(X, Wqkv, Wg, Wu, Wo, Wd, gw, ws);
    k2_mix<<<768, 256, 0, stream>>>(ws, (u16*)d_out);
}
```

```cpp
#include <hip/hip_runtime.h>
#include <hip/hip_bf16.h>
#include <stdint.h>
#include <type_traits>

typedef unsigned short u16;
typedef short bf16x8 __attribute__((ext_vector_type(8)));
typedef short s16x4 __attribute__((ext_vector_type(4)));
typedef float f32x4 __attribute__((ext_vector_type(4)));
typedef float f32x2 __attribute__((ext_vector_type(2)));
typedef unsigned u32x4 __attribute__((ext_vector_type(4)));
typedef unsigned u32x2 __attribute__((ext_vector_type(2)));
typedef __bf16 bf2_t __attribute__((ext_vector_type(2)));

#define LDS_AS __attribute__((address_space(3)))

constexpr int S_ = 2048, D_ = 640, F_ = 1728, H_ = 32, DH_ = 20, NQKV = 1920;
constexpr int KS_O = D_ / 32;
constexpr int KS_H = F_ / 32;

constexpr size_t WS_Q = 0;
constexpr size_t QKV_BYTES = (size_t)H_ * S_ * DH_ * 2;
constexpr size_t WS_K = WS_Q + QKV_BYTES;
constexpr size_t WS_V = WS_K + QKV_BYTES;
constexpr size_t WS_H = WS_V + QKV_BYTES + 4096;
constexpr size_t WS_O = WS_H + (size_t)S_ * F_ * 2;
constexpr size_t WS_WO = WS_O + (size_t)S_ * D_ * 2;
constexpr size_t WS_WD = WS_WO + (size_t)D_ * D_ * 2;
constexpr size_t WS_G = WS_WD + (size_t)F_ * D_ * 2;
constexpr size_t WS_FLAG = WS_G + (size_t)S_ * 4;
constexpr int FLAG_LD = 4352;

__device__ __forceinline__ unsigned pack_bf16(float a, float b) {
    bf2_t v = {(__bf16)a, (__bf16)b};
    return __builtin_bit_cast(unsigned, v);
}
__device__ __forceinline__ float rbf(float x) { return (float)(__bf16)x; }
__device__ __forceinline__ float bf2f(u16 v) { return __uint_as_float(((unsigned)v) << 16); }

template <int CTRL> __device__ __forceinline__ float dpp_add(float x) {
    return x + __builtin_bit_cast(float, __builtin_amdgcn_update_dpp(0, __builtin_bit_cast(int, x), CTRL, 0xf, 0xf, false));
}
__device__ __forceinline__ float wave_sum(float x) {
    x = dpp_add<0xB1>(x); x = dpp_add<0x4E>(x); x = dpp_add<0x141>(x); x = dpp_add<0x140>(x);
    const int xi = __builtin_bit_cast(int, x);
    return (__builtin_bit_cast(float, __builtin_amdgcn_readlane(xi, 0)) + __builtin_bit_cast(float, __builtin_amdgcn_readlane(xi, 16))) +
           (__builtin_bit_cast(float, __builtin_amdgcn_readlane(xi, 32)) + __builtin_bit_cast(float, __builtin_amdgcn_readlane(xi, 48)));
}
__device__ __forceinline__ s16x4 tr_read(const char* lds_ptr) {
    return __builtin_amdgcn_ds_read_tr16_b64_v4i16((LDS_AS s16x4*)(lds_ptr));
}
__device__ __forceinline__ bf16x8 cat4(s16x4 a, s16x4 b) {
    bf16x8 r;
    r[0] = a[0]; r[1] = a[1]; r[2] = a[2]; r[3] = a[3];
    r[4] = b[0]; r[5] = b[1]; r[6] = b[2]; r[7] = b[3];
    return r;
}
__device__ __forceinline__ void glds16(const void* g, void* l) {
    __builtin_amdgcn_global_load_lds((const unsigned*)g, (LDS_AS unsigned*)l, 16, 0, 0);
}
__device__ __forceinline__ void glds16_asm(const void* gsrc, unsigned lds_dst) {
    unsigned keep;
    asm volatile("s_mov_b32 %0, m0\n\ts_mov_b32 m0, %2\n\ts_nop 0\n\tglobal_load_lds_dwordx4 %1, off\n\ts_mov_b32 m0, %0"
                 : "=&s"(keep) : "v"(gsrc), "s"(lds_dst) : "memory");
}
__device__ __forceinline__ void glds4_asm(const void* gsrc, unsigned lds_dst) {
    unsigned keep;
    asm volatile("s_mov_b32 %0, m0\n\ts_mov_b32 m0, %2\n\ts_nop 0\n\tglobal_load_lds_dword %1, off\n\ts_mov_b32 m0, %0"
                 : "=&s"(keep) : "v"(gsrc), "s"(lds_dst) : "memory");
}
__device__ __forceinline__ unsigned lds_addr(const void* p) { return (unsigned)(size_t)(LDS_AS const char*)p; }
typedef __amdgpu_buffer_rsrc_t rsrc_t;
constexpr int AUX_AGENT = 16;
__device__ __forceinline__ rsrc_t mk_rsrc(const void* p, unsigned bytes) { return __builtin_amdgcn_make_buffer_rsrc((void*)p, 0, (int)bytes, 0x00020000); }
template <int AUX> __device__ __forceinline__ void bst4(rsrc_t r, int voff, u32x4 v) { __builtin_amdgcn_raw_buffer_store_b128(v, r, voff, 0, AUX); }
template <int AUX> __device__ __forceinline__ void bst2(rsrc_t r, int voff, u32x2 v) { __builtin_amdgcn_raw_buffer_store_b64(v, r, voff, 0, AUX); }
#define WAIT_VM(n) asm volatile("s_waitcnt vmcnt(%0)" :: "n"(n) : "memory")
#define CBAR() asm volatile("" ::: "memory")
#define STAMP(kid, i) do {} while (0)
#define STAMPK(kid, i) do { if ((i) == 3) STAMP(kid, i); } while (0)

#define ET(i) do {} while (0)
#define ETW() do {} while (0)
#define ENTARG
#define ENTPASS
#define WPG 64
#define OSC 0
#define ESC
#define HAUX 16
#define QAUX 16
#define WSC1
#define K1SB 0
#define K1P0 0
#define K1UNR 1
constexpr int K1_ABYTES = 256 * 64 * 2;
constexpr int K1_BSLOT = 32 * 192 * 2;
constexpr int K1_BOFF = 3 * K1_ABYTES;
constexpr int K1_AUX = K1_BOFF + 5 * K1_BSLOT;
constexpr int K1_FLG = K1_AUX + 2048;
#define PSLP 0
#define PSCH 0
#define BDEP 2
#define EARLYLD 0
constexpr int NPRD = (PSCH == 6) ? 4 : 2;
constexpr int K1_T0 = 32 + 16 * NPRD;
constexpr int K1_LDS = K1_FLG + K1_T0 + 32;
#define K1FJ 2
#define K1FS 6
#define PFL 4
#define MF1(c, b, a) c = __builtin_amdgcn_mfma_f32_16x16x32_bf16(b, a, c, 0, 0, 0)
typedef volatile LDS_AS unsigned* lflag_t;
template <int SLP> __device__ __forceinline__ void k1_wait_rel(unsigned sb0, int lane, unsigned n) {
    lflag_t p = (lflag_t)(size_t)(sb0 + K1_FLG + (lane & 7) * 4);
    while (__builtin_amdgcn_ballot_w64(*p < n) != 0ull) { if (SLP) __builtin_amdgcn_s_sleep(SLP); }
    CBAR();
}
__device__ __forceinline__ void glds_x4(unsigned lds, const void* sb, unsigned v0, unsigned v1, unsigned v2, unsigned v3) {
    unsigned keep;
    asm volatile("s_mov_b32 %0, m0\n\ts_mov_b32 m0, %1\n\ts_nop 0\n\t"
                 "global_load_lds_dwordx4 %3, %2\n\t"
                 "global_load_lds_dwordx4 %4, %2 offset:1024\n\t"
                 "global_load_lds_dwordx4 %5, %2 offset:2048\n\t"
                 "global_load_lds_dwordx4 %6, %2 offset:3072\n\t"
                 "s_mov_b32 m0, %0"
                 : "=&s"(keep) : "s"(lds), "s"(sb), "v"(v0), "v"(v1), "v"(v2), "v"(v3) : "memory");
}
__device__ __forceinline__ void glds_x2(unsigned lds, const void* sb, unsigned v0, unsigned v1) {
    unsigned keep;
    asm volatile("s_mov_b32 %0, m0\n\ts_mov_b32 m0, %1\n\ts_nop 0\n\t"
                 "global_load_lds_dwordx4 %3, %2\n\t"
                 "global_load_lds_dwordx4 %4, %2 offset:1024\n\t"
                 "s_mov_b32 m0, %0"
                 : "=&s"(keep) : "s"(lds), "s"(sb), "v"(v0), "v"(v1) : "memory");
}
__device__ __forceinline__ void glds_x1(unsigned lds, const void* sb, unsigned v0) {
    unsigned keep;
    asm volatile("s_mov_b32 %0, m0\n\ts_mov_b32 m0, %1\n\ts_nop 0\n\t"
                 "global_load_lds_dwordx4 %3, %2\n\t"
                 "s_mov_b32 m0, %0"
                 : "=&s"(keep) : "s"(lds), "s"(sb), "v"(v0) : "memory");
}
__device__ __forceinline__ int lane_id() {
    int l;
    asm volatile("v_mbcnt_lo_u32_b32 %0, -1, 0\n\tv_mbcnt_hi_u32_b32 %0, -1, %0" : "=v"(l));
    return l;
}
__device__ __forceinline__ s16x4 tr_read_l(unsigned a) { return __builtin_amdgcn_ds_read_tr16_b64_v4i16((LDS_AS s16x4*)(size_t)a); }

template <int NJ>
__device__ __forceinline__ void k1_epilogue(f32x4 (&acc)[4][NJ], char* __restrict__ ws, char* smem, int m0, int c0, int nt, int wid ENTARG) {
    const int lane = lane_id();
    const int t = wid * 64 + lane;
    const int wr = wid >> 1, wc = wid & 1;
    const int fr = lane & 15, g = lane >> 4;
    const rsrc_t rw = mk_rsrc(ws, (unsigned)WS_FLAG);
#define EPI5 1
    if (NJ == 5 && EPI5) {
        const unsigned sb0 = lds_addr(smem);
        k1_wait_rel<0>(sb0, lane, 18u);
        char* img = smem + K1_ABYTES + wid * 10240;
        if (nt < 4) {
            constexpr float qs = 0.22360679774997896f * 1.4426950408889634f;
#pragma unroll
            for (int j = 0; j < NJ; ++j)
#pragma unroll
                for (int i = 0; i < 4; ++i) {
                    f32x2 lo = {acc[i][j][0], acc[i][j][1]}, hi = {acc[i][j][2], acc[i][j][3]};
                    lo = lo * qs; hi = hi * qs;
                    acc[i][j] = (f32x4){lo[0], lo[1], hi[0], hi[1]};
                }
        }
#pragma unroll
        for (int j = 0; j < NJ; ++j) {
            const unsigned u = 4 * j + g;
            char* dst = img + fr * 40 + u * 8 + ((u * 13u) >> 6) * 2520u;
#pragma unroll
            for (int i = 0; i < 4; ++i) {
                u32x2 a = {pack_bf16(acc[i][j][0], acc[i][j][1]), pack_bf16(acc[i][j][2], acc[i][j][3])};
                *(u32x2*)(dst + i * 640) = a;
            }
        }
        CBAR();
        u32x4 v[10];
#pragma unroll
        for (int it = 0; it < 10; ++it) v[it] = *(const u32x4*)(img + it * 1024 + lane * 16);
        const int which = nt >> 2;
        const int dofs = (int)(WS_Q + (size_t)which * QKV_BYTES) + (((nt & 3) * 8 + wc * 4) * S_ + m0 + wr * 64) * 40;
        constexpr int HS = S_ * 40 - 2560;
        const int v0 = lane * 16, v1 = v0 + (lane >= 32 ? HS : 0);
#pragma unroll
        for (int it = 0; it < 10; ++it) {
            const int hh = (it / 5) * 2 + ((it % 5) >= 3 ? 1 : 0);
            __builtin_amdgcn_raw_buffer_store_b128(v[it], rw, (it % 5) == 2 ? v1 : v0, dofs + it * 1024 + hh * HS, QAUX);
        }
    } else if (NJ == 5) {
        const unsigned sb0 = lds_addr(smem);
        CBAR(); *(lflag_t)(size_t)(sb0 + K1_FLG + wid * 4) = 21u; CBAR();
        k1_wait_rel<0>(sb0, lane, 21u);
        const float qs = (nt < 4) ? 0.22360679774997896f * 1.4426950408889634f : 1.f;
#pragma unroll
        for (int j = 0; j < NJ; ++j) {
            const int n = wc * 80 + j * 16 + g * 4;
            const int hh = n / DH_, d = n % DH_;
            char* dst = smem + hh * 10240 + (wr * 64 + fr) * 40 + d * 2;
#pragma unroll
            for (int i = 0; i < 4; ++i) {
                u32x2 a = {pack_bf16(acc[i][j][0] * qs, acc[i][j][1] * qs), pack_bf16(acc[i][j][2] * qs, acc[i][j][3] * qs)};
                *(u32x2*)(dst + i * 640) = a;
            }
        }
        CBAR(); *(lflag_t)(size_t)(sb0 + K1_FLG + wid * 4) = 22u; CBAR();
        k1_wait_rel<0>(sb0, lane, 22u);
        const int which = nt >> 2;
        const int h0 = (nt & 3) * 8;
        char* dbase = ws + WS_Q + (size_t)which * QKV_BYTES + ((size_t)h0 * S_ + m0) * 40;
        const int dofs = (int)(WS_Q + (size_t)which * QKV_BYTES + ((size_t)h0 * S_ + m0) * 40);
#pragma unroll
        for (int it = 0; it < 10; ++it) {
            const int e = it * 512 + t;
            const int hh = e / 640, r = e % 640;
            const u32x4 v = *(const u32x4*)(smem + e * 16);
            bst4<QAUX>(rw, dofs + hh * (S_ * 40) + r * 16, v);
        }
    } else {
        u16* hF = (u16*)(ws + WS_H);
#pragma unroll
        for (int j = 0; j < 3; ++j) {
            const int f = c0 + wc * 48 + j * 16 + g * 4;
            const int ks = f >> 5;
            const int lq = (f & 31) >> 3;
            const int hf = f & 4;
#pragma unroll
            for (int i = 0; i < 4; ++i) {
                const int m = m0 + wr * 64 + i * 16 + fr;
                unsigned pk[2];
#pragma unroll
                for (int r = 0; r < 2; ++r) {
                    const f32x2 gt = {acc[i][j][2 * r], acc[i][j][2 * r + 1]};
                    const f32x2 up = {acc[i][(j + 3) % NJ][2 * r], acc[i][(j + 3) % NJ][2 * r + 1]};
                    f32x2 e = gt * -1.4426950408889634f;
                    e = (f32x2){__builtin_amdgcn_exp2f(e[0]), __builtin_amdgcn_exp2f(e[1])};
                    e = e + 1.f;
                    const f32x2 rc = {__builtin_amdgcn_rcpf(e[0]), __builtin_amdgcn_rcpf(e[1])};
                    const f32x2 hv = (gt * up) * rc;
                    pk[r] = pack_bf16(hv[0], hv[1]);
                }
                u32x2 a = {pk[0], pk[1]};
                u16* dst = hF + ((size_t)((m >> 4) * KS_H + ks) * 64 + (m & 15) + 16 * lq) * 8 + hf;
                bst2<HAUX>(rw, (int)WS_H + ((((m >> 4) * KS_H + ks) * 64 + (m & 15) + 16 * lq) * 8 + hf) * 2, a);
            }
        }
    }
}

template <int NJ>
__device__ __forceinline__ void k1_tile(const u16* __restrict__ X, const u16* __restrict__ W0, const u16* __restrict__ W1,
                                        char* __restrict__ ws, char* smem, int mt, int nt, int xs, int xcnt, int uw ENTARG) {
    constexpr int BN = 32 * NJ;
    constexpr int ROWB = (NJ == 5) ? 320 : 192;
    constexpr int UPR = ROWB / 16;
    constexpr int NBH = UPR / 2;
    constexpr int NB = 2 * NJ;
    constexpr int LDB = (NJ == 5) ? NQKV : F_;
    const int m0 = mt * 256;
    const int c0 = (NJ == 5) ? nt * 160 : nt * 96;
    const unsigned sb0 = lds_addr(smem);
    auto boffs = [&](int i, int lane) {
        const int p = i * 64 + lane;
        const int k = p / UPR, u = p % UPR;
        return (unsigned)((k * LDB + (((u >> 1) ^ ((k >> 3) & 1)) << 4) + (u & 1) * 8) * 2);
    };
    auto aoffs_f = [&](int lane) { return (unsigned)((lane >> 3) * (D_ * 2) + (((lane & 7) ^ (lane >> 3)) << 4)); };
    const char* xa = (const char*)(X + (size_t)m0 * D_);
    const char* wb0 = (const char*)(W0 + c0);
    const char* wb1 = (const char*)(W1 + c0);

#define KRA 0
#define KRB 0
    const int rot = (xs * KRA + ((mt & 3) * KRB >> 1)) % 10;
    if (uw >= 8) {
        const int dw = uw & 1;
        const int lane = lane_id();
        const unsigned aoffs = aoffs_f(lane);
        if (uw == 8 && lane < (K1_T0 + 32) / 4) *(lflag_t)(size_t)(sb0 + K1_FLG + lane * 4) = 0u;
        constexpr int NBP = NJ;
        unsigned vb[NBP], va[4];
#pragma unroll
        for (int i = 0; i < NBP; ++i) vb[i] = boffs((NJ == 5) ? 5 * dw + i : i, lane) - (i & 3) * 1024;
#pragma unroll
        for (int r = 0; r < 4; ++r) va[r] = aoffs + r * (8 * D_ * 2 - 1024);
        const char* xs0 = xa + dw * (128 * D_ * 2);
        const char* ws0 = (NJ == 6 && dw) ? wb1 : wb0;
        auto loadA = [&](int kt, int q0) {
            const unsigned base = sb0 + (kt % 3) * K1_ABYTES + dw * 16384;
            const int kr = kt + rot;
            const char* src = xs0 + (kr >= 10 ? kr - 10 : kr) * 128;
#pragma unroll
            for (int q = q0; q < q0 + 2; ++q) glds_x4(base + q * 4096, src + q * (32 * D_ * 2), va[0], va[1], va[2], va[3]);
        };
        auto loadB = [&](int h) {
            const unsigned base = sb0 + K1_BOFF + (h % 5) * K1_BSLOT + dw * (NBP * 1024);
            const int hr = h + 2 * rot;
            const char* s0 = ws0 + (size_t)(hr >= 20 ? hr - 20 : hr) * (32 * LDB * 2);
            glds_x4(base, s0, vb[0], vb[1], vb[2], vb[3]);
            if (NJ == 5) glds_x1(base + 4096, s0, vb[4]);
            else glds_x2(base + 4096, s0, vb[4], vb[5 % NBP]);
        };
        asm volatile("s_waitcnt lgkmcnt(0)" ::: "memory");
        __builtin_amdgcn_s_barrier();
        CBAR();
        ET(1);
        volatile LDS_AS u16* rdy = (volatile LDS_AS u16*)(size_t)(sb0 + K1_FLG + 32 + (lane & 7) * (2 * NPRD) + (uw - 8) * 2);
        loadB(1);
#pragma unroll 1
        for (int kt = 1; kt < 10; ++kt) {
            if (kt >= 3) k1_wait_rel<PSLP>(sb0, lane, 2 * kt - 4);
            loadB(2 * kt); loadA(kt, 0);
            WAIT_VM(2 * NBP + 8);
            *rdy = (u16)(2 * kt - 1);
            loadA(kt, 2);
            WAIT_VM(NBP + 16);
            *rdy = (u16)(2 * kt);
            if (kt >= 2) k1_wait_rel<PSLP>(sb0, lane, 2 * kt - 3);
            loadB(2 * kt + 1);
        }
        WAIT_VM(NBP);
        *rdy = (u16)19;
        WAIT_VM(0);
        *rdy = (u16)30;
        ET(4); ETW();
        return;
    }

    ET(1);
    const int lane = lane_id();
    {
        const unsigned aoffs = aoffs_f(lane);
        wb0 += (size_t)rot * (64 * LDB * 2); wb1 += (size_t)rot * (64 * LDB * 2);
        glds_x4(sb0 + uw * 4096, xa + uw * (32 * D_ * 2) + rot * 128, aoffs, aoffs + (8 * D_ * 2 - 1024), aoffs + 2 * (8 * D_ * 2 - 1024), aoffs + 3 * (8 * D_ * 2 - 1024));
        if (NJ == 5) {
            if (uw < 5) glds_x2(sb0 + K1_BOFF + uw * 2048, wb0, boffs(2 * uw, lane), boffs(2 * uw + 1, lane) - 1024);
        } else {
            if (uw < 6) {
                const unsigned v = boffs(uw, lane);
                glds_x1(sb0 + K1_BOFF + uw * 1024, wb0, v);
                glds_x1(sb0 + K1_BOFF + 6144 + uw * 1024, wb1, v);
            }
        }
    }
    ET(2);
    __builtin_amdgcn_s_barrier();
    CBAR();
    const int wr = uw >> 1, wc = uw & 1;
    const int fr = lane & 15, g = lane >> 4;
    f32x4 acc[4][NJ];
#pragma unroll
    for (int i = 0; i < 4; ++i)
#pragma unroll
        for (int j = 0; j < NJ; ++j) acc[i][j] = (f32x4){0.f, 0.f, 0.f, 0.f};

    unsigned aa[2][2], be, bo;
    {
        const int row = wr * 64 + fr;
        aa[0][0] = sb0 + row * 128 + ((g ^ (row & 7)) << 4);
        aa[1][0] = sb0 + row * 128 + (((4 + g) ^ (row & 7)) << 4);
        aa[0][1] = aa[0][0] + 2 * K1_ABYTES;
        aa[1][1] = aa[1][0] + 2 * K1_ABYTES;
        const int q4 = (lane & 15) >> 2, p4 = lane & 3;
        const unsigned bb = sb0 + K1_BOFF + (8 * g + q4) * ROWB + p4 * 8 + wc * (NJ == 5 ? 5 : 3) * 32;
        const int sx = ((g & 1) << 5) * (wc ? -1 : 1);
        be = bb + sx; bo = bb - sx;
    }
    unsigned frel = sb0 + K1_FLG + uw * 4;
    unsigned frdy = sb0 + K1_FLG + 32 + uw * (2 * NPRD) + (lane & (NPRD - 1)) * 2;
    asm volatile("" : "+v"(aa[0][0]), "+v"(aa[1][0]), "+v"(aa[0][1]), "+v"(aa[1][1]), "+v"(be), "+v"(bo), "+v"(frel), "+v"(frdy));
    WAIT_VM(0);
    ET(3);
    *(lflag_t)(size_t)(frel + K1_T0) = 1u;
    {
        lflag_t p = (lflag_t)(size_t)(sb0 + K1_FLG + K1_T0 + (lane & 7) * 4);
        while (__builtin_amdgcn_ballot_w64(*p == 0u) != 0ull) {}
    }
    CBAR();
    ET(4);
    bf16x8 af[2][4], bq[NJ];
    auto rdA = [&](int h, int i) {
        const int st = (h >> 1) % 3;
        return *(const LDS_AS bf16x8*)(size_t)(aa[h & 1][st == 2] + (st == 1 ? K1_ABYTES : 0) + i * 2048);
    };
    auto rdB = [&](int h, int j) {
        const int jj = (NJ == 5) ? j : j % 3, sub = (NJ == 5) ? 0 : (j / 3) * 6144;
        const unsigned b = ((jj & 1) ? bo : be) + (h % 5) * K1_BSLOT + jj * 32 + sub;
        s16x4 lo = tr_read_l(b);
        s16x4 hi = tr_read_l(b + 4 * ROWB);
        return cat4(lo, hi);
    };
    auto rd_rdy = [&]() { return (unsigned)*(volatile LDS_AS u16*)(size_t)frdy; };
#define WAIT_RDY(fl, need) asm volatile("v_cmp_gt_u32 vcc, %2, %0\n\ts_cbranch_vccz L_ok_%=\n" \
        "L_spin_%=:\n\tds_read_u16 %0, %1\n\ts_waitcnt lgkmcnt(0)\n\tv_cmp_gt_u32 vcc, %2, %0\n\ts_cbranch_vccnz L_spin_%=\n" \
        "L_ok_%=:" : "+v"(fl) : "v"(frdy), "n"(need) : "vcc", "memory")
    auto release = [&](unsigned n) { CBAR(); *(lflag_t)(size_t)frel = n; CBAR(); };
#pragma unroll
    for (int i = 0; i < 4; ++i) af[0][i] = rdA(0, i);
    __builtin_amdgcn_sched_barrier(0);
#pragma unroll
    for (int j = 0; j < NJ; ++j) { bq[j] = rdB(0, j); __builtin_amdgcn_sched_barrier(0); }
    release(1u);
    unsigned fl = rd_rdy();
    __builtin_amdgcn_sched_barrier(0);
#pragma unroll
    for (int p = 0; p < 20; ++p) {
#pragma unroll
        for (int sl = 0; sl < 4 * NJ; ++sl) {
            const int j = sl >> 2, i = sl & 3;
            MF1(acc[i][j], bq[j], af[p & 1][i]);
            __builtin_amdgcn_sched_barrier(0);
            if (sl == 0 && p > 0) bq[NJ - 1] = rdB(p, NJ - 1);
            if (sl == 1 && p > 0 && p < (NJ == 5 ? 18 : 16)) release(p + 1);
            if (p < 19) {
                if (sl == 2) WAIT_RDY(fl, p + 2);
                if (sl >= 4 && i == 0) bq[j - 1] = rdB(p + 1, j - 1);
                if (sl == 5) af[(p + 1) & 1][0] = rdA(p + 1, 0);
                if (sl == 6) af[(p + 1) & 1][1] = rdA(p + 1, 1);
                if (sl == 7) af[(p + 1) & 1][2] = rdA(p + 1, 2);
                if (sl == 9) af[(p + 1) & 1][3] = rdA(p + 1, 3);
                if (sl == 4 * NJ - K1FS && p < 18) fl = rd_rdy();
            }
            __builtin_amdgcn_sched_barrier(0);
        }
    }

    k1_epilogue<NJ>(acc, ws, smem, m0, c0, nt, uw ENTPASS);
    ET(5); ETW();
}

#define AUXM 0
#define AUXS 12
#define K1NW 12
template <int NU, int NR, bool EARLY>
__device__ __forceinline__ void k1_aux(const u16* __restrict__ X, const u16* __restrict__ Wo, const u16* __restrict__ Wd,
                                       const u16* __restrict__ gate_w, char* __restrict__ ws, char* wl,
                                       int u0, int us, int ue, int rb, int r0, int rs, int re) {
    const int lane = lane_id();
    float* gout = (float*)(ws + WS_G);
    const int q4 = (lane & 15) >> 2, p4 = lane & 3, g = lane >> 4;
    const rsrc_t rw = mk_rsrc(ws, (unsigned)WS_FLAG);
    auto fetch = [&](int unit, u32x4 (&r)[4]) {
        const int ksg = unit / 10, cc = unit % 10;
#define OPERM 1
        const int e = lane >> 3;
#pragma unroll
        for (int i = 0; i < 4; ++i) {
            const int G = ksg * 4 + i, P = G / 5, gi = G % 5;
            const int ro = (gi < 4) ? (2 * P + (gi >> 1)) * DH_ + (e >> 2) * 8 + 4 * (gi & 1) + (e & 3) : (2 * P + (e >> 2)) * DH_ + 16 + (e & 3);
            const u16* src = (ksg < KS_O) ? Wo + (size_t)ro * D_ : Wd + (size_t)((ksg - KS_O) * 32 + i * 8 + e) * D_;
            r[i] = *(const u32x4*)(src + cc * 64 + (lane & 7) * 8);
        }
    };
    auto emit = [&](int unit, const u32x4 (&r)[4]) {
        const int ksg = unit / 10, cc = unit % 10;
#pragma unroll
        for (int hf = 0; hf < 2; ++hf) {
            CBAR();
            if (((lane >> 2) & 1) == hf) {
#pragma unroll
                for (int i = 0; i < 4; ++i) *(u32x4*)(wl + (i * 8 + (lane >> 3)) * 64 + (lane & 3) * 16) = r[i];
            }
            CBAR();
#pragma unroll
            for (int j = 0; j < 2; ++j) {
                const char* a = wl + (8 * g + q4) * 64 + j * 32 + p4 * 8;
                s16x4 lo = tr_read(a);
                s16x4 hi = tr_read(a + 4 * 64);
                bf16x8 v = cat4(lo, hi);
                const int nt = cc * 4 + hf * 2 + j;
                const int dst = (ksg < KS_O) ? (int)WS_WO + ((nt * KS_O + ksg) * 64 + lane) * 16
                                             : (int)WS_WD + ((nt * KS_H + (ksg - KS_O)) * 64 + lane) * 16;
#define WAUX 0
                bst4<WAUX>(rw, dst, __builtin_bit_cast(u32x4, v));
            }
        }
    };
    u32x4 r[NU ? NU : 1][4];
    u32x4 x0[NR ? NR : 1], x1[NR ? NR : 1], w0, w1;
    auto gfetch = [&]() {
        w0 = *(const u32x4*)(gate_w + lane * 8);
        w1 = *(const u32x4*)(gate_w + 512 + (lane & 15) * 8);
#pragma unroll
        for (int i = 0; i < NR; ++i) {
            int row = r0 + rs * i;
            row = rb + (row < re ? row : r0);
            x0[i] = *(const u32x4*)(X + (size_t)row * D_ + lane * 8);
            x1[i] = *(const u32x4*)(X + (size_t)row * D_ + 512 + (lane & 15) * 8);
        }
    };
    if (NU) {
        const bool extra = u0 + (NU - 1) * us < ue;
#pragma unroll
        for (int u = 0; u < NU - 1; ++u) fetch(u0 + u * us, r[u]);
        if (extra) fetch(u0 + (NU - 1) * us, r[NU - 1]);
        if (EARLY && NR) gfetch();
#pragma unroll
        for (int u = 0; u < NU - 1; ++u) emit(u0 + u * us, r[u]);
        if (extra) emit(u0 + (NU - 1) * us, r[NU - 1]);
    }
    CBAR();
    if (NR == 0) return;
    if (!EARLY || !NU) gfetch();
    if (lane >= 16) w1 = (u32x4){0u, 0u, 0u, 0u};
    float sacc[NR ? NR : 1];
#pragma unroll
    for (int i = 0; i < NR; ++i) {
        float a = 0.f;
#pragma unroll
        for (int e = 0; e < 4; ++e) {
            const unsigned xa = x0[i][e], wa = w0[e], xb = x1[i][e], wb = w1[e];
            a = __builtin_amdgcn_fdot2_f32_bf16(__builtin_bit_cast(bf2_t, xa), __builtin_bit_cast(bf2_t, wa), a, false);
            a = __builtin_amdgcn_fdot2_f32_bf16(__builtin_bit_cast(bf2_t, xb), __builtin_bit_cast(bf2_t, wb), a, false);
        }
        sacc[i] = a;
    }
    {
        float z = 0.f;
#pragma unroll
        for (int i = 0; i < NR; ++i) {
            const float tot = wave_sum(sacc[i]);
            z = (lane == i) ? tot : z;
        }
        const int row = r0 + rs * lane;
        z = rbf(z);
        if (lane < NR && row < re) gout[rb + row] = rbf(1.f / (1.f + __expf(-z)));
    }
}

__global__ __launch_bounds__(K1NW * 64) void k1_gemm(const u16* __restrict__ X, const u16* __restrict__ Wqkv,
                                               const u16* __restrict__ Wg, const u16* __restrict__ Wu,
                                               const u16* __restrict__ Wo, const u16* __restrict__ Wd,
                                               const u16* __restrict__ gate_w, char* __restrict__ ws) {
    __shared__ __attribute__((aligned(16))) char smem[K1_LDS];
    const int bid = blockIdx.x;
    const int uw = __builtin_amdgcn_readfirstlane(threadIdx.x >> 6);
#define CPE 1
    {
        unsigned o, d;
        asm volatile("s_cmp_lg_u32 %2, 10\n\ts_cbranch_scc1 L_cp_%=\n\ts_cmp_ge_u32 %3, 16\n\ts_cbranch_scc1 L_cp_%=\n\t"
                     "s_getpc_b64 s[20:21]\n\ts_add_u32 s20, s20, _Z7k1_gemmPKtS0_S0_S0_S0_S0_S0_Pc@rel32@lo+4\n\ts_addc_u32 s21, s21, _Z7k1_gemmPKtS0_S0_S0_S0_S0_S0_Pc@rel32@hi+12\n\t"
                     "v_and_b32 %0, 63, %4\n\tv_lshlrev_b32 %0, 7, %0\n\tglobal_load_dword %1, %0, s[20:21]\n\t"
                     "v_add_u32 %0, 0x2000, %0\n\tglobal_load_dword %1, %0, s[20:21]\n\t"
                     "v_add_u32 %0, 0x2000, %0\n\tglobal_load_dword %1, %0, s[20:21]\n\t"
                     "v_add_u32 %0, 0x2000, %0\n\tv_min_u32 %0, 0x7300, %0\n\tglobal_load_dword %1, %0, s[20:21]\n\t"
                     "s_waitcnt vmcnt(0)\n\ts_endpgm\nL_cp_%=:"
                     : "=&v"(o), "=&v"(d) : "s"(uw), "s"(bid), "v"(threadIdx.x) : "s20", "s21", "scc", "memory");
    }
    const int xcd = bid & 7, idx = bid >> 3;
    const int ng = xcd >> 1, mg = xcd & 1;
    const int cnt = (ng < 2) ? 8 : 7;
#define NBAL 1
    const int nstart = ((idx >> 2) < 3) ? ng * 3 : ((ng < 2) ? 9 + ng * 5 : 11 + ng * 4);
#define CPF (CPE ? 2 : 3)
#define CPB 2
#define CPP 12
#define CPX 1
    if (CPF && __builtin_expect(uw >= 10 && idx < CPB, CPX)) {
        const int ln = lane_id();
        unsigned l1, h1, l2, h2;
        asm volatile("s_getpc_b64 s[20:21]\n\ts_add_u32 %0, s20, _Z7k1_gemmPKtS0_S0_S0_S0_S0_S0_Pc@rel32@lo+4\n\ts_addc_u32 %1, s21, _Z7k1_gemmPKtS0_S0_S0_S0_S0_S0_Pc@rel32@hi+12\n\t"
                     "s_getpc_b64 s[20:21]\n\ts_add_u32 %2, s20, _Z6k2_mixPcPt@rel32@lo+4\n\ts_addc_u32 %3, s21, _Z6k2_mixPcPt@rel32@hi+12"
                     : "=&s"(l1), "=&s"(h1), "=&s"(l2), "=&s"(h2) :: "s20", "s21", "scc");
        const unsigned long long a1 = ((unsigned long long)h1 << 32) | l1, a2 = ((unsigned long long)h2 << 32) | l2;
        unsigned d = 0;
        if (uw == 10) {
            if (CPF & 1) {
                const unsigned len = (a2 > a1 && a2 - a1 < 65536ull) ? (unsigned)(a2 - a1) : 0u;
#pragma unroll
                for (int q = 0; q < 4; ++q) {
                    const unsigned o = (unsigned)(q * 64 + ln) * 128u;
                    if (o < len) asm volatile("global_load_dword %0, %1, %2" : "+v"(d) : "v"(o), "s"(a1) : "memory");
                }
            }
        } else if (CPF & 2) {
            if (CPP) {
                __builtin_amdgcn_s_barrier();
                CBAR();
                k1_wait_rel<4>(lds_addr(smem), ln, CPP);
            }
#pragma unroll
            for (int q = 0; q < 2; ++q) {
                const unsigned o = (unsigned)(q * 64 + ln) * 128u;
                if (o < 13824u) asm volatile("global_load_dword %0, %1, %2" : "+v"(d) : "v"(o), "s"(a2) : "memory");
            }
        }
        asm volatile("s_waitcnt vmcnt(0)" : "+v"(d) :: "memory");
        return;
    }
    if (idx >= 4 * cnt) {
        const int sbk = (xcd - 4) * 4 + (idx - 28);
        if (threadIdx.x < 32)
            *(u32x4*)(ws + WS_FLAG + (size_t)(sbk * 2 + (threadIdx.x >> 4)) * FLAG_LD + (threadIdx.x & 15) * 16) = (u32x4){0u, 0u, 0u, 0u};
        constexpr int NWV = 16 * K1NW;
        const int gw = sbk * K1NW + uw, wq = (((xcd - 4) >> 1) * 4 + (idx - 28)) * K1NW + uw;
        constexpr int NRS = (1024 + NWV / 2 - 1) / (NWV / 2);
        if (AUXM == 0) k1_aux<(740 + NWV - 1) / NWV, NRS, false>(X, Wo, Wd, gate_w, ws, smem + uw * 2048, gw, NWV, 740, mg * 1024, wq, NWV / 2, 1024);
        if (AUXM == 1) k1_aux<1, 0, false>(X, Wo, Wd, gate_w, ws, smem + uw * 2048, 720 + uw * 16 + sbk, 0, 740, 0, 0, 0, 0);
        if (AUXM == 2) k1_aux<1, NRS, true>(X, Wo, Wd, gate_w, ws, smem + uw * 2048, 720 + uw * 16 + sbk, 0, 740, mg * 1024, wq, NWV / 2, 1024);
        if (AUXM == 3) k1_aux<(740 + NWV - 1) / NWV, 0, false>(X, Wo, Wd, gate_w, ws, smem + uw * 2048, gw, NWV, 740, mg * 1024, wq, NWV / 2, 1024);
        if (AUXM == 4) k1_aux<(260 + NWV - 1) / NWV, 0, false>(X, Wo, Wd, gate_w, ws, smem + uw * 2048, 480 + gw, NWV, 740, mg * 1024, wq, NWV / 2, 1024);
        return;
    }
    const int mt = mg * 4 + (idx & 3);
    const int nt = nstart + (idx >> 2);
#define L2PF 0
#define PFD 0
#define PFK 2
#define PFS 0
    if (uw >= 8 + NPRD) {
        if (AUXM == 0 || uw > 10) return;
        __builtin_amdgcn_s_barrier();
        CBAR();
        k1_wait_rel<4>(lds_addr(smem), lane_id(), AUXS);
        const int lin = mt * 30 + nt;
        if (AUXM == 1) k1_aux<3, 9, true>(X, Wo, Wd, gate_w, ws, smem + K1_AUX, lin, 240, 720, mt * 256, nt, 30, 256);
        if (AUXM == 2) k1_aux<3, 0, true>(X, Wo, Wd, gate_w, ws, smem + K1_AUX, lin, 240, 720, mt * 256, nt, 30, 256);
        if (AUXM == 3) k1_aux<0, 9, true>(X, Wo, Wd, gate_w, ws, smem + K1_AUX, lin, 240, 720, mt * 256, nt, 30, 256);
        if (AUXM == 4) k1_aux<2, 9, true>(X, Wo, Wd, gate_w, ws, smem + K1_AUX, lin, 240, 480, mt * 256, nt, 30, 256);
        return;
    }
    if (nt < 12) k1_tile<5>(X, Wqkv, Wqkv, ws, smem, mt, nt, idx >> 2, cnt, uw ENTPASS);
    else k1_tile<6>(X, Wg, Wu, ws, smem, mt, nt - 12, idx >> 2, cnt, uw ENTPASS);
}

typedef unsigned long long u64;
#define GDELAY 10
#define PF2 0
#define ACQ 0
#define ROT 0
#define ROLL 0
#define GPRIO 0
#define POLLSLP 4
#define APRIO 2
#define ST2(i) do {} while (0)
#define ST2ID() do {} while (0)
__device__ __forceinline__ void st_agent(void* p, u32x2 v) {
    __hip_atomic_store((u64*)p, __builtin_bit_cast(u64, v), __ATOMIC_RELAXED, __HIP_MEMORY_SCOPE_AGENT);
}
template <int AUX> __device__ __forceinline__ bf16x8 bld(rsrc_t r, int voff, int soff) {
    return __builtin_bit_cast(bf16x8, __builtin_amdgcn_raw_buffer_load_b128(r, voff, soff, AUX));
}

__device__ __forceinline__ float fmx(float a, float b) { return __builtin_elementwise_maximum(a, b); }

typedef float f32x16 __attribute__((ext_vector_type(16)));
#define MF32(a, b, c) __builtin_amdgcn_mfma_f32_32x32x16_bf16(a, b, c, 0, 0, 0)
#define MF8(a, b, c) __builtin_amdgcn_mfma_f32_32x32x8bf16_1k(a, b, c, 0, 0, 0)
constexpr int AT_V = 15360, AT_PAD = 30720, AT_ONE = AT_PAD + 64;
constexpr int AT_LDS = AT_ONE + 35 * 320 + 16;
template <bool EDGE>
__device__ __forceinline__ void k2_attn_wave(char* __restrict__ oF, const char* smem, const bf16x8 q1, const s16x4 q2,
                                             const float gg, int h, int qw, int rbase, int lane) {
    const int hi = lane >> 5, ql = lane & 31;
    const float NEG = -__builtin_inff();
    const int ts = 8 - (qw >> 5);
    const char* kb = smem + (rbase + ql) * 40;
    const char* ka = kb + hi * 16;
    const char* vb;
    {
        const int q4 = (lane & 15) >> 2, p4 = lane & 3, db = (lane >> 4) & 1;
        vb = smem + AT_V + (rbase + 4 * hi + q4) * 40 + db * 32 + p4 * 8;
        if (db == 1 && p4 >= 1) vb = smem + AT_ONE + (p4 >> 1) * 8;
    }
    auto rd16 = [&](const char* p) {
        const u32x2 lo = *(const u32x2*)p, up = *(const u32x2*)(p + 8);
        const u32x4 v = {lo[0], lo[1], up[0], up[1]};
        return __builtin_bit_cast(bf16x8, v);
    };
    auto rdK1 = [&](int t) { return rd16(ka + t * 1280); };
    auto rdK2 = [&](int t) { return *(const s16x4*)(kb + t * 1280 + 32); };
    auto rdV = [&](int t, int a) {
        s16x4 lo = tr_read(vb + t * 1280 + a * 640);
        s16x4 up = tr_read(vb + t * 1280 + a * 640 + 320);
        return cat4(lo, up);
    };
    f32x16 zero;
#pragma unroll
    for (int i = 0; i < 16; ++i) zero[i] = 0.f;
#define SB0() __builtin_amdgcn_sched_barrier(0)
    auto TL = [](int u) { return u < 7 ? u + 1 : (u == 7 ? 0 : 8); };
    float mx = NEG, my = NEG;
    f32x16 s[3];
    bf16x8 k1[2], vf[2][2];
    s16x4 k2[2];
    k1[0] = rdK1(TL(0)); k2[0] = rdK2(TL(0)); k1[1] = rdK1(TL(1)); k2[1] = rdK2(TL(1));
    SB0();
#pragma unroll
    for (int u = -1; u < 9; ++u) {
        if (u + 1 <= 8) {
            s[(u + 1) % 3] = MF32(k1[(u + 1) & 1], q1, zero);
            SB0();
            if (u + 3 <= 8) k1[(u + 1) & 1] = rdK1(TL(u + 3));
            else if (u + 3 == 9) k1[(u + 1) & 1] = rdK1(1);
            else k1[(u + 1) & 1] = rdK1(2);
        }
        SB0();
        const int p = u - 1;
        if (p >= 0 && p < 7 && (!EDGE || p + 1 >= ts)) {
            const f32x16 x = s[p % 3];
            mx = fmx(fmx(mx, x[0]), x[1]); my = fmx(fmx(my, x[2]), x[3]);
            mx = fmx(fmx(mx, x[4]), x[5]); my = fmx(fmx(my, x[6]), x[7]);
        }
        SB0();
        if (u >= 0) {
            s[u % 3] = MF8(k2[u & 1], q2, s[u % 3]);
            SB0();
            if (u + 2 <= 8) k2[u & 1] = rdK2(TL(u + 2));
            else if (u + 2 == 9) k2[u & 1] = rdK2(1);
            else k2[u & 1] = rdK2(2);
        }
        SB0();
        if (p >= 0 && p < 7 && (!EDGE || p + 1 >= ts)) {
            const f32x16 x = s[p % 3];
            mx = fmx(fmx(mx, x[8]), x[9]); my = fmx(fmx(my, x[10]), x[11]);
            mx = fmx(fmx(mx, x[12]), x[13]); my = fmx(fmx(my, x[14]), x[15]);
        }
        SB0();
    }
    vf[0][0] = rdV(8, 0); vf[0][1] = rdV(8, 1);
    if (!EDGE) { vf[1][0] = rdV(0, 0); vf[1][1] = rdV(0, 1); }
    const int z = ql - 4 * hi;
    f32x16 mg;
#pragma unroll
    for (int i = 0; i < 16; ++i) {
        const bool c = 8 * (i >> 2) + (i & 3) > z;
        mg[i] = c ? (EDGE ? NEG : s[1][i]) : s[2][i];
    }
#pragma unroll
    for (int i = 0; i < 16; i += 4) { mx = fmx(fmx(mx, mg[i]), mg[i + 1]); my = fmx(fmx(my, mg[i + 2]), mg[i + 3]); }
    mx = fmx(mx, my);
    {
        const unsigned mi = __builtin_bit_cast(unsigned, mx);
        auto r = __builtin_amdgcn_permlane32_swap(mi, mi, false, false);
        mx = fmx(__builtin_bit_cast(float, (unsigned)r[0]), __builtin_bit_cast(float, (unsigned)r[1]));
    }
    const float nm = -mx;
    f32x16 cm;
#pragma unroll
    for (int i = 0; i < 16; ++i) cm[i] = nm;
    SB0();
    f32x16 o = zero;
    s[1] = MF32(k1[1], q1, cm);
    SB0();
    k1[1] = rdK1(3);
    {
        u32x4 pa, pb;
#pragma unroll
        for (int a = 0; a < 2; ++a) {
#pragma unroll
            for (int e = 0; e < 4; ++e) {
                const int i = 8 * a + 2 * e;
                const float x0 = __builtin_amdgcn_exp2f(mg[i] + nm), x1 = __builtin_amdgcn_exp2f(mg[i + 1] + nm);
                const unsigned pk = pack_bf16(x0, x1);
                if (EDGE) pa[e] = pk;
                else {
                    const bool c0 = 8 * (i >> 2) + (i & 3) > z, c1 = 8 * (i >> 2) + (i & 3) + 1 > z;
                    pb[e] = pack_bf16(c0 ? x0 : 0.f, c1 ? x1 : 0.f);
                    pa[e] = pk ^ pb[e];
                }
                if (a == 0 && e == 1) { SB0(); s[1] = MF8(k2[1], q2, s[1]); SB0(); k2[1] = rdK2(3); }
            }
            SB0();
            o = MF32(vf[0][a], __builtin_bit_cast(bf16x8, pa), o);
            SB0();
            if (!EDGE) {
                if (a == 1) { s[0] = MF32(k1[0], q1, cm); SB0(); k1[0] = rdK1(4); }
                o = MF32(vf[1][a], __builtin_bit_cast(bf16x8, pb), o);
                SB0();
            } else if (a == 1) { s[0] = MF32(k1[0], q1, cm); SB0(); k1[0] = rdK1(4); }
            vf[1][a] = rdV(1, a);
            vf[0][a] = rdV(2, a);
            SB0();
        }
    }
    u32x4 pk;
    float xa, xb;
#pragma unroll
    for (int t = 1; t < 8; ++t) {
        const bool on = !EDGE || t >= ts;
        auto EX = [&](int i) { xa = __builtin_amdgcn_exp2f(s[t & 1][i]); xb = __builtin_amdgcn_exp2f(s[t & 1][i + 1]); };
        float ya, yb;
        if (t < 7) { s[(t + 1) & 1] = MF8(k2[(t + 1) & 1], q2, s[(t + 1) & 1]); SB0(); if (t + 3 < 8) k2[(t + 1) & 1] = rdK2(t + 3); }
        SB0();
        if (on) {
            EX(0); SB0();
            ya = xa; yb = xb; EX(2); pk[0] = pack_bf16(ya, yb); SB0();
            ya = xa; yb = xb; EX(4); pk[1] = pack_bf16(ya, yb); SB0();
            ya = xa; yb = xb; EX(6); pk[2] = pack_bf16(ya, yb); SB0();
            ya = xa; yb = xb; EX(8); pk[3] = pack_bf16(ya, yb); SB0();
            ya = xa; yb = xb; EX(10);
            const unsigned n0 = pack_bf16(ya, yb); SB0();
            o = MF32(vf[t & 1][0], __builtin_bit_cast(bf16x8, pk), o);
            SB0();
            if (t + 2 < 8) vf[t & 1][0] = rdV(t + 2, 0);
            u32x4 pn;
            pn[0] = n0;
            ya = xa; yb = xb; EX(12); pn[1] = pack_bf16(ya, yb); SB0();
            ya = xa; yb = xb; EX(14); pn[2] = pack_bf16(ya, yb); SB0();
            if (!EDGE && t + 2 < 8) { s[t & 1] = MF32(k1[t & 1], q1, cm); SB0(); if (t + 4 < 8) k1[t & 1] = rdK1(t + 4); }
            pn[3] = pack_bf16(xa, xb); SB0();
            o = MF32(vf[t & 1][1], __builtin_bit_cast(bf16x8, pn), o);
            SB0();
            if (t + 2 < 8) vf[t & 1][1] = rdV(t + 2, 1);
        } else if (t + 2 < 8) { vf[t & 1][0] = rdV(t + 2, 0); vf[t & 1][1] = rdV(t + 2, 1); }
        SB0();
        if (EDGE && t + 2 < 8) { s[t & 1] = MF32(k1[t & 1], q1, cm); SB0(); if (t + 4 < 8) k1[t & 1] = rdK1(t + 4); }
        SB0();
    }
    float sum;
    {
        const float o8 = o[8];
        const unsigned si = __float_as_uint(o8);
        auto r = __builtin_amdgcn_permlane32_swap(si, si, false, false);
        sum = __builtin_bit_cast(float, (unsigned)r[1]);
    }
    const float inv = gg * __builtin_amdgcn_rcpf(sum);
    const int m = qw + ql;
    {
        const rsrc_t ro = mk_rsrc(oF, (unsigned)(S_ * D_ * 2));
        const int ob = ((m >> 4) * KS_O * 64 + (m & 15)) * 16 + (h >> 1) * 1280;
        const u32x4 a = {pack_bf16(o[0] * inv, o[1] * inv), pack_bf16(o[2] * inv, o[3] * inv), pack_bf16(o[4] * inv, o[5] * inv), pack_bf16(o[6] * inv, o[7] * inv)};
        bst4<AUX_AGENT>(ro, ob + (2 * (h & 1) + hi) * 256, a);
        const u32x2 b = {pack_bf16(o[8] * inv, o[9] * inv), pack_bf16(o[10] * inv, o[11] * inv)};
        if (hi == 0) bst2<AUX_AGENT>(ro, ob + 1024 + (h & 1) * 8, b);
    }
}

__device__ __forceinline__ void k2_attn(char* __restrict__ ws, char* smem, int bid) {
    const int h = (bid & 7) + 8 * ((bid >> 3) & 3);
    const int qb = bid >> 5;
    const int q0 = qb * 128;
    const int t = threadIdx.x, lane = t & 63;
    const int w = __builtin_amdgcn_readfirstlane(t >> 6);
    const char* Qb = ws + WS_Q + (size_t)h * S_ * 40;
    const char* Kb = ws + WS_K + (size_t)h * S_ * 40;
    const char* Vb = ws + WS_V + (size_t)h * S_ * 40;
    char* sK = smem;
    char* sV = smem + AT_V;

    ST2(0); ST2ID();
    if (APRIO) __builtin_amdgcn_s_setprio(APRIO);
#pragma unroll
    for (int i = 0; i < 4; ++i) {
        const int p = i * 256 + t;
        if (i < 3 || w < 3) {
            int off = (q0 - 256) * 40 + p * 16;
            off = off < 0 ? 0 : off;
            glds16(Kb + off, sK + p * 16);
            glds16(Vb + off, sV + p * 16);
        }
    }
    if (t < 4) *(u32x4*)(smem + AT_PAD + t * 16) = (u32x4){0u, 0u, 0u, 0u};
    if (t >= 64 && t < 100) *(u32x4*)(smem + AT_ONE + (t - 64) * 320) = (u32x4){0x3f80u, 0u, 0u, 0u};
    const int hi = lane >> 5;
    const int qw = q0 + 32 * w;
    bf16x8 q1;
    s16x4 q2;
    {
        const char* qp = Qb + (size_t)(qw + (lane & 31)) * 40;
        const u32x2 a = *(const u32x2*)(qp + hi * 16), b = *(const u32x2*)(qp + hi * 16 + 8);
        u32x2 c = *(const u32x2*)(qp + 32);
        if (hi) c = (u32x2){0u, 0u};
        const u32x4 v1 = {a[0], a[1], b[0], b[1]};
        q1 = __builtin_bit_cast(bf16x8, v1); q2 = __builtin_bit_cast(s16x4, c);
    }
    const float gg = ((const float*)(ws + WS_G))[qw + (lane & 31)];
    __syncthreads();
    ST2(1);
    char* oF = ws + WS_O;
    if (qb >= 2) k2_attn_wave<false>(oF, smem, q1, q2, gg, h, qw, 32 * w, lane);
    else k2_attn_wave<true>(oF, smem, q1, q2, gg, h, qw, 32 * w, lane);
    ST2(2);
    asm volatile("s_waitcnt vmcnt(0)" ::: "memory");
    ST2(3);
    if (lane == 0)
        __hip_atomic_store((unsigned*)(ws + WS_FLAG + (qw >> 6) * FLAG_LD) + h * 2 + ((qw >> 5) & 1), 1u, __ATOMIC_RELAXED, __HIP_MEMORY_SCOPE_AGENT);
}

__device__ __forceinline__ void k2_gemm(char* __restrict__ ws, u16* __restrict__ out, char* smem, int bid) {
    const int xcd = bid & 7, idx = bid >> 3;
    const int mt = (xcd >> 1) * 8 + (idx & 7);
    const int nt = (xcd & 1) * 4 + (idx >> 3);
    const int t = threadIdx.x, lane = t & 63;
    const int w = __builtin_amdgcn_readfirstlane(t >> 6);
    const int fr = lane & 15, g = lane >> 4;
    const int nsteps = (w < 3) ? 14 : 12;
    const rsrc_t rs = __builtin_amdgcn_make_buffer_rsrc(ws, 0, (int)WS_FLAG, 0x00020000);
    const int vo = lane * 16;
    int ap[4];
#pragma unroll
#define K2SAME 0
    for (int i = 0; i < 4; ++i) ap[i] = (int)WS_H + ((((K2SAME & 1) ? 0 : (K2SAME & 4) ? (mt & 24) : mt) * 4 + (i ^ w)) * KS_H + 14 * w) * 1024;
    int bp = (int)WS_WD + ((((K2SAME & 2) ? 0 : nt) * 5) * KS_H + 14 * w) * 1024;
#define RTA 0
#define RTB 0
    int cur = ((RTA * (nt & 3) + (RTB * (mt & 7) >> 2)) % nsteps) * 1024;
    auto adv = [&]() { cur += 1024; cur = (cur == nsteps * 1024) ? 0 : cur; };

    f32x4 acc[4][5];
#pragma unroll
    for (int i = 0; i < 4; ++i)
#pragma unroll
        for (int j = 0; j < 5; ++j) acc[i][j] = (f32x4){0.f, 0.f, 0.f, 0.f};
    bf16x8 a0[4], b0[5], a1[4], b1[5];
#define SB() __builtin_amdgcn_sched_barrier(0)
#define MFA(c, b, a) asm volatile("v_mfma_f32_16x16x32_bf16 %0, %1, %2, %0" : "+v"(c) : "v"(b), "v"(a))
    const u32x4 rq = {(unsigned)(size_t)ws, (unsigned)((size_t)ws >> 32) & 0xffffu, (unsigned)WS_FLAG, 0x00020000u};
#define LDN(r, so) asm volatile("buffer_load_dwordx4 %0, %1, %2, %3 offen" : "=v"(r) : "v"(vo), "s"(rq), "s"(so) : "memory")
#define LDR(r, so) asm volatile("buffer_load_dwordx4 %0, %1, %2, %3 offen" : "+v"(r) : "v"(vo), "s"(rq), "s"(so) : "memory")
#define LDC(r, so) asm volatile("buffer_load_dwordx4 %0, %1, %2, %3 offen sc1" : "+v"(r) : "v"(vo), "s"(rq), "s"(so) : "memory")
    auto ld = [&](bf16x8 (&a)[4], bf16x8 (&b)[5]) {
#pragma unroll
        for (int j = 0; j < 4; ++j) LDN(b[j], bp + j * KS_H * 1024 + cur);
#pragma unroll
        for (int i = 0; i < 4; ++i) LDN(a[i], ap[i] + cur);
        LDN(b[4], bp + 4 * KS_H * 1024 + cur);
        adv();
    };
    auto mmx = [&](auto w0, auto w4, bf16x8 (&a)[4], bf16x8 (&b)[5], auto fa, auto fb) {
        constexpr int W0 = decltype(w0)::value, W4 = decltype(w4)::value;
#pragma unroll
        for (int j = 0; j < 5; ++j) {
#pragma unroll
            for (int i = 0; i < 4; ++i) {
                if (j == 0) { if (i == 0) WAIT_VM(W0); if (i == 1) WAIT_VM(W0 - 1); if (i == 2) WAIT_VM(W0 - 2); if (i == 3) WAIT_VM(W0 - 3); }
                if (j == 4 && i == 0 && W4 >= 0) WAIT_VM(W4 >= 0 ? W4 : 0);
                MFA(acc[i][j], b[j], a[i]);
                if (j == 4) fa(a[i], i);
            }
            fb(b[j], j);
        }
    };
#define IC(n) std::integral_constant<int, n>{}
    auto nof = [&](bf16x8&, int) {};
    auto fha = [&](bf16x8& r, int i) { LDR(r, ap[i] + cur); };
    auto fhb = [&](bf16x8& r, int j) { LDR(r, bp + j * KS_H * 1024 + cur); };
    ST2(0); ST2ID();
    if (GPRIO) __builtin_amdgcn_s_setprio(GPRIO);
#pragma unroll 1
    for (int i = 0; i < GDELAY; ++i) __builtin_amdgcn_s_sleep(8);
    float gl;
    asm volatile("global_load_dword %0, %1, %2" : "=v"(gl) : "v"(vo >> 2), "s"(ws + WS_G + mt * 256) : "memory");
    ld(a0, b0);
    ld(a1, b1);
#pragma unroll
    for (int i = 0; i < 4; ++i) asm volatile("s_nop 1" : "+v"(acc[i][0]), "+v"(acc[i][1]), "+v"(acc[i][2]), "+v"(acc[i][3]), "+v"(acc[i][4]));
#pragma unroll 1
    for (int s = 0; s < nsteps - 4; s += 2) {
        mmx(IC(13), IC(13), a0, b0, fha, fhb); adv();
        mmx(IC(13), IC(13), a1, b1, fha, fhb); adv();
    }
    const char* fb_ = ws + WS_FLAG + mt * FLAG_LD + 64 * w;
    const int fv = (vo >> 2) & 60;
    mmx(IC(13), IC(13), a0, b0, fha, fhb); adv();
    unsigned fl;
    asm volatile("global_load_dword %0, %1, %2 sc1" : "=v"(fl) : "v"(fv), "s"(fb_) : "memory");
    mmx(IC(14), IC(14), a1, b1, fha, fhb);
    ST2(1);
    int op[4];
#pragma unroll
    for (int i = 0; i < 4; ++i) op[i] = (int)WS_O + ((mt * 4 + (i ^ w)) * KS_O + 5 * w) * 1024;
    const int wp = (int)WS_WO + ((nt * 5) * KS_O + 5 * w) * 1024;
    int so = 0;
    auto foa = [&](bf16x8& r, int i) { LDC(r, op[i] + so); };
    auto fob = [&](bf16x8& r, int j) { LDR(r, wp + j * KS_O * 1024 + so); };
    mmx(IC(14), IC(14), a0, b0, nof, fob);
    ST2(2);
    asm volatile("s_waitcnt vmcnt(14)\n\tv_cmp_ne_u32 vcc, 1, %0\n\ts_cbranch_vccz L_pok_%=\n"
                 "L_pspin_%=:\n\ts_sleep %3\n\tglobal_load_dword %0, %1, %2 sc1\n\ts_waitcnt vmcnt(0)\n\t"
                 "v_cmp_ne_u32 vcc, 1, %0\n\ts_cbranch_vccnz L_pspin_%=\n"
                 "L_pok_%=:" : "+v"(fl) : "v"(fv), "s"(fb_), "n"(POLLSLP) : "vcc", "memory");
    ST2(3);
#pragma unroll
    for (int i = 0; i < 4; ++i) foa(a0[i], i);
    so = 1024;
    mmx(IC(13), IC(13), a1, b1, foa, fob);
    asm volatile("s_nop 7\n\ts_nop 7" : "+v"(gl) :: "memory");
#pragma unroll
    for (int i = 0; i < 4; ++i) {
        const float sc = rbf(1.f - __builtin_bit_cast(float, __builtin_amdgcn_ds_bpermute((i ^ w) * 64 + fv, __builtin_bit_cast(int, gl))));
        asm volatile("" : "+v"(acc[i][0]), "+v"(acc[i][1]), "+v"(acc[i][2]), "+v"(acc[i][3]), "+v"(acc[i][4]));
#pragma unroll
        for (int j = 0; j < 5; ++j) acc[i][j] = acc[i][j] * sc;
        asm volatile("" : "+v"(acc[i][0]), "+v"(acc[i][1]), "+v"(acc[i][2]), "+v"(acc[i][3]), "+v"(acc[i][4]));
    }
    asm volatile("s_nop 3" ::: "memory");
    so = 2048; mmx(IC(12), IC(-1), a0, b0, foa, fob);
    so = 3072; mmx(IC(13), IC(13), a1, b1, foa, fob);
    so = 4096; mmx(IC(13), IC(13), a0, b0, foa, fob);
    mmx(IC(13), IC(9), a1, b1, nof, nof);
    mmx(IC(4), IC(0), a0, b0, nof, nof);
    asm volatile("s_nop 7\n\ts_nop 7" ::: "memory");
#pragma unroll
    for (int i = 0; i < 4; ++i) asm volatile("" : "+v"(acc[i][0]), "+v"(acc[i][1]), "+v"(acc[i][2]), "+v"(acc[i][3]), "+v"(acc[i][4]));
    ST2(4);
    const int ln = lane_id();
    char* mine = smem + w * 10240 + ln * 16;
    char* peer = smem + (w ^ 2) * 10240 + ln * 16;
#pragma unroll
    for (int i = 0; i < 2; ++i)
#pragma unroll
        for (int j = 0; j < 5; ++j) *(f32x4*)(mine + (i * 5 + j) * 1024) = acc[2 + i][j];
    __syncthreads();
#pragma unroll
    for (int j = 0; j < 5; ++j) acc[1][j] += *(const f32x4*)(peer + (5 + j) * 1024);
#pragma unroll
    for (int j = 0; j < 5; ++j) *(f32x4*)(peer + (5 + j) * 1024) = acc[1][j];
#pragma unroll
    for (int j = 0; j < 5; ++j) acc[0][j] += *(const f32x4*)(peer + j * 1024);
    __syncthreads();
    {
        const char* q = smem + (w ^ 3) * 10240 + ln * 16 + 5 * 1024;
        u16* dst = out + (size_t)(mt * 64 + w * 16 + (ln & 15)) * D_ + nt * 80 + (ln >> 4) * 4;
#pragma unroll
        for (int j = 0; j < 5; ++j) {
            const f32x4 v = acc[0][j] + *(const f32x4*)(q + j * 1024);
            u32x2 pk = {pack_bf16(v[0], v[1]), pack_bf16(v[2], v[3])};
            *(u32x2*)(dst + j * 16) = pk;
        }
    }
    ST2(5);
}

__global__ __launch_bounds__(256, 3) void k2_mix(char* __restrict__ ws, u16* __restrict__ out) {
    __shared__ __attribute__((aligned(16))) char smem[AT_LDS > 40960 ? AT_LDS : 40960];
    if (blockIdx.x < 256) k2_gemm(ws, out, smem, blockIdx.x);
    else k2_attn(ws, smem, blockIdx.x - 256);
}

extern "C" void kernel_launch(void* const* d_in, const int* in_sizes, int n_in,
                              void* d_out, int out_size, void* d_ws, size_t ws_size,
                              hipStream_t stream) {
    const u16* X = (const u16*)d_in[0];
    const u16* Wqkv = (const u16*)d_in[1];
    const u16* Wo = (const u16*)d_in[2];
    const u16* Wg = (const u16*)d_in[4];
    const u16* Wu = (const u16*)d_in[5];
    const u16* Wd = (const u16*)d_in[6];
    const u16* gw = (const u16*)d_in[7];
    char* ws = (char*)d_ws;
    k1_gemm<<<256, K1NW * 64, 0, stream>>>(X, Wqkv, Wg, Wu, Wo, Wd, gw, ws);
    k2_mix<<<768, 256, 0, stream>>>(ws, (u16*)d_out);
}
```
